```python
import torch

def _softmax(x, axis=-1):
    return torch.softmax(x, dim=axis)
def _gelu(x):
    return torch.nn.functional.gelu(x, approximate="tanh")
def _silu(x):
    return torch.nn.functional.silu(x)
def _sigmoid(x):
    return torch.sigmoid(x)
def _layernorm(x, axis=-1, eps=1e-5):
    m = x.mean(dim=axis, keepdim=True)
    v = x.var(dim=axis, keepdim=True, unbiased=False)
    return (x - m) / torch.sqrt(v + eps)
def _scatter_add(tgt, idx, src, axis=0):
    return tgt.clone().index_add_(axis, idx, src)
def _segment_sum(src, seg, n):
    out = torch.zeros((n,) + src.shape[1:], dtype=src.dtype)
    return out.index_add_(0, seg, src)
def _pad(x, pw):
    flat = tuple(v for pair in reversed(pw) for v in pair)
    return torch.nn.functional.pad(x, flat)

kernel_name = 'recurrent_scan__sliding_local_dilated__moe_arch_mod__prompt'

def setup_inputs():

    _w = (lambda a: (lambda t: t.to(torch.bfloat16) if t.is_floating_point() else t)(torch.as_tensor(a)))
    def _A_setup():
        B, S, D = 1, 2048, 640
        return {
            "x": _w((lambda *s: torch.randn(*s, dtype=torch.bfloat16) * 0.05)(B, S, D)),
            "wqkv": _w((lambda *s: torch.randn(*s, dtype=torch.bfloat16) * 0.05)(D, 3 * D)),
            "wo": _w((lambda *s: torch.randn(*s, dtype=torch.bfloat16) * 0.05)(D, D)),
        }
    _A_in = _A_setup()

    def _B_setup():
        B, S, D, F, C = 1, 2048, 640, 1728, 2
        return {
            "x": _w((lambda *s: torch.randn(*s, dtype=torch.bfloat16) * 0.05)(B, S, D)),
            "w_r": _w((lambda *s: torch.randn(*s, dtype=torch.bfloat16) * 0.05)(D)),
            "w_gate": _w((lambda *s: torch.randn(*s, dtype=torch.bfloat16) * 0.05)(D, F)),
            "w_up": _w((lambda *s: torch.randn(*s, dtype=torch.bfloat16) * 0.05)(D, F)),
            "w_down": _w((lambda *s: torch.randn(*s, dtype=torch.bfloat16) * 0.05)(F, D)),
        }
    _B_in = _B_setup()

    return {
        "A_x": _A_in["x"],
        "A_wqkv": _A_in["wqkv"],
        "A_wo": _A_in["wo"],
        "B_x": _B_in["x"],
        "B_w_r": _B_in["w_r"],
        "B_w_gate": _B_in["w_gate"],
        "B_w_up": _B_in["w_up"],
        "B_w_down": _B_in["w_down"],
    }

def reference(A_x, A_wqkv, A_wo, B_x, B_w_r, B_w_gate, B_w_up, B_w_down):
    def _A_ref(x, wqkv, wo):
        B, S, D = x.shape
        H = 32; Dh = D // H
        W, dil = 8, 2
        qkv = (lambda x, *d: x.permute(*d))((x @ wqkv).reshape(B, S, 3, H, Dh), 2, 0, 3, 1, 4)
        q, k, v = qkv[0], qkv[1], qkv[2]
        scale = Dh ** -0.5
        pos = torch.arange(S)
        off = torch.arange(W) * dil
        idx = pos[:, None] - off[None, :]
        mask = idx >= 0
        idx_c = torch.where(mask, idx, 0).reshape(-1)
        kg = k[:, :, idx_c, :].reshape(B, H, S, W, Dh)
        vg = v[:, :, idx_c, :].reshape(B, H, S, W, Dh)
        logits = torch.einsum("bhsd,bhswd->bhsw", q, kg) * scale
        logits = torch.where(mask[None, None, :, :], logits, -1e9)
        attn = _softmax(logits, axis=-1)
        o = torch.einsum("bhsw,bhswd->bhsd", attn, vg)
        return (lambda x, *d: x.permute(*d))(o, 0, 2, 1, 3).reshape(B, S, D) @ wo

    def _B_ref(x, w_r, w_gate, w_up, w_down):
        B, S, D = x.shape
        C = 2
        r = x @ w_r
        idx = torch.argsort(-r, axis=-1)[:, :C]
        idx_e = (lambda x, n, axis: torch.repeat_interleave(x, n, dim=axis))(idx.reshape(B, C, 1), D, axis=2)
        x_sel = (lambda x, i, axis: torch.take_along_dim(x, i, dim=axis))(x, idx_e, axis=1)
        g = _silu(x_sel @ w_gate)
        u = x_sel @ w_up
        y = (g * u) @ w_down
        r_sel = _sigmoid((lambda x, i, axis: torch.take_along_dim(x, i, dim=axis))(r, idx, axis=1))
        y = y * r_sel.reshape(B, C, 1)
        b_off = (torch.arange(B) * S).reshape(B, 1)
        flat_idx = (idx + b_off).reshape(B * C)
        zero = (x * 0.0).reshape(B * S, D)
        upd = _scatter_add(zero, flat_idx, y.reshape(B * C, D), axis=0)
        return x + upd.reshape(B, S, D)

    x = A_x
    xn = _layernorm(x, axis=-1)
    s = _A_ref(xn, A_wqkv, A_wo)
    g = _silu(_B_ref(xn, B_w_r, B_w_gate, B_w_up, B_w_down))
    return x + s * g
```

```cpp
#include <hip/hip_runtime.h>
#include <stdint.h>

typedef unsigned short u16;
typedef unsigned int u32;
typedef unsigned long long u64;
typedef __attribute__((ext_vector_type(4))) float f4;
typedef __attribute__((ext_vector_type(2))) float f2;
typedef __attribute__((ext_vector_type(4))) unsigned u4;
typedef __attribute__((ext_vector_type(2))) unsigned u2;
typedef __attribute__((ext_vector_type(8))) __bf16 bf8;
typedef __attribute__((ext_vector_type(2))) __bf16 bf2;
typedef __attribute__((ext_vector_type(4))) short s4;
#define LAS __attribute__((address_space(3)))
#define GAS __attribute__((address_space(1)))
#define DEV __device__ __forceinline__

constexpr int S_ = 2048, D_ = 640, F_ = 1728;

constexpr u32 WS_EPOCH = 0;
constexpr u32 WS_FIN = 256;
constexpr u32 WS_KEY = 24576;
constexpr u32 WS_YP = 8u << 20;
constexpr u32 WS_ST2 = 448u << 10;
constexpr u32 WS_FLAG = 512u << 10;
constexpr u32 WS_BOX = 640u << 10;
constexpr u32 WS_O = 1u << 20;
constexpr u32 WS_XS = 4u << 20;
constexpr u32 WS_END = WS_YP + 16 * 54 * 512;
constexpr u32 WS_STAMP = 16u << 20;

constexpr u32 SLOT = 38912;
constexpr u32 XB = 0, WB = 14336;
constexpr u32 MISC = 4 * SLOT;
constexpr u32 L_CS = MISC;
constexpr u32 L_SYNC = MISC + 768;
constexpr u32 L_MR = MISC + 1024;
constexpr u32 L_DUMP = MISC + 2048;
constexpr u32 L_Y = MISC + 3072;
constexpr u32 L_HV = MISC + 6144;
constexpr u32 LDS_BYTES = 163840;
constexpr u32 Y_Y = L_SYNC;
constexpr u32 Y_PUB = L_SYNC + 8;
constexpr u32 Y_WO = L_SYNC + 12;
constexpr u32 Y_CB = L_SYNC + 16;
constexpr u32 Y_S = L_SYNC + 128;
constexpr u32 Y_GO = L_SYNC + 48;
constexpr u32 Y_F = L_SYNC + 64;
constexpr u32 L_NONE = 0x30000;
constexpr u32 L_X = SLOT;
constexpr u32 L_HK = L_X + 21 * 512;
constexpr u32 L_WO = MISC - 103680;
constexpr u32 ST_N = 1040, ST_W = 5 * ST_N;
static_assert(L_HK + 2048 <= L_WO && 8 * ST_W <= L_WO, "lds map");

#define KD 44
constexpr int KEY_DELAY = KD;
constexpr int HELLO_DELAY = 4;
constexpr int POLL_SLEEP = 1;
#define WOA_ 14
#define WO7_ 5
constexpr int WO7 = WO7_, WOA = WOA_;
#define GSP 5
constexpr int GSPLIT = GSP;
#define OPRE_ 10
#define WPRE 6
#define RD0 5
#define RDX 6
#define STL 5
#define EBAR 1
#define WPR1 6
#define PGAP 1
#define LPRIO 2
#define IPRIO 2
#define BOXW 2
#define MPRIO 0
#define RDLY 10
#define KSLP 6
#define AWO 1
#define AWT 63
#define AW0 63
#define YG2 12
#define DLATE 0
#define GS2 14
#define NFD 40
#define PBR2 1
#define OSLP 0
#define ARR 0
constexpr int OPRE = OPRE_;
#define PADC4 "1"
#define NOTOUCH
#define KOFF 0x6420
#define TX0 10
#define INIT1 1
#define TH0 3
#define TH1 3
#define FFN_DELAY 12
#define WOC 12
#define YG 0
#define SSLP 1
#define FPERM 0x54362107
#define OUTP 0
#define OUTPOL ""
#define PBRK 1
#define WOST 0
#define WOSTA 0
#define WTH 63
#define WTH2 63
#define PACE 0
#define PACE0 1
#define MORD 0
#define REVM 0xc
#define KORD 12
constexpr u64 KT0 = 0x9786453120, KT1 = 0x8975642301, KT2 = KT0, KT3 = KT1, KT4 = 0x9867534210;
#define SCAP 15
constexpr u32 SPIN_CAP = 1u << SCAP;

struct Buf { char* p; u32 bytes, flags; };
struct Params { Buf x, wqkv, ws, wo, wr, out, wg, wu, wd; };

DEV float bflo(u32 v) { return __uint_as_float(v << 16); }
DEV float bfhi(u32 v) { return __uint_as_float(v & 0xffff0000u); }
DEV u32 pk2(float a, float b) {
  bf2 r; r[0] = (__bf16)a; r[1] = (__bf16)b;
  return __builtin_bit_cast(u32, r);
}
DEV float rbf(float a) { return bflo(pk2(a, a) & 0xffffu); }
DEV u4 srd(const Buf& b) { return __builtin_bit_cast(u4, b); }
template <int N> DEV void wait_vm() { asm volatile("s_waitcnt vmcnt(%0)" :: "i"(N) : "memory"); }
template <int N> DEV void wait_bar() { asm volatile("s_waitcnt vmcnt(%0) lgkmcnt(0)\n\ts_barrier" :: "i"(N) : "memory"); }
DEV void lds_bar() { asm volatile("s_waitcnt lgkmcnt(0)\n\ts_barrier" ::: "memory"); }

DEV bf8 ld_b128(u32 a) { return *(const LAS bf8*)(uintptr_t)a; }
DEV u2 ld_u2(u32 a) { return *(const LAS u2*)(uintptr_t)a; }
DEV f4 ld_f4(u32 a) { return *(const LAS f4*)(uintptr_t)a; }
DEV float ld_f(u32 a) { return *(const LAS float*)(uintptr_t)a; }
DEV u32 ld_u(u32 a) { return *(const LAS u32*)(uintptr_t)a; }
DEV void st_u4(u32 a, u4 v) { *(LAS u4*)(uintptr_t)a = v; }
DEV void st_u2(u32 a, u2 v) { *(LAS u2*)(uintptr_t)a = v; }
DEV void st_f4(u32 a, f4 v) { *(LAS f4*)(uintptr_t)a = v; }
DEV void st_f(u32 a, float v) { *(LAS float*)(uintptr_t)a = v; }
DEV void st_u(u32 a, u32 v) { *(LAS u32*)(uintptr_t)a = v; }
DEV u2 ld_tr(u32 a) {
  return __builtin_bit_cast(u2, __builtin_amdgcn_ds_read_tr16_b64_v4i16((LAS s4*)(uintptr_t)a));
}
DEV bf8 mk8(u2 a, u2 b) { u4 r; r.x = a.x; r.y = a.y; r.z = b.x; r.w = b.y; return __builtin_bit_cast(bf8, r); }
DEV bf8 ld_tr8(u32 a, u32 d) { return mk8(ld_tr(a), ld_tr(a + d)); }
DEV f4 mfma(bf8 a, bf8 b, f4 c) { return __builtin_amdgcn_mfma_f32_16x16x32_bf16(a, b, c, 0, 0, 0); }
DEV f4 mfma16(u2 a, u2 b, f4 c) { return __builtin_amdgcn_mfma_f32_16x16x16bf16_1k(__builtin_bit_cast(s4, a), __builtin_bit_cast(s4, b), c, 0, 0, 0); }

#define PIN(x) asm volatile("" : "+v"(x))
#define SB() __builtin_amdgcn_sched_barrier(0)
#define RLX __ATOMIC_RELAXED, __HIP_MEMORY_SCOPE_AGENT
DEV u32 gld32(const char* p) { return __hip_atomic_load((const GAS u32*)p, RLX); }
DEV void gst64(char* p, u64 v) { __hip_atomic_store((GAS u64*)p, v, RLX); }
DEV void gst32(char* p, u32 v) { __hip_atomic_store((GAS u32*)p, v, RLX); }
typedef __amdgpu_buffer_rsrc_t rsrc_t;
DEV u4 bld128_sc1(rsrc_t r, u32 off) { return __builtin_amdgcn_raw_buffer_load_b128(r, off, 0, 16); }
DEV void bst128_sc1(rsrc_t r, u32 off, u4 v) { __builtin_amdgcn_raw_buffer_store_b128(v, r, off, 0, 16); }

#define STAMP(i) do {} while (0)
#define CSTAMP(i) do {} while (0)
#define FST(i) do {} while (0)
#define GST(i) do {} while (0)
#define KST(i) do {} while (0)
#define PST(i) do {} while (0)
#define STM_ARG
#define STM

template <int CTRL> DEV float dppf(float v) {
  return __uint_as_float(__builtin_amdgcn_update_dpp(0u, __float_as_uint(v), CTRL, 0xf, 0xf, true));
}
template <int CTRL> DEV u32 dppu(u32 v) { return __builtin_amdgcn_update_dpp(0u, v, CTRL, 0xf, 0xf, true); }
DEV float row_sum16(float v) {
  v += dppf<0xB1>(v);
  v += dppf<0x4E>(v);
  v += dppf<0x141>(v);
  v += dppf<0x140>(v);
  return v;
}
DEV void top2_merge(u32& a1, u32& a2, u32 b1, u32 b2) {
  u32 lo = min(a1, b1);
  a1 = max(a1, b1);
  a2 = max(lo, max(a2, b2));
}
DEV void top2_wave(u32& k1, u32& k2) {
  top2_merge(k1, k2, dppu<0xB1>(k1), dppu<0xB1>(k2));
  top2_merge(k1, k2, dppu<0x4E>(k1), dppu<0x4E>(k2));
  top2_merge(k1, k2, dppu<0x124>(k1), dppu<0x124>(k2));
  top2_merge(k1, k2, dppu<0x128>(k1), dppu<0x128>(k2));
  {
    auto r1 = __builtin_amdgcn_permlane16_swap(k1, k1, false, false);
    auto r2 = __builtin_amdgcn_permlane16_swap(k2, k2, false, false);
    k1 = r1[0]; k2 = r2[0]; top2_merge(k1, k2, r1[1], r2[1]);
  }
  {
    auto r1 = __builtin_amdgcn_permlane32_swap(k1, k1, false, false);
    auto r2 = __builtin_amdgcn_permlane32_swap(k2, k2, false, false);
    k1 = r1[0]; k2 = r2[0]; top2_merge(k1, k2, r1[1], r2[1]);
  }
}
DEV u32 mk_tag(u32 ep) {
  u32 tag = ep * 0x9E3779B1u + 0x7F4A7C15u;
  tag ^= tag >> 15; tag *= 0x85EBCA77u; tag ^= tag >> 13;
  return tag + (tag == ep);
}
DEV u32 epoch_get(const Params& p) { u32 e; asm volatile("s_load_dword %0, %1, 0" : "=s"(e) : "s"(p.ws.p)); return e; }
DEV u32 epoch_tag(u32 e) { asm volatile("s_waitcnt lgkmcnt(0)" : "+s"(e)); return mk_tag(e); }

DEV float silu_f(float v) { return v * __builtin_amdgcn_rcpf(1.f + __expf(-v)); }

DEV u32 key_tok(u32 k) { return ~k & 2047u; }
DEV float key_r(u32 k) {
  u32 o = k >> 16;
  u32 b = (o & 0x8000u) ? (o & 0x7fffu) : (~o & 0xffffu);
  return bflo(b);
}
DEV float vmax(float a, float b) { return __builtin_elementwise_maximum(a, b); }
DEV void bar() { asm volatile("s_barrier" ::: "memory"); }
#define NEW_PAGE() asm volatile("s_branch 1f\n\t.p2align 12\n1:" ::: "memory")
#define FENCE() asm volatile("" ::: "memory")
DEV u32 lds_peek(u32 a) { return __hip_atomic_load((LAS u32*)(uintptr_t)a, __ATOMIC_RELAXED, __HIP_MEMORY_SCOPE_WORKGROUP); }
DEV void lds_add(u32 a, u32 v) {
  asm volatile("s_mov_b64 exec, 1\n\tds_add_u32 %0, %1\n\ts_mov_b64 exec, -1" :: "v"(a), "v"(v) : "memory");
}
DEV void lds_set(u32 a, u32 v) {
  asm volatile("s_mov_b64 exec, 1\n\tds_write_b32 %0, %1\n\ts_mov_b64 exec, -1" :: "v"(a), "v"(v) : "memory");
}
template <int SLP> DEV void lds_wait_ge(u32 a, u32 v) {
  u32 n = 1u << 20, t, l, r;
  asm volatile(".if %6\n\ts_branch 2f\n\t.endif\n1:\n\t.if %6\n\ts_sleep %6\n\t.endif\n2:\n\t"
               "ds_read_b32 %1, %4\n\ts_sub_u32 %0, %0, 1\n\ts_cselect_b32 %2, 0, %5\n\ts_waitcnt lgkmcnt(0)\n\t"
               "v_readfirstlane_b32 %3, %1\n\ts_cmp_lt_u32 %3, %2\n\ts_cbranch_scc1 1b"
               : "+s"(n), "=&v"(t), "=&s"(l), "=&s"(r) : "v"(a), "s"(v), "i"(SLP) : "scc", "memory");
}
DEV u32 norm2(u32 x, float m, float rs) {
  const u32 d = pk2(bflo(x) - m, bfhi(x) - m);
  return pk2(bflo(d) * rs, bfhi(d) * rs);
}

DEV float dot2(u32 a, u32 b, float c) { return __builtin_amdgcn_fdot2_f32_bf16(__builtin_bit_cast(bf2, a), __builtin_bit_cast(bf2, b), c, false); }
DEV float fsel(u32 m, float a, float b) {
  return __uint_as_float(__builtin_amdgcn_bitop3_b32(m, __float_as_uint(a), __float_as_uint(b), 0xCA));
}
struct RowIn {
  u4 x[2][5], w[5];
  DEV void take() {
    asm volatile("" : "={v[100:103]}"(w[0]), "={v[104:107]}"(x[0][0]), "={v[108:111]}"(x[1][0]), "={v[112:115]}"(w[1]), "={v[116:119]}"(x[0][1]), "={v[120:123]}"(x[1][1]),
                 "={v[124:127]}"(w[2]), "={v[128:131]}"(x[0][2]), "={v[132:135]}"(x[1][2]), "={v[136:139]}"(w[3]), "={v[140:143]}"(x[0][3]), "={v[144:147]}"(x[1][3]),
                 "={v[148:151]}"(w[4]), "={v[152:155]}"(x[0][4]), "={v[156:159]}"(x[1][4]));
  }
  DEV void wait(int i) {
    if (i == 0) asm volatile("s_waitcnt vmcnt(12)" : "+{v[100:103]}"(w[0]), "+{v[104:107]}"(x[0][0]), "+{v[108:111]}"(x[1][0]));
    if (i == 1) asm volatile("s_waitcnt vmcnt(9)" : "+{v[112:115]}"(w[1]), "+{v[116:119]}"(x[0][1]), "+{v[120:123]}"(x[1][1]));
    if (i == 2) asm volatile("s_waitcnt vmcnt(6)" : "+{v[124:127]}"(w[2]), "+{v[128:131]}"(x[0][2]), "+{v[132:135]}"(x[1][2]));
    if (i == 3) asm volatile("s_waitcnt vmcnt(3)" : "+{v[136:139]}"(w[3]), "+{v[140:143]}"(x[0][3]), "+{v[144:147]}"(x[1][3]));
    if (i == 4) asm volatile("s_waitcnt vmcnt(0)" : "+{v[148:151]}"(w[4]), "+{v[152:155]}"(x[0][4]), "+{v[156:159]}"(x[1][4]));
  }
  DEV void stats(const Params& p, rsrc_t rws, u32& tag, int lane, int s8, u32 slot STM_ARG) {
    const u32 ep = epoch_get(p);
    const u32 dq = ((lane >> 2) ^ (lane >> 4)) & 3;
    const int sidx = s8 + (lane >> 4) + dq * 2, tok = 2 * (sidx & 1023) + (sidx >> 10);
    u32 so = ((dq & 1) | (lane & 3)) ? 0x80000000u : WS_ST2 + sidx * 16, ko = lane ? L_NONE + lane * 16 : slot;
    u32 m1 = __builtin_amdgcn_sbfe(lane, 0, 1), m2 = __builtin_amdgcn_sbfe(lane, 1, 1), mz = __builtin_amdgcn_sbfe(dq, 1, 1), mv = ~__builtin_amdgcn_sbfe(dq, 0, 1);
    u4 on; on.x = on.y = on.z = on.w = 0x3f803f80u;
    PIN(so); PIN(ko); PIN(m1); PIN(m2); PIN(mz); PIN(mv); PIN(on);
    const bf8 ones = __builtin_bit_cast(bf8, on);
    const f4 z4 = {0.f, 0.f, 0.f, 0.f};
    f4 ax[2] = {z4, z4}, aq[2] = {z4, z4}, aw[2] = {z4, z4}, a1 = z4;
    SB();
#pragma unroll
    for (int i = 0; i < 5; ++i) {
      wait(i);
      const bf8 wv = __builtin_bit_cast(bf8, w[i]);
#pragma unroll
      for (int z = 0; z < 2; ++z) {
        const bf8 v = __builtin_bit_cast(bf8, x[z][i]);
        aq[z] = mfma(v, v, aq[z]); ax[z] = mfma(v, ones, ax[z]); aw[z] = mfma(v, wv, aw[z]);
      }
      a1 = mfma(wv, ones, a1);
      SB();
      PST(6 + i);
    }
    tag = epoch_tag(ep);
    PST(11);
    const float sw = (a1[0] + a1[1]) + (a1[2] + a1[3]);
    auto quad = [](float v) { v += dppf<0xB1>(v); v += dppf<0x4E>(v); return v; };
    auto diag = [&](f4 d) { return fsel(m2, fsel(m1, d[3], d[2]), fsel(m1, d[1], d[0])); };
    auto both = [&](float a, float b) { return fsel(mz, dppf<0x128>(b), a); };
    const float mean = fsel(mz, (ax[1][0] + ax[1][1]) + (ax[1][2] + ax[1][3]), (ax[0][0] + ax[0][1]) + (ax[0][2] + ax[0][3])) * (1.f / 640.f);
    const float sxw = quad(both(diag(aw[0]), diag(aw[1])));
    float var = fmaf(-mean, mean, quad(both(diag(aq[0]), diag(aq[1]))) * (1.f / 640.f));
    if (__builtin_expect(__any(mv && !(var * 64.f >= mean * mean)), 0)) {
      float q[2];
#pragma unroll
      for (int z = 0; z < 2; ++z) {
        f4 a = z4;
#pragma unroll
        for (int i = 0; i < 5; ++i) a = mfma(ones, __builtin_bit_cast(bf8, x[z][i]), a);
        const float mu = quad(a[0]) * (1.f / 640.f);
        float t = 0.f;
#pragma unroll
        for (int i = 0; i < 5; ++i)
#pragma unroll
          for (int e = 0; e < 4; ++e) { const float c = bflo(x[z][i][e]) - mu, d = bfhi(x[z][i][e]) - mu; t = fmaf(c, c, fmaf(d, d, t)); }
        t = quad(t);
        t += __shfl_xor(t, 16); t += __shfl_xor(t, 32);
        q[z] = t;
      }
      var = both(q[0], q[1]) * (1.f / 640.f);
    }
    const float mb = rbf(mean);
    const float sd = rbf(__builtin_amdgcn_sqrtf(rbf(rbf(var) + 1e-5f)));
    const float rs = __builtin_amdgcn_rcpf(sd);
    u4 st; st.x = __float_as_uint(mb); st.z = __float_as_uint(rs); st.y = st.w = tag;
    PST(12);
    bst128_sc1(rws, so, st);
    PST(13);
    const float rr = fmaf(-mb, sw, sxw) * rs;
    const u32 b = pk2(rr, rr) & 0xffffu;
    const u32 o = (b & 0x8000u) ? (~b & 0xffffu) : (b | 0x8000u);
    u32 k1 = ((o << 16) | (0xffffu - tok)) & mv, k2 = 0;
    top2_merge(k1, k2, dppu<0x128>(k1), 0);
    k1 = max(k1, dppu<0x124>(k1)); k2 = max(k2, dppu<0x124>(k2));
    {
      auto r1 = __builtin_amdgcn_permlane16_swap(k1, k1, false, false);
      auto r2 = __builtin_amdgcn_permlane16_swap(k2, k2, false, false);
      k1 = r1[0]; k2 = r2[0]; top2_merge(k1, k2, r1[1], r2[1]);
    }
    {
      auto r1 = __builtin_amdgcn_permlane32_swap(k1, k1, false, false);
      auto r2 = __builtin_amdgcn_permlane32_swap(k2, k2, false, false);
      k1 = r1[0]; k2 = r2[0]; top2_merge(k1, k2, r1[1], r2[1]);
    }
    u4 kk; kk.x = k1; kk.z = k2; kk.y = kk.w = tag;
    PST(14);
    st_u4(ko, kk);
  }
};

template <int N, int DM, int DS, int E60 = 0, int TH = 63> DEV void dma_run(u32 m0v, u32 voff, u4 srd, u32 so) {
  asm volatile("s_mov_b32 m0, %2\n\t"
               "s_mov_b32 exec_hi, %7\n\t"
               ".rept %4\n\t"
               "buffer_load_dwordx4 %1, %3, %0 offen lds\n\t"
               "s_add_u32 m0, m0, %5\n\t"
               "s_add_u32 %0, %0, %6\n\t"
               ".if %8 < 63\n\ts_waitcnt vmcnt(%8)\n\t.endif\n\t"
               ".endr\n\t"
               "s_mov_b32 exec_hi, -1"
               : "+s"(so) : "v"(voff), "s"(m0v), "s"(srd), "i"(N), "i"(DM), "i"(DS), "i"(E60 ? 0x0fffffff : -1), "i"(TH) : "scc");
}

struct WoDma {
  u32 vo, vol, m, so, m0;
  DEV void init(int lane, int lw, int ng, int start) {
    const int l60 = min(lane, 59);
    vo = (lw * 6 + l60 / 10) * 1280 + (l60 % 10) * 16 + ng * 160;
    vol = min((104 + lw) * 6 + l60 / 10, 639) * 1280 + (l60 % 10) * 16 + ng * 160;
    m0 = L_WO + lw * 960; m = m0 + start * 3840; so = start * (24 * 1280);
    PIN(vo); PIN(vol);
    asm volatile("" : "+s"(m), "+s"(so), "+s"(m0));
  }
  template <int I0, int I1, int TH = 63, int SKIP = 0> DEV void issue(u4 srd, int lw, u32 f = 0) {
    constexpr int E = I1 > 26 ? 26 : I1;
    if (E > I0)
      asm volatile(".if %7\n\ts_cmp_gt_u32 %8, 53\n\ts_cbranch_scc1 1f\n\t.endif\n\t"
                   "s_mov_b32 m0, %0\n\ts_mov_b32 exec_hi, 0x0fffffff\n\t"
                   ".rept %5\n\tbuffer_load_dwordx4 %2, %3, %1 offen lds\n\ts_addk_i32 m0, 3840\n\ts_addk_i32 %1, 30720\n\t"
                   ".if %6 < 63\n\ts_waitcnt vmcnt(%6)\n\t.endif\n\t.endr\n\t"
                   "s_mov_b32 %0, m0\n\ts_mov_b32 exec_hi, -1\n1:"
                   : "+s"(m), "+s"(so) : "v"(vo), "s"(srd), "s"(m0), "i"(E > I0 ? E - I0 : 1), "i"(TH), "i"(SKIP), "s"(f) : "scc");
    if (I1 > 26) dma_run<1, 0, 0, 1>(L_WO + lw * 960 + 26 * 3840, vol, srd, 0);
  }
};

struct Epi {
  u4 xv[2], sv, fin; u2 xh; u32 goff[2], soff;
  float gf[20], c[4];
  static DEV u32 row(u32 lane) { return ((lane >> 1) & 12) | ((lane >> 2) & 1) | ((lane >> 4) & 2); }
  DEV void init(int lane, int m, int mg, int ng) {
    const int mt = mg * 4 + m, parity = mt >> 6;
    const u32 u = (mt & 63) * 16 + row(lane), o = (2 * u + parity) * 1280 + ng * 160;
    goff[0] = o + (ng & 1) * 32 + (lane & 3) * 16; goff[1] = o + ((ng & 1) ? 0 : 128) + (lane & 3) * 8;
    soff = WS_ST2 + (parity * 1024 + u) * 16;
    PIN(goff[0]); PIN(goff[1]); PIN(soff);
  }
  DEV void issue(u4 srdx, u4 srdws, u32 fo = WS_FIN) {
    asm volatile("buffer_load_dwordx4 %0, %1, %2, 0 offen sc1" : "=v"(sv) : "v"(soff), "s"(srdws));
    asm volatile("buffer_load_dwordx4 %0, %1, %2, 0 offen" : "=v"(xv[0]) : "v"(goff[0]), "s"(srdx));
    asm volatile("buffer_load_dwordx4 %0, %1, %2, 0 offen offset:64" : "=v"(xv[1]) : "v"(goff[0]), "s"(srdx));
    asm volatile("buffer_load_dwordx2 %0, %1, %2, 0 offen" : "=v"(xh) : "v"(goff[1]), "s"(srdx));
    asm volatile("buffer_load_dwordx4 %0, off, %1, %2 sc1" : "=v"(fin) : "s"(srdws), "s"(fo));
  }
  template <int N> DEV void wait() {
    asm volatile("s_waitcnt vmcnt(%5)" : "+v"(xv[0]), "+v"(xv[1]), "+v"(xh), "+v"(sv), "+v"(fin) : "i"(N) : "memory");
  }
  DEV void check(char* ws, u32 tag) {
    if (__builtin_expect(__any(__builtin_amdgcn_bitop3_b32(sv.y, sv.w, tag, 0x7e) != 0u), 0)) {
      const rsrc_t rws = __builtin_amdgcn_make_buffer_rsrc(ws, 0, WS_END, 0x00020000);
#pragma nounroll
      for (u32 spin = 0; !__all(sv.y == tag && sv.w == tag) && spin < SPIN_CAP; ++spin) {
        FENCE();
        sv = bld128_sc1(rws, soff);
      }
      __builtin_amdgcn_s_waitcnt(0x0f70);
    }
  }
  DEV u32 xw(int i) const { return i < 8 ? xv[i >> 2][i & 3] : xh[i & 1]; }
  DEV void gate(int i) {
    if (i == 0) {
      c[0] = __uint_as_float(sv.z); c[1] = -__uint_as_float(sv.x) * c[0];
      c[2] = c[0] * -1.4426950408889634f; c[3] = c[1] * -1.4426950408889634f;
    }
    float x = i & 1 ? bfhi(xw(i >> 1)) : bflo(xw(i >> 1));
    PIN(x);
    float z = fmaf(x, c[0], c[1]), t = fmaf(x, c[2], c[3]);
    PIN(z); PIN(t);
    gf[i] = z * __builtin_amdgcn_rcpf(1.f + __builtin_amdgcn_exp2f(t));
    PIN(gf[i]);
  }
};
#define SPIN(x) asm volatile("" : "+s"(x))
#define HALF_A(SB, VX, VW, SX, SW, SA, SC, EA, EC) \
    "s_add_u32 m0, " SB ", 12288\n\ts_mov_b64 exec, " EC "\n\ts_cbranch_execz 7f\n\tbuffer_load_dwordx4 " VX ", " SX ", " SC " offen lds\n7:\n\t" \
    "s_mov_b32 m0, " SB "\n\ts_mov_b64 exec, " EA "\n\tbuffer_load_dwordx4 " VX ", " SX ", " SA " offen lds\n\t" \
    "s_addk_i32 m0, 14336\n\ts_mov_b64 exec, -1\n\tbuffer_load_dwordx4 " VW ", " SW ", 0 offen lds\n\t" \
    "s_addk_i32 m0, 6912\n\ts_nop 0\n\tbuffer_load_dwordx4 " VW ", " SW ", 0 offen offset:1280 lds\n\t" \
    "s_addk_i32 m0, 6912\n\ts_nop 0\n\tbuffer_load_dwordx4 " VW ", " SW ", 0 offen offset:2560 lds\n\t"
#define HALF_B(SB, VX, VW, SX, SW, SB1, SB2, SH) \
    "s_add_u32 m0, " SB ", 4096\n\ts_nop 0\n\tbuffer_load_dwordx4 " VX ", " SX ", " SB1 " offen lds\n\t" \
    "s_addk_i32 m0, 4096\n\ts_nop 0\n\tbuffer_load_dwordx4 " VX ", " SX ", " SB2 " offen lds\n\t" \
    "s_addk_i32 m0, 10240\n\ts_nop 0\n\tbuffer_load_dwordx4 " VW ", " SW ", " SH " offen lds\n\t" \
    "s_addk_i32 m0, 6912\n\ts_nop 0\n\tbuffer_load_dwordx4 " VW ", " SW ", " SH " offen offset:1280 lds\n\t" \
    "s_addk_i32 m0, 6912\n\ts_nop 0\n\tbuffer_load_dwordx4 " VW ", " SW ", " SH " offen offset:2560 lds\n\t"
struct LdIn {
  u4 srdx, srdw; u32 vx, vw, lw, j, tp, cls, kb, sa, sb1, sb2, sc, sb, sh; u64 ec, ea;
  DEV void take() {
    asm volatile("" : "={s[4:7]}"(srdx), "={s[8:11]}"(srdw), "={v1}"(vx), "={v2}"(vw), "={s12}"(lw), "={s13}"(j), "={s14}"(tp), "={s15}"(cls), "={s16}"(kb),
                 "={s18}"(sa), "={s19}"(sb1), "={s20}"(sb2), "={s21}"(sc), "={s[22:23]}"(ec), "={s[24:25]}"(ea), "={s26}"(sb), "={s27}"(sh));
  }
};
DEV void main_loader(Params& p, LdIn& in, int lane, int parity, int mg, int ng, u32 koff, Epi& ei STM_ARG) {
  const int lw = in.lw, tp = in.tp, j = in.j, rt = parity * 10 + tp, u0 = tp * 104 - 8;
  const u4 srdx = in.srdx;
  u64 ktab = KT0;
  ktab = in.cls == 1 ? KT1 : ktab; SPIN(ktab); ktab = in.cls == 2 ? KT2 : ktab; SPIN(ktab); ktab = in.cls == 3 ? KT3 : ktab; SPIN(ktab); ktab = in.cls == 4 ? KT4 : ktab;
  auto next = [&]() {
    ktab >>= 4;
    u32 kb = ((u32)ktab & 15u) + koff;
    kb = min(kb, kb - 10u);
    const int d = kb - in.kb;
    in.kb = kb;
    in.vx += d * 128; in.vw += d * (64 * 3840);
    in.sb = in.sb >= 3 * SLOT ? in.sb - 3 * SLOT : in.sb + SLOT;
    PIN(in.vx); PIN(in.vw); SPIN(in.sb);
  };
  auto issue = [&]() {
    asm volatile("s_bitcmp1_b32 %12, 1\n\ts_cselect_b64 vcc, -1, 0\n\ts_cbranch_vccnz 5f\n4:\n\t"
                 HALF_A("%0", "%1", "%2", "%3", "%4", "%5", "%6", "%7", "%8") "s_cbranch_vccnz 6f\n5:\n\t"
                 HALF_B("%0", "%1", "%2", "%3", "%4", "%9", "%10", "%11") "s_cbranch_vccnz 4b\n6:"
                 :: "s"(in.sb), "v"(in.vx), "v"(in.vw), "s"(in.srdx), "s"(in.srdw), "s"(in.sa), "s"(in.sc), "s"(in.ea), "s"(in.ec),
                    "s"(in.sb1), "s"(in.sb2), "s"(in.sh), "s"(in.cls) : "scc", "vcc");
    next();
  };
#define STAGE_BAR(E) asm volatile("s_cmp_lt_u32 %0, 2\n\ts_cbranch_scc1 1f\n\ts_waitcnt vmcnt(%1)\n1:\n\ts_waitcnt vmcnt(%2)\n\ts_barrier" :: "s"(lw), "i"(9 + E), "i"(10 + E) : "scc", "memory")
  next();
  u32 ep = epoch_get(p);
  const u4 srdws = srd(p.ws);
  const int srow = lw * 28 + min(lane, 27);
  u32 go1 = WS_ST2 + (parity * 1024 + min(max(u0 + srow, 0), 1023)) * 16;
  u32 wa = lw < 2 ? WS_XS + ((rt * 10 + j) * 14 + (lw ? 14 : -7)) * 512 + min(lane, 27) * 128 : (BOXW == 0 || (BOXW == 2 && lane) ? 0x80000000u : WS_BOX + (mg * 8 + ng) * 256 + (lw & 1) * 64);
  u32 ho = (lane | (lw ^ 3)) ? 0x80000000u : WS_FLAG + (rt * 10 + j) * 256 + 128;
  PIN(go1); PIN(wa); PIN(ho);
  WoDma wo; wo.init(lane, lw, ng, (WOST >> (8 * (mg & 3))) & 255);
  ei.init(lane, lw, mg, ng);
  SB();
  PST(4);
  issue();
  u32 tagv = epoch_tag(ep);
  PIN(tagv);
  STAMP(9);
  PST(5);
  STAGE_BAR(0);
  PST(7);
  STAMP(2);
  asm volatile("buffer_store_dword %0, %1, %2, 0 offen sc1" :: "v"(tagv), "v"(ho), "s"(srdws));
#pragma unroll 1
  for (int s = 2; s < 9; ++s) {
    issue();
    STAGE_BAR(0);
  }
  u4 g1;
  asm volatile("buffer_load_dwordx4 %0, %1, %2, 0 offen sc1" : "=v"(g1) : "v"(go1), "s"(srdws));
  asm volatile("s_mov_b32 m0, %0\n\ts_mov_b64 exec, 0x0fffffff\n\tbuffer_load_dword %1, %2, 0 offen sc1 lds\n\t"
               "s_add_u32 m0, m0, 504\n\ts_nop 0\n\tbuffer_load_dword %1, %2, 0 offen offset:8 sc1 lds\n\ts_mov_b64 exec, -1"
               :: "s"(L_MR + lw * 112), "v"(go1), "s"(srdws) : "scc");
  issue();
  STAGE_BAR(3);
  STAMP(1);
  asm volatile("s_waitcnt vmcnt(11)" : "+v"(g1) :: "memory");
  if (__builtin_expect(__any(__builtin_amdgcn_bitop3_b32(g1.y, g1.w, tagv, 0x7e) != 0u), 0)) {
    const rsrc_t rws = __builtin_amdgcn_make_buffer_rsrc(p.ws.p, 0, WS_END, 0x00020000);
#pragma nounroll
    for (u32 spin = 0; spin < SPIN_CAP; ++spin) {
      FENCE();
      g1 = bld128_sc1(rws, go1);
      if (__all(g1.y == tagv && g1.w == tagv)) break;
      __builtin_amdgcn_s_sleep(2);
    }
    __builtin_amdgcn_s_waitcnt(0x0f70);
    st_u(L_MR + srow * 4, g1.x); st_u(L_MR + 512 + srow * 4, g1.z);
  }
  const u4 srdo = srd(p.wo);
  if (BOXW == 2)
    asm volatile("s_mov_b32 m0, %0\n\ts_cmp_lt_u32 %3, 2\n\ts_cbranch_scc1 1f\n\tbuffer_atomic_add %4, %1, %2, 0 offen\n\ts_branch 2f\n"
                 "1:\n\tbuffer_load_dword %1, %2, 0 offen sc1 lds\n2:" :: "s"(L_DUMP + lw * 256), "v"(wa), "s"(srdws), "s"(lw), "v"(0u) : "scc");
  else
  asm volatile("s_mov_b32 m0, %0\n\ts_nop 0\n\tbuffer_load_dword %1, %2, 0 offen sc1 lds" :: "s"(L_DUMP + lw * 256), "v"(wa), "s"(srdws));
  wo.issue<0, WO7>(srdo, lw);
  __builtin_amdgcn_s_setprio(0);
  wait_bar<WO7 + 1>();
  STAMP(3);
  wo.issue<WO7, WOA, WTH>(srdo, lw);
  SB();
  ei.issue(srdx, srdws);
  wo.issue<WOA, 27, WTH2>(srdo, lw);
  ei.wait<27 - WOA>();
  ei.check(p.ws.p, tagv);
  STAMP(4);
}

DEV int fresh_lane() {
  int l;
  asm volatile("v_mbcnt_lo_u32_b32 %0, -1, 0\n\tv_mbcnt_hi_u32_b32 %0, -1, %0" : "=v"(l));
  __builtin_assume(l >= 0 && l < 64);
  return l;
}
struct G2 {
  u32 b0, oo, fo, ga, one, pa, pm;
  DEV void init(int wave, int mg, int ng) {
    const int lane = fresh_lane(), g = lane >> 4, i16 = lane & 15, m = wave & 3, kh = wave >> 2;
    b0 = L_WO + (kh * 320 + 16 * (g >> 1) + 4 * (g & 1) + (i16 >> 2)) * 160 + (lane & 3) * 8;
    oo = WS_O + (mg * 4 + m) * 20480 + kh * 10240 + lane * 16;
    fo = WS_BOX + (mg * 8 + ng) * 256 + kh * 64 + lane * 4; ga = Y_GO + kh * 4 + lane * 4 + min(lane, 1) * 0x10000; one = 1; pa = Y_PUB;
    pm = ((0x12u >> wave) & 1) ? (((0x4b5au >> (mg & 15)) & 1) ? 0x3ffu : 0x1fu) : 0u;
    PIN(b0); PIN(oo); PIN(fo); PIN(ga); PIN(one); PIN(pa); SPIN(pm);
  }
};

constexpr u64 sel_mask(int r) {
  u64 m = 0;
  for (int l = 0; l < 64; ++l) {
    const int i = l & 15, c = 4 * ((l >> 4) & 1) + r;
    if (i < 8 ? c <= i : c > i - 8) m |= 1ull << l;
  }
  return m;
}
constexpr u64 SEL0 = sel_mask(0), SEL1 = sel_mask(1), SEL2 = sel_mask(2), SEL3 = sel_mask(3);
#define LM(m) __builtin_amdgcn_inverse_ballot_w64(m)
DEV u2 and2(u2 a, u2 m) { u2 r; r.x = a.x & m.x; r.y = a.y & m.y; return r; }

struct Att {
  u32 aA0[2], aA1[2], aB[2];
  u32 mOwn, mRot, mV, mLo, so[2], xg, aXw, aXr, aCS, aCV, aST, aMR, aH, aF;
  u4 mS;
  DEV void init_k(u32 ln, u32 n) {
    const u32 a0 = XB + ((ln & 15) << 7) + ((((ln >> 1) & 7) ^ (ln >> 4)) << 4);
    const u32 b0 = WB + ((ln & 48) << 6) + ((ln & 12) << 5) + (((2 * n + ((ln >> 1) & 1)) ^ ((ln >> 2) & 6)) << 4) + ((ln & 1) << 3);
#pragma unroll
    for (int s = 0; s < 2; ++s) {
      aA0[s] = a0 + s * 2 * SLOT; aA1[s] = (a0 ^ 64) + s * 2 * SLOT; aB[s] = b0 + s * 2 * SLOT;
      PIN(aA0[s]); PIN(aA1[s]); PIN(aB[s]);
    }
  }
  template <int NS> DEV void init(u32 ln, int n, int tau, bool edge, int tp, int parity, int j) {
    constexpr int s0 = NS == 4 ? 0 : 4;
    constexpr u32 SGN = 0x80000000u;
    const u32 i16 = ln & 15, g16 = ln & 48, h32 = ln >> 5, i4 = i16 * 4;
    mLo = ~(u32)__builtin_amdgcn_sbfe((int)ln, 3, 1);
    const u32 gt = (u32)(((int)g16 - 16 * tau) >> 31);
    mOwn = mLo & gt; mRot = ~mLo & ~gt;
    mV = (u32)(((int)i16 - 4 * tau) >> 31);
    u32 l0 = (u32)SEL0, l1 = (u32)SEL1, l2 = (u32)SEL2, l3 = (u32)SEL3;
    SPIN(l0); SPIN(l1); SPIN(l2); SPIN(l3);
    mS.x = __builtin_amdgcn_sbfe(l0, ln, 1); mS.y = __builtin_amdgcn_sbfe(l1, ln, 1); mS.z = __builtin_amdgcn_sbfe(l2, ln, 1); mS.w = __builtin_amdgcn_sbfe(l3, ln, 1);
    PIN(mOwn); PIN(mRot); PIN(mV); PIN(mLo); PIN(mS);
    const u32 up = tp * 104 + 8 + s0 * 16 + h32 * 16 + i16;
    const u32 hs = ln << 26;
    so[0] = WS_O + (parity * 64 - 1) * 20480 + (2 * j + (n >> 1)) * 1024 + (n & 1) * 512 + (up >> 4) * 20480 + (((ln & 16) | (up & 15)) << 4);
    so[1] = so[0] + 2 * 20480;
    if (NS == 4) so[0] |= ~hs & mLo & SGN;
    else so[1] |= (hs | (tp == 9 ? SGN : 0u)) & SGN;
    PIN(so[0]); PIN(so[1]);
    const int xl = n > 0 ? L_X + ((n - 1) * 7 + s0) * 512 : L_NONE, xh = n < 3 ? L_X + (n * 7 + s0) * 512 : L_NONE;
    aXr = xl + (int)h32 * (xh - xl) + ln * 4;
    aXw = aXr ^ 128;
    aCS = L_CS + n * 64 + g16;
    aCV = L_CS + 512 + n * 64 + i4;
    if (NS == 4) { aCS += i16 * 64 + min(i16, 1u) * 0x10000; aCV += min(g16, 1u) * 0x10000; }
    aF = Y_F + s0 * 4 + n * 4 + ln * 4 + min(ln, 1u) * 0x10000;
    aST = L_MR + s0 * 64 + i4;
    aMR = L_MR + s0 * 64 + g16;
    aH = n * 512 + ln * 8;
    const u32 xa = WS_XS + (((parity * 10 + tp) * 10 + j) * 14 + (n ? 7 : 0) + s0) * 512 + (edge ? 0u : SGN);
    xg = (xa + (ln & 31) * 16) | ((hs ^ (n ? SGN : 0u)) & SGN);
    PIN(aXw); PIN(aXr); PIN(aCS); PIN(aCV); PIN(aST); PIN(aMR); PIN(aH); PIN(aF); PIN(xg);
  }
};

template <int NS>
DEV void main_compute(const Params& p, rsrc_t rws, u32& tag, int lane, int wave, int tp, int parity, int rb, int j, int mg, int ng, G2& g2 STM_ARG) {
  constexpr int s0 = NS == 4 ? 0 : 4;
  const int n = (wave & 3) ^ (NS == 3);
  const int rt = parity * 10 + tp;
  u32 ep = epoch_get(p);
  Att at;
  at.init_k(lane, n);
  if (NS == 4) st_u(lane < 32 ? wave * SLOT + XB + lane * 4 : L_SYNC + wave * 64 + (lane & 15) * 4, lane < 32 ? 0x3f803f80u : 0u);
  int tau = n - rb; tau += tau < 0 ? 5 : 0;
  const bool edge = ((0x0f00001e >> (n * 8 + tau)) & 1) != 0;
  SB();
  PST(2);
  if (INIT1) at.template init<NS>(lane, n, tau, edge, tp, parity, j);
  SB();
  PST(3);
  tag = epoch_tag(ep);
  const u32 (&aA0)[2] = at.aA0, (&aA1)[2] = at.aA1, (&aB)[2] = at.aB, (&so)[2] = at.so;
  const u32 &mOwn = at.mOwn, &mRot = at.mRot, &mV = at.mV, &mLo = at.mLo, &xg = at.xg, &aXw = at.aXw, &aXr = at.aXr, &aCS = at.aCS, &aCV = at.aCV,
            &aST = at.aST, &aMR = at.aMR, &aH = at.aH, &aF = at.aF;
  const u4& mS = at.mS;
  f4 acc[NS][3];
  u2 m0, mm, mp;
#pragma unroll
  for (int a = 0; a < NS; ++a)
#pragma unroll
    for (int b = 0; b < 3; ++b) acc[a][b] = f4{0.f, 0.f, 0.f, 0.f};
  bf8 A[3][NS], B[3][3];
  auto ldf = [&](int buf, int st, int kk) {
    const int rs = (st + 3) & 3, h = rs >> 1;
    const u32 of = (rs & 1) * SLOT;
    B[buf][0] = ld_tr8(aB[h] + of + kk * 4096, 512);
    A[buf][0] = ld_b128((kk ? aA1[h] : aA0[h]) + of + s0 * 2048);
    B[buf][1] = ld_tr8(aB[h] + of + kk * 4096 + 8192, 512);
    B[buf][2] = ld_tr8(aB[h] + of + kk * 4096 + 16384, 512);
#pragma unroll
    for (int a = 1; a < NS; ++a) A[buf][a] = ld_b128((kk ? aA1[h] : aA0[h]) + of + (s0 + a) * 2048);
  };
  auto mma = [&](int buf, int b0, int b1) {
    if (MORD == 1) {
#pragma unroll
      for (int b = b0; b < b1; ++b)
#pragma unroll
        for (int i = 0; i < NS; ++i) {
          const int a = (b & 1) ? NS - 1 - i : i;
          acc[a][b] = b < 2 ? mfma(B[buf][b], A[buf][a], acc[a][b]) : mfma(A[buf][a], B[buf][b], acc[a][b]);
        }
    } else if (MORD == 2) {
#pragma unroll
      for (int a = 0; a < NS; ++a)
#pragma unroll
        for (int i = 0; i < 2; ++i) { const int b = (a & 1) ? 1 - i : i; acc[a][b] = mfma(B[buf][b], A[buf][a], acc[a][b]); }
      if (b1 == 3) {
#pragma unroll
        for (int a = 0; a < NS; ++a) acc[a][2] = mfma(A[buf][a], B[buf][2], acc[a][2]);
      }
    } else {
#pragma unroll
    for (int a = 0; a < NS; ++a)
#pragma unroll
      for (int b = b0; b < b1; ++b)
        acc[a][b] = b < 2 ? mfma(B[buf][b], A[buf][a], acc[a][b]) : mfma(A[buf][a], B[buf][b], acc[a][b]);
    }
  };
  auto pattern = [&]() {
#pragma unroll
    for (int i = 0; i < NS + 6; ++i) {
      if (i < 3 * NS) __builtin_amdgcn_sched_group_barrier(0x008, 1, 0);
      __builtin_amdgcn_sched_group_barrier(0x100, 1, 0);
    }
    if (3 * NS > NS + 6) __builtin_amdgcn_sched_group_barrier(0x008, 2 * NS - 6, 0);
  };

  STAMP(1);
  PST(4);
  if (PBRK && NS == 4) NEW_PAGE();
  lds_bar();
  PST(5);
  STAMP(2);
  CSTAMP(0);
  ldf(0, 0, 0);
#pragma unroll
  for (int s = 0; s < 9; ++s) {
    SB();
    ldf(1, s, 1);
    mma(0, 0, 3);
    if (s == 8) {
#pragma unroll
      for (int i = 0; i < (NS + 7) / 2; ++i) {
        __builtin_amdgcn_sched_group_barrier(0x008, 1, 0);
        __builtin_amdgcn_sched_group_barrier(0x100, 2, 0);
      }
    } else pattern();
    SB();
    if (s == 0 && !INIT1) { at.template init<NS>(lane, n, tau, edge, tp, parity, j); SB(); }
    if (s == 8) break;
    if (s >= 1) KST(2 * s - 1);
    if (s < 3) PST(6 + 2 * s);
    if (NS == 3 && s >= PACE0 && PACE) asm volatile(".rept %0\n\ts_nop 7\n\t.endr" :: "i"(PACE));
    bar();
    if (PBRK && NS == 3 && s == 7) NEW_PAGE();
    if (s < 3) PST(7 + 2 * s);
    KST(2 * s);
    SB();
    ldf(0, s + 1, 0);
    mma(1, 0, 3);
    pattern();
    SB();
  }
  u32 inner = (n + 1) & 2;
  SPIN(inner);
#define PRIO(PI, PO) asm volatile("s_setprio " #PI "\n\ts_cmp_lg_u32 %0, 0\n\ts_cbranch_scc1 1f\n\ts_setprio " #PO "\n\t1:" :: "s"(inner) : "scc")
  u32 lev = 0;
  auto step = [&]() { ++lev; asm volatile("ds_write_b32 %0, %1" :: "v"(aF), "v"(lev) : "memory"); };
  PRIO(2, 3);
  KST(15);
  if (PBRK && NS == 4) NEW_PAGE();
  lds_bar();
  FST(0);
  SB();
  auto mmqk = [&](int buf) {
    mma(buf, 0, 2);
    if (NS == 4) acc[0][2] = mfma(A[buf][0], B[buf][2], acc[0][2]);
  };
  f2 stt[NS];
  ldf(0, 9, 0);
  ldf(2, 9, 1);
  if (NS == 3) {
#pragma unroll
    for (int a = 0; a < NS; ++a) { stt[a].x = ld_f(aST + a * 64); stt[a].y = ld_f(aST + a * 64 + 512); FENCE(); }
    SB();
    mmqk(1);
    SB();
  } else {
    mmqk(1);
#pragma unroll
    for (int i = 0; i < 7; ++i) {
      __builtin_amdgcn_sched_group_barrier(0x008, 1, 0);
      __builtin_amdgcn_sched_group_barrier(0x100, 3, 0);
    }
    SB();
#pragma unroll
    for (int a = 0; a < NS; ++a) { stt[a].x = ld_f(aST + a * 64); stt[a].y = ld_f(aST + a * 64 + 512); FENCE(); }
    SB();
  }
  FST(1);
  mmqk(0);
  SB();
  f4 cs[2];
  u32 lvl;
  auto reached = [&](u32 v) { return __builtin_expect((u32)__builtin_amdgcn_readfirstlane(lvl) >= v, 1); };
  auto peekcs = [&]() {
    FENCE(); lvl = lds_peek(Y_F + n * 4); FENCE();
    cs[0] = ld_f4(aCS); cs[1] = ld_f4(aCS + 256);
    FENCE();
  };
  if (NS == 4) {
    acc[0][0] = mfma(B[2][0], A[2][0], acc[0][0]); acc[0][1] = mfma(B[2][1], A[2][0], acc[0][1]); acc[0][2] = mfma(A[2][0], B[2][2], acc[0][2]);
#pragma unroll
    for (int a = 1; a < 3; ++a) { acc[a][0] = mfma(B[2][0], A[2][a], acc[a][0]); acc[a][1] = mfma(B[2][1], A[2][a], acc[a][1]); }
#pragma unroll
    for (int a = 0; a < NS; ++a) PIN(stt[a]);
    SB();
    st_f4(aCS, acc[0][0]); st_f4(aCS + 256, acc[0][1]); st_f(aCV, acc[0][2][0]);
    step();
    SB();
    acc[NS - 1][0] = mfma(B[2][0], A[2][NS - 1], acc[NS - 1][0]); acc[NS - 1][1] = mfma(B[2][1], A[2][NS - 1], acc[NS - 1][1]);
#pragma unroll
    for (int b = 1; b >= 0; --b)
#pragma unroll
      for (int r = 0; r < 4; ++r) cs[b][r] = dppf<0x150>(acc[0][b][r]);
    PIN(cs[0]); PIN(cs[1]);
  } else {
    acc[0][0] = mfma(B[2][0], A[2][0], acc[0][0]); acc[0][1] = mfma(B[2][1], A[2][0], acc[0][1]);
    SB();
    peekcs();
    SB();
#pragma unroll
    for (int a = 1; a < NS; ++a) { acc[a][0] = mfma(B[2][0], A[2][a], acc[a][0]); acc[a][1] = mfma(B[2][1], A[2][a], acc[a][1]); }
    SB();
    if (!reached(1)) {
      lds_wait_ge<0>(Y_F + n * 4, 1);
      peekcs();
    }
  }
#pragma unroll
  for (int a = 0; a < NS; ++a) { PIN(acc[a][0]); PIN(acc[a][1]); PIN(acc[a][2]); }
  SB();
  CSTAMP(1);
  STAMP(3);
  FST(2);
  auto bc4 = [](float v) { return f4{v, v, v, v}; };
  auto pk4 = [](f4 v) { u2 r; r.x = pk2(v[0], v[1]); r.y = pk2(v[2], v[3]); return r; };
  u2 q[NS], kLo[NS], kHi[NS + 1];
#pragma unroll
  for (int a = 0; a < NS; ++a) { acc[a][1] = __builtin_elementwise_fma(bc4(-stt[a].x), cs[1], acc[a][1]); PIN(acc[a][1]); }
  SB();
#pragma unroll
  for (int a = 0; a < NS; ++a) { acc[a][1] *= stt[a].y; PIN(acc[a][1]); }
  SB();
  {
    u2 k[NS];
#pragma unroll
    for (int a = 0; a < NS; ++a) { k[a] = pk4(acc[a][1]); PIN(k[a]); }
    SB();
#pragma unroll
    for (int a = 0; a < NS; ++a) { acc[a][0] = __builtin_elementwise_fma(bc4(-stt[a].x), cs[0], acc[a][0]); PIN(acc[a][0]); }
    SB();
#pragma unroll
    for (int i = 0; i < NS; ++i) {
      const int a = NS - 1 - i;
      asm volatile("v_and_b32 %0, %4, %7\n\tv_and_b32 %1, %5, %7\n\t"
                   "v_and_b32_dpp %0, %4, %6 row_ror:8 row_mask:0xf bank_mask:0x3\n\tv_and_b32_dpp %1, %5, %6 row_ror:8 row_mask:0xf bank_mask:0x3\n\t"
                   "v_and_b32 %2, %4, %6\n\tv_and_b32 %3, %5, %6\n\t"
                   "v_and_b32_dpp %2, %4, %7 row_ror:8 row_mask:0xf bank_mask:0xc\n\tv_and_b32_dpp %3, %5, %7 row_ror:8 row_mask:0xf bank_mask:0xc"
                   : "=&v"(kHi[a + 1].x), "=&v"(kHi[a + 1].y), "=&v"(kLo[a].x), "=&v"(kLo[a].y) : "v"(k[a].x), "v"(k[a].y), "v"(mOwn), "v"(mRot));
      if (NS == 4 && i == 0) { st_u2(L_HK + aH, kHi[NS]); step(); }
    }
  }
  if (NS == 3) { FENCE(); lvl = lds_peek(Y_F + n * 4); FENCE(); kHi[0] = ld_u2(L_HK + aH); FENCE(); }
  SB();
#pragma unroll
  for (int a = 0; a < NS; ++a) { q[a] = pk4(acc[a][0]); PIN(q[a]); }
  SB();
  FST(3);
  const f4 z = {0.f, 0.f, 0.f, 0.f};
  f4 t[NS];
  u4 tv[NS];
  {
    f4 d0[NS], d1[NS];
    u4 qq[NS];
    u32 c0 = tp == 0 ? 0xf149f2cau : 0u;
    SPIN(c0);
#pragma unroll
    for (int a = NS - 1; a >= 0; --a) {
      d0[a] = mfma16(kLo[a], q[a], (NS == 4 && a == 0) ? bc4(__uint_as_float(c0)) : z);
      qq[a].x = q[a].x & mLo; qq[a].y = q[a].y & mLo; qq[a].z = q[a].x & ~mLo; qq[a].w = q[a].y & ~mLo;
    }
#pragma unroll
    for (int a = NS - 1; a > 0; --a) { d1[a] = mfma(mk8(kHi[a], kHi[a + 1]), __builtin_bit_cast(bf8, qq[a]), z); tv[a].y = tv[a].w = tag; }
    tv[0].y = tv[0].w = tag;
    if (NS == 4) {
      d1[0] = mfma16(kHi[1], q[0], z);
    } else {
      if (!reached(2)) { lds_wait_ge<0>(Y_F + n * 4, 2); kHi[0] = ld_u2(L_HK + aH); }
      d1[0] = mfma(mk8(kHi[0], kHi[1]), __builtin_bit_cast(bf8, qq[0]), z);
    }
#pragma unroll
    for (int a = NS - 1; a >= 0; --a) {
#pragma unroll
      for (int r = 0; r < 4; ++r) t[a][r] = fsel(mS[r], d0[a][r], d1[a][r]);
      PIN(t[a]);
    }
  }
  SB();
  FST(4);
#pragma unroll
  for (int a = NS - 1; a >= 0; --a) { tv[a].x = pk2(t[a][0], t[a][1]); tv[a].z = pk2(t[a][2], t[a][3]); PIN(tv[a]); }
  if (__builtin_expect(edge, 1)) {
#pragma unroll
    for (int a = NS - 1; a >= 0; --a) __builtin_amdgcn_raw_buffer_store_b128(tv[a], rws, xg + a * 512, 0, 16);
  }
  SB();
#pragma unroll
  for (int a = 0; a < NS; ++a)
    asm volatile("ds_write2st64_b32 %0, %1, %2 offset0:%3 offset1:%4" :: "v"(aXw), "v"(tv[a].x), "v"(tv[a].z), "i"(2 * a), "i"(2 * a + 1) : "memory");
  step();
  STAMP(10);
  FST(5);
  SB();
  PRIO(2, 1);
  f4 vm[NS], vr[NS];
  const float csv = ld_f((aCV & 0xffffu) | (L_CS & ~0xffffu));
#pragma unroll
  for (int a = 0; a < NS; ++a) { vm[a] = ld_f4(aMR + a * 64); vr[a] = ld_f4(aMR + 512 + a * 64); }
  SB();
#pragma unroll
  for (int bi = 0; bi < 3; ++bi)
#pragma unroll
    for (int a = NS - 1; a >= (NS == 4 ? 1 : 0); --a) { const int buf = bi == 0 ? 1 : (bi == 1 ? 0 : 2); acc[a][2] = mfma(A[buf][a], B[buf][2], acc[a][2]); }
  m0.x = (mS.x & 0xffffu) | (mS.y & 0xffff0000u); m0.y = (mS.z & 0xffffu) | (mS.w & 0xffff0000u);
  mm.x = ~m0.x & mLo; mm.y = ~m0.y & mLo; mp.x = ~(m0.x | mLo); mp.y = ~(m0.y | mLo);
  u32 uLo = 0x3f80u, uHi = 0x3f800000u;
  u2 hs; hs.x = hs.y = (lane < 32 ? mV : ~mV) & 0x3f803f80u;
  u32 aFn = Y_F + s0 * 4 + min(max(n + (lane < 32 ? -1 : 1), 0), 3) * 4;
  u32 xr = xg + (n ? 7 * 512 : -7 * 512);
  float cq[NS];
#pragma unroll
  for (int a = 0; a < NS; ++a) { cq[a] = stt[a].y * (0.22360679774997896f * 1.4426950408889634f); PIN(cq[a]); }
  PIN(m0); PIN(mm); PIN(mp); PIN(uLo); PIN(uHi); PIN(hs); PIN(aFn); PIN(xr);
#pragma unroll
  for (int a = 0; a < NS; ++a) PIN(acc[a][2]);
  SB();
  FST(6);
  u4 e[NS];
#pragma unroll
  for (int a = 0; a < NS; ++a) asm volatile("" : "=v"(e[a]));
  auto look = [&]() {
#pragma unroll
    for (int a = 0; a < NS; ++a) e[a] = __builtin_amdgcn_raw_buffer_load_b128(rws, xr + a * 512, 0, 16);
  };
  u2 vLo[NS], vHi[NS + 1];
#pragma unroll
  for (int a = NS - 1; a >= 0; --a) { acc[a][2] = __builtin_elementwise_fma(vm[a], bc4(-csv), acc[a][2]); PIN(acc[a][2]); }
  SB();
#pragma unroll
  for (int a = NS - 1; a >= 0; --a) { acc[a][2] *= vr[a]; PIN(acc[a][2]); }
  SB();
#pragma unroll
  for (int a = NS - 1; a >= 0; --a) { vLo[a] = pk4(acc[a][2]); PIN(vLo[a]); }
  SB();
  if (__builtin_expect(edge, 1)) look();
  SB();
  constexpr u32 XT = NS == 4 ? 3 : 1;
  u2 y[NS];
  u32 cx;
  auto ldy = [&]() {
    FENCE(); cx = lds_peek(aFn); FENCE();
#pragma unroll
    for (int a = 0; a < NS; ++a) { y[a].x = ld_u(aXr + a * 512); y[a].y = ld_u(aXr + a * 512 + 256); }
    FENCE();
  };
  ldy();
  SB();
#pragma unroll
  for (int a = NS - 1; a >= 0; --a) {
    vHi[a + 1].x = vLo[a].x & ~mV; vHi[a + 1].y = vLo[a].y & ~mV; vLo[a].x &= mV; vLo[a].y &= mV;
    PIN(vLo[a]); PIN(vHi[a + 1]);
  }
  SB();
#pragma unroll
  for (int a = NS - 1; a >= 0; --a) {
    auto wx = __builtin_amdgcn_permlane32_swap(vLo[a].x, vHi[a + 1].x, false, false);
    auto wy = __builtin_amdgcn_permlane32_swap(vLo[a].y, vHi[a + 1].y, false, false);
    vLo[a].x = wx[0]; vLo[a].y = wy[0]; vHi[a + 1].x = wx[1]; vHi[a + 1].y = wy[1];
    PIN(vLo[a]); PIN(vHi[a + 1]);
    if (NS == 4 && a == NS - 2) { st_u2(L_HV + aH, vHi[NS]); step(); }
  }
  if (NS == 3) { FENCE(); lvl = lds_peek(Y_F + n * 4); FENCE(); vHi[0] = ld_u2(L_HV + aH); FENCE(); }
  bf8 vH[NS];
#pragma unroll
  for (int a = 1; a < NS; ++a) { vH[a] = mk8(vHi[a], vHi[a + 1]); PIN(vH[a]); }
  SB();
  FST(7);
  if (__any(cx < XT)) {
#pragma nounroll
    for (u32 spin = 0; spin < (1u << 20); ++spin) {
      FENCE();
      if (__all(lds_peek(aFn) >= XT)) break;
    }
    ldy();
  }
  auto addp = [](f4& t, u32 a, u32 b, u32 lo, u32 hi) {
    t[0] = __builtin_amdgcn_fdot2_f32_bf16(__builtin_bit_cast(bf2, a), __builtin_bit_cast(bf2, lo), t[0], false);
    t[1] = __builtin_amdgcn_fdot2_f32_bf16(__builtin_bit_cast(bf2, a), __builtin_bit_cast(bf2, hi), t[1], false);
    t[2] = __builtin_amdgcn_fdot2_f32_bf16(__builtin_bit_cast(bf2, b), __builtin_bit_cast(bf2, lo), t[2], false);
    t[3] = __builtin_amdgcn_fdot2_f32_bf16(__builtin_bit_cast(bf2, b), __builtin_bit_cast(bf2, hi), t[3], false);
  };
#pragma unroll
  for (int a = 0; a < NS; ++a) { addp(t[a], y[a].x, y[a].y, uLo, uHi); PIN(t[a]); }
  if (NS == 3) {
    if (!reached(4)) { lds_wait_ge<0>(Y_F + n * 4, 4); vHi[0] = ld_u2(L_HV + aH); }
    vH[0] = mk8(vHi[0], vHi[1]); PIN(vH[0]);
  }
  PRIO(2, 3);
  STAMP(8);
  FST(8);
  if (__builtin_expect(edge, 1)) {
    u32 etag = (int)xg < 0 ? 0u : tag;
    PIN(etag);
    auto bad = [&]() {
      u32 b = __builtin_amdgcn_bitop3_b32(e[0].y, e[0].w, etag, 0x7e);
#pragma unroll
      for (int a = 1; a < NS; ++a) b |= __builtin_amdgcn_bitop3_b32(e[a].y, e[a].w, etag, 0x7e);
      PIN(b);
      return __any(b != 0u);
    };
    if (bad()) {
#pragma nounroll
      for (u32 spin = 0; spin < SPIN_CAP; ++spin) {
        FENCE();
        look();
        if (!bad()) break;
      }
    }
#pragma unroll
    for (int a = 0; a < NS; ++a) { addp(t[a], e[a].x, e[a].z, uLo, uHi); PIN(t[a]); }
  }
  STAMP(6);
  FST(9);
  SB();
  float mx[4];
#pragma unroll
  for (int a = 0; a < NS; ++a) { mx[a] = vmax(vmax(t[a][0], t[a][1]), t[a][2]); PIN(mx[a]); }
#pragma unroll
  for (int a = 0; a < NS; ++a) { mx[a] = vmax(mx[a], t[a][3]); PIN(mx[a]); }
  if (NS == 3) mx[3] = mx[2];
  SB();
  {
    u32 c[2];
#pragma unroll
    for (int h = 0; h < 2; ++h) {
      auto w = __builtin_amdgcn_permlane16_swap(__float_as_uint(mx[2 * h]), __float_as_uint(mx[2 * h + 1]), false, false);
      mx[2 * h] = __uint_as_float(w[0]); mx[2 * h + 1] = __uint_as_float(w[1]);
    }
    SB();
#pragma unroll
    for (int h = 0; h < 2; ++h) c[h] = __float_as_uint(vmax(mx[2 * h], mx[2 * h + 1]));
    SB();
#pragma unroll
    for (int h = 0; h < 2; ++h) {
      if (NS == 3 && h == 1) { mx[2] = __uint_as_float(c[1]); continue; }
      auto w = __builtin_amdgcn_permlane16_swap(c[h], c[h], false, false);
      mx[2 * h] = __uint_as_float(w[0]); mx[2 * h + 1] = __uint_as_float(w[1]);
    }
    SB();
  }
#pragma unroll
  for (int a = 0; a < NS; ++a) { mx[a] *= -cq[a]; PIN(mx[a]); }
  SB();
#pragma unroll
  for (int a = 0; a < NS; ++a) { t[a] = __builtin_elementwise_fma(t[a], bc4(cq[a]), bc4(mx[a])); PIN(t[a]); }
  SB();
#pragma unroll
  for (int a = 0; a < NS; ++a) {
#pragma unroll
    for (int r = 0; r < 4; ++r) t[a][r] = __builtin_amdgcn_exp2f(t[a][r]);
    PIN(t[a]);
  }
  SB();
  FST(10);
  u2 pp[NS], p0[NS];
  bf8 p1[NS];
#pragma unroll
  for (int a = 0; a < NS; ++a) { pp[a] = pk4(t[a]); PIN(pp[a]); }
  SB();
  f4 o[NS], sm[NS];
#pragma unroll
  for (int a = 0; a < NS; ++a) {
    sm[a] = mfma16(hs, pp[a], z);
    p0[a] = and2(pp[a], m0); PIN(p0[a]);
    SB();
  }
#pragma unroll
  for (int a = 0; a < NS; ++a) {
    o[a] = mfma16(vLo[a], p0[a], z);
    if (NS == 4 && a == 0) { p0[0] = and2(pp[0], mp); PIN(p0[0]); }
    else { p1[a] = mk8(and2(pp[a], mm), and2(pp[a], mp)); PIN(p1[a]); }
    SB();
  }
#pragma unroll
  for (int a = 0; a < NS; ++a) {
    o[a] = (NS == 4 && a == 0) ? mfma16(vHi[1], p0[0], o[a]) : mfma(vH[a], p1[a], o[a]);
    mx[a] = __builtin_amdgcn_rcpf(sm[a][0]); PIN(mx[a]);
    SB();
  }
  u2 ob[4];
#pragma unroll
  for (int a = 0; a < NS; ++a) { o[a] *= mx[a]; PIN(o[a]); }
  SB();
#pragma unroll
  for (int a = 0; a < NS; ++a) { ob[a] = pk4(o[a]); PIN(ob[a]); }
  if (NS == 3) ob[3] = ob[2];
  SB();
#pragma unroll
  for (int h = 0; h < 2; ++h) {
    auto wx = __builtin_amdgcn_permlane32_swap(ob[2 * h].x, ob[2 * h + 1].x, false, false);
    auto wy = __builtin_amdgcn_permlane32_swap(ob[2 * h].y, ob[2 * h + 1].y, false, false);
    u4 v; v.x = wx[0]; v.y = wy[0]; v.z = wx[1]; v.w = wy[1];
    __builtin_amdgcn_raw_buffer_store_b128(v, rws, so[h], 0, 16);
  }
  CSTAMP(2);
  STAMP(4);
  FST(11);
  __builtin_amdgcn_s_setprio(0);
  SB();
  if (PBR2) NEW_PAGE();
  SB();
  {
    const u32 r0 = tp * 104, mq = (r0 >> 6) + (lane >> 3);
    u32 old, tv1 = tag;
    u32 fo = WS_BOX + (parity * 16 + (r0 >> 6)) * 2048 + (j >= 5 ? 64 : 0) + rb * 4 + lane * 256 + (mq * 64 < r0 ? 20 : 0);
    fo |= mq * 64 > min(r0 + 103u, 1023u) ? 0x80000000u : 0u;
    PIN(fo); PIN(tv1);
    g2.init(wave, mg, ng);
    SB();
    __builtin_amdgcn_s_setprio(3);
    asm volatile("s_mov_b64 exec, 1\n\ts_waitcnt vmcnt(0)\n\tds_add_rtn_u32 %0, %1, %2\n\ts_mov_b32 exec_lo, 0xffffff\n\ts_waitcnt lgkmcnt(0)\n\t"
                 "v_readfirstlane_b32 vcc_lo, %0\n\ts_cmp_lg_u32 vcc_lo, 7\n\ts_cbranch_scc1 1f\n\t"
                 "buffer_atomic_swap %3, %4, %5, 0 offen\n1:\n\ts_mov_b64 exec, -1"
                 : "=&v"(old) : "v"(g2.pa), "v"(g2.one), "v"(tv1), "v"(fo), "s"(srd(p.ws)) : "scc", "vcc", "memory");
  }
  STAMP(5);
  FST(12);
}

constexpr u32 A_D = 0, A_G = L_WO, A_U = L_WO + 40960;
constexpr u32 A_XN = 40960;
constexpr u32 A_RED = A_XN + 2560;
constexpr u32 A_H = A_RED + 2048;
constexpr u32 A_TOP = A_H + 1024;

DEV void aux_loader(Params& p, int lane, int lw, int a7, int f, bool ffn, int mg, int ng, int koff, Epi& ei STM_ARG) {
  const u32 ep = epoch_get(p);
  const u4 srdo = srd(p.wo), srdg = srd(p.wg), srdu = srd(p.wu), srdd = srd(p.wd);
  const int rr = lane >> 2;
  u32 vg = (lw * 16 + rr) * (F_ * 2) + f * 64 + (((lane & 3) ^ (((rr >> 3) & 1) << 1)) << 4);
  u32 vd = (f * 32 + lw * 8 + (lane >> 3)) * 1280 + (lane & 7) * 16;
  u32 vt = min((f * 4 + lw) * 30 + min(lane, 29), 6399) * 128;
  u32 ho = (lane | lw) ? 0x80000000u : WS_FLAG + (200 + f) * 256 + 128;
  PIN(vg); PIN(vd); PIN(vt); PIN(ho);
  ei.init(lane, lw, mg, ng);
  WoDma wo; wo.init(lane, lw, ng, WOSTA);
  u32 tag = epoch_tag(ep);
  PIN(tag);
  if (FFN_DELAY) __builtin_amdgcn_s_sleep(FFN_DELAY);
  STAMP(9);
  if (ffn) {
    const u4 s0 = (f & 1) ? srdu : srdg, s1 = (f & 1) ? srdg : srdu;
    const u32 a0 = ((f & 1) ? A_U : A_G) + lw * 1024, a1 = ((f & 1) ? A_G : A_U) + lw * 1024;
    dma_run<10, 4096, 64 * F_ * 2, 0, TH0>(a0, vg, s0, 0);
    asm volatile("s_mov_b32 m0, %0\n\ts_nop 0\n\tbuffer_load_dword %1, %2, 0 offen lds" :: "s"(L_DUMP + lw * 256), "v"(vt), "s"(srdo));
    STAMP(10);
    dma_run<10, 4096, 64 * F_ * 2, 0, TH1>(a1, vg, s1, 0);
    if (!DLATE) dma_run<10, 4096, 128>(A_D + lw * 1024, vd, srdd, 0);
    STAMP(1);
  } else {
    wo.issue<0, 27, TH0>(srdo, lw);
  }
  wait_bar<DLATE ? 0 : 10>();
  if (DLATE && ffn) dma_run<10, 4096, 128>(A_D + lw * 1024, vd, srdd, 0);
  asm volatile("buffer_store_dword %0, %1, %2, 0 offen sc1" :: "v"(tag), "v"(ho), "s"(srd(p.ws)));
  if (BOXW == 2) asm volatile("buffer_atomic_add %0, %1, %2, 0 offen" :: "v"(0u), "v"((lane | (lw & 2)) ? 0x80000000u : WS_BOX + (mg * 8 + ng) * 256 + lw * 64), "s"(srd(p.ws)));
  bar();
  STAMP(2);
  ei.issue(srd(p.x), srd(p.ws), WS_ST2);
  wo.issue<0, WOC, AW0, 1>(srdo, lw, f);
  asm volatile("s_cmp_gt_u32 %0, 53\n\ts_cbranch_scc1 1f\n\ts_waitcnt vmcnt(%1)\n\ts_barrier\n\ts_branch 2f\n1:\n\ts_waitcnt vmcnt(0)\n2:" :: "s"(f), "i"(WOC + 5) : "scc", "memory");
  STAMP(3);
  ei.wait<WOC>();
  ei.check(p.ws.p, tag);
  SB();
#pragma unroll
  for (int i = 0; i < YG; ++i) ei.gate(i);
  SB();
  asm volatile("buffer_load_dwordx4 %0, off, %1, 0 offset:%2 sc1" : "=v"(ei.fin) : "s"(srd(p.ws)), "i"(WS_FIN));
  wo.issue<WOC, 27, AWT, 1>(srdo, lw, f);
  asm volatile("s_cmp_gt_u32 %1, 53\n\ts_cbranch_scc0 1f\n\ts_waitcnt vmcnt(1)\n\ts_sleep %3\n1:\n\ts_waitcnt vmcnt(%2)" : "+v"(ei.fin) : "s"(f), "i"(27 - WOC), "i"(NFD) : "scc");
  STAMP(4);
}

DEV void aux_compute(const Params& p, rsrc_t rws, u32& tag, int lane, int wave, int f, bool ffn, int xcd, int a7, RowIn& rin STM_ARG) {
  const int task = a7 + 7 * wave;
  if (task < 32) rin.stats(p, rws, tag, lane, xcd * 256 + task * 8, A_TOP + wave * 16 STM);
  else tag = epoch_tag(epoch_get(p));
  PST(4);
  const int g = lane >> 4, i16 = lane & 15;
  const int q = i16 >> 2, pp = lane & 3;
  const int t = wave & 1, kq = wave >> 1;
  u32 ab = (kq * 160 + 8 * g + q) * 64 + (((2 * t + (pp >> 1)) ^ ((g & 1) << 1)) << 4) + (pp & 1) * 8;
  u32 xb = A_XN + (i16 & 1) * 1280 + (kq * 160 + 8 * g) * 2;
  u32 ar = i16 < 2 ? A_RED + (wave * 16 + g * 2 + i16) * 16 : L_NONE + lane * 16;
  u32 ad[5], ah[5];
#pragma unroll
  for (int i = 0; i < 5; ++i) {
    const int nt = wave * 5 + i;
    ad[i] = A_D + (8 * g + q) * 128 + pp * 8 + (nt >> 2) * 4096 + (nt & 3) * 32;
    ah[i] = (i16 & 7) == i ? A_H + wave * 128 + (i16 >> 3) * 64 + g * 16 : L_NONE + lane * 16;
    PIN(ad[i]); PIN(ah[i]);
  }
  u32 as = A_RED + (lane >> 5) * 256 + (lane & 31) * 4, aw = A_H + wave * 128 + ((lane >> 2) & 1) * 64 + (4 * (lane >> 3) + (lane & 3)) * 2;
  u32 yo = WS_YP + (((i16 >> 3) * 8 + wave) * 54 + f) * 512 + g * 128 + (i16 & 7) * 16;
  PIN(ab); PIN(xb); PIN(ar); PIN(as); PIN(aw); PIN(yo);
  STAMP(1);
  u32 so = 0x80000000u, xo = 0x80000000u;
  if (wave >= 5) {
    if (wave == 5) st_u(L_SYNC + lane * 4, 0);
    const u32 nt = (a7 < 4 && wave == 5) ? 5 : 4, a = A_TOP + min((u32)lane & 7, nt - 1) * 16;
    u4 k;
#pragma nounroll
    for (u32 spin = 0; spin < (1u << 20); ++spin) {
      FENCE();
      k = __builtin_bit_cast(u4, ld_f4(a));
      if (__all(k.y == tag && k.w == tag)) break;
      __builtin_amdgcn_s_sleep(1);
    }
    if (wave == 5) {
      if ((lane & 7) >= nt) k.x = k.z = 0;
      u32 k1 = k.x, k2 = k.z;
      top2_merge(k1, k2, dppu<0xB1>(k1), dppu<0xB1>(k2));
      top2_merge(k1, k2, dppu<0x4E>(k1), dppu<0x4E>(k2));
      top2_merge(k1, k2, dppu<0x141>(k1), dppu<0x141>(k2));
      k.x = k1; k.z = k2;
      bst128_sc1(rws, lane < 8 ? WS_KEY + f * 128 + lane * 16 : 0x80000000u, k);
    } else {
      __builtin_amdgcn_s_sleep(KEY_DELAY);
#pragma nounroll
      for (u32 spin = 0;; ++spin) {
        FENCE();
        k = bld128_sc1(rws, WS_KEY + min(lane, 55) * 128);
        if (__all(k.y == tag && k.w == tag) || spin > SPIN_CAP) break;
        __builtin_amdgcn_s_sleep(8);
      }
      u32 k1 = lane < 56 ? k.x : 0, k2 = lane < 56 ? k.z : 0;
      top2_wave(k1, k2);
      k.x = k1; k.z = k2;
      if (f == 55) bst128_sc1(rws, (lane | t * 8) < 8 ? WS_FIN + lane * 16 : 0x80000000u, k);
      const int tk = key_tok(__builtin_amdgcn_readfirstlane(t ? k2 : k1));
      xo = tk * 1280; so = WS_ST2 + ((tk & 1) * 1024 + (tk >> 1)) * 16;
    }
  }
  const rsrc_t rx = __builtin_amdgcn_make_buffer_rsrc(p.x.p, 0, S_ * D_ * 2, 0x00020000);
  const u4 xa = __builtin_amdgcn_raw_buffer_load_b128(rx, xo + lane * 16, 0, 0);
  const u32 xc = __builtin_amdgcn_raw_buffer_load_b32(rx, xo + 1024 + lane * 4, 0, 0);
  u4 sv = bld128_sc1(rws, so);
  FENCE();
  STAMP(2);
  NEW_PAGE();
  lds_bar();
  STAMP(3);
  bf8 Wg[5], Wu[5];
#pragma unroll
  for (int ks = 0; ks < 5; ++ks) { Wg[ks] = ld_tr8(A_G + ab + ks * 2048, 256); Wu[ks] = ld_tr8(A_U + ab + ks * 2048, 256); }
#pragma unroll
  for (int ks = 0; ks < 5; ++ks) { PIN(Wg[ks]); PIN(Wu[ks]); }
  bar();
  if (__builtin_expect(wave >= 6, 1)) {
    if (__builtin_expect(__any(__builtin_amdgcn_bitop3_b32(sv.y, sv.w, tag, 0x7e) != 0u), 0)) {
#pragma nounroll
      for (u32 spin = 0; !__all(sv.y == tag && sv.w == tag) && spin < SPIN_CAP; ++spin) {
        FENCE();
        __builtin_amdgcn_s_sleep(1);
        sv = bld128_sc1(rws, so);
      }
    }
    const float m = __uint_as_float(sv.x), r_ = __uint_as_float(sv.z);
    u4 o; o.x = norm2(xa.x, m, r_); o.y = norm2(xa.y, m, r_); o.z = norm2(xa.z, m, r_); o.w = norm2(xa.w, m, r_);
    st_u4(A_XN + t * 1280 + lane * 16, o); st_u(A_XN + t * 1280 + 1024 + lane * 4, norm2(xc, m, r_));
    lds_add(Y_CB, 1);
  }
  if (__builtin_expect(!ffn, 0)) {
    if (f == 55 && wave == 5) {
#pragma nounroll
      for (int i = 0; i < HELLO_DELAY; ++i) __builtin_amdgcn_s_sleep(8);
#pragma nounroll
      for (u32 spin = 0;; ++spin) {
        FENCE();
        u32 bad = 0;
#pragma unroll
        for (int i = 0; i < 4; ++i) bad |= __builtin_amdgcn_raw_buffer_load_b32(rws, WS_FLAG + 128 + (i * 64 + lane) * 256, 0, 16) ^ tag;
        if (__all(bad == 0u) || spin > SPIN_CAP) break;
        __builtin_amdgcn_s_sleep(16);
      }
      if (lane == 0) gst32(p.ws.p + WS_EPOCH, tag);
    }
    return;
  }
  lds_wait_ge<1>(Y_CB, 2);
  STAMP(8);
  {
    bf8 xb8[5];
#pragma unroll
    for (int ks = 0; ks < 5; ++ks) xb8[ks] = ld_b128(xb + ks * 64);
    SB();
    f4 ag = {0.f, 0.f, 0.f, 0.f}, au = {0.f, 0.f, 0.f, 0.f};
#pragma unroll
    for (int ks = 0; ks < 5; ++ks) { ag = mfma(Wg[ks], xb8[ks], ag); au = mfma(Wu[ks], xb8[ks], au); }
    SB();
    st_f4(ar, ag); st_f4(ar + 128, au);
  }
  STAMP(5);
  lds_bar();
  STAMP(6);
  {
    float a[4], b[4];
#pragma unroll
    for (int k = 0; k < 4; ++k) a[k] = ld_f(as + k * 512);
#pragma unroll
    for (int k = 0; k < 4; ++k) b[k] = ld_f(as + k * 512 + 128);
    bf8 Wd[5], hb[5];
#pragma unroll
    for (int i = 0; i < 5; ++i) Wd[i] = ld_tr8(ad[i], 512);
    SB();
    const float hh = rbf(silu_f(rbf((a[0] + a[1]) + (a[2] + a[3])))) * rbf((b[0] + b[1]) + (b[2] + b[3]));
    *(LAS u16*)(uintptr_t)aw = (u16)(pk2(hh, hh) & 0xffffu);
    FENCE();
#pragma unroll
    for (int i = 0; i < 5; ++i) hb[i] = ld_b128(ah[i]);
    SB();
    f4 y = {0.f, 0.f, 0.f, 0.f};
#pragma unroll
    for (int i = 0; i < 5; ++i) y = mfma(Wd[i], hb[i], y);
    u4 v; v.x = pk2(y[0], y[1]); v.z = pk2(y[2], y[3]); v.y = v.w = tag;
    bst128_sc1(rws, yo, v);
  }
  STAMP(4);
}

DEV void gemm2(const Params& p, rsrc_t rws, u32 tag, int wave, int ng, G2& c STM_ARG) {
  const int m = wave & 3, kh = wave >> 2;
  const u32 b0 = c.b0, oo = c.oo, fo = c.fo, ga = c.ga, one = c.one, pa = c.pa, pm = c.pm;
  u4 of[10];
  asm volatile("" : "={v[160:163]}"(of[0]));
#define POLL(R) "v_mov_b32 " R ", 0\n\tbuffer_atomic_add " R ", %[fo], %[srd], 0 offen sc0\n\t"
  GST(14);
  lds_wait_ge<1>(Y_WO, 4);
  asm volatile("s_cmp_eq_u32 %[pm], 0\n\ts_cbranch_scc1 1f\n\ts_mov_b64 exec, %[pm64]\n\t" POLL("v160") "s_mov_b64 exec, -1\n1:"
               : "+{v[160:163]}"(of[0]) : [fo] "v"(fo), [srd] "s"(srd(p.ws)), [pm] "s"(pm), [pm64] "s"((u64)pm) : "scc", "memory");
  bf8 W[WPRE][5];
  auto ldw = [&](int ks) {
#pragma unroll
    for (int n = 0; n < 5; ++n) W[ks][n] = ld_tr8(b0 + ks * 5120 + n * 32, 8 * 160);
  };
#pragma unroll
  for (int ks = 0; ks < WPR1; ++ks) ldw(ks);
  SB();
  GST(15);
  __builtin_amdgcn_s_setprio(IPRIO);
  {
    u32 n = SPIN_CAP, l, r;
#define CHK(R) "s_waitcnt vmcnt(2)\n\tv_cmp_ne_u32 vcc, %[tag], " R "\n\ts_cbranch_vccz 2f\n\t" POLL(R)
#define LSPIN(L, A, V) L ":\n\tds_read_b32 v163, " A "\n\ts_sub_u32 %1, %1, 1\n\ts_cselect_b32 %2, 0, " V "\n\ts_waitcnt lgkmcnt(0)\n\t" \
                 "v_readfirstlane_b32 %3, v163\n\ts_cmp_lt_u32 %3, %2\n\ts_cbranch_scc1 " L "b\n\t"
    asm volatile("s_lshl_b32 %1, %1, 5\n\ts_cmp_eq_u32 %[pm], 0\n\ts_cbranch_scc1 5f\n\t"
                 "s_mov_b64 exec, %[pm64]\n\t" POLL("v161") LSPIN("3", "%[pa]", "8") ".if %[gap]\n\ts_sleep %[gap]\n\t.endif\n\t" POLL("v162")
                 "s_movk_i32 %1, 0x4000\n"
                 "1:\n\t" CHK("v160") CHK("v161") CHK("v162") "s_sub_u32 %1, %1, 1\n\ts_cbranch_scc0 1b\n"
                 "2:\n\tds_write_b32 %[ga], %[one]\n\ts_mov_b64 exec, -1\n\ts_branch 4f\n"
                 LSPIN("5", "%[ga]", "1")
                 "4:\n\tbuffer_load_dwordx4 v[160:163], %[oo], %[srd], 0 offen sc1"
                 : "+{v[160:163]}"(of[0]), "+s"(n), "=&s"(l), "=&s"(r)
                 : [fo] "v"(fo), [srd] "s"(srd(p.ws)), [tag] "s"(tag), [ga] "v"(ga), [one] "v"(one), [pm] "s"(pm), [pm64] "s"((u64)pm), [gap] "i"(PGAP),
                   [pa] "v"(pa), [oo] "v"(oo)
                 : "scc", "vcc", "memory");
  }
  GST(0);
  STAMP(7);
#pragma unroll
  for (int ks = 0; ks < OPRE; ++ks) {
    if (ks) of[ks] = __builtin_amdgcn_raw_buffer_load_b128(rws, oo, ks * 1024, 16);
    SB();
    constexpr int NW = (WPRE - WPR1) * 5;
#pragma unroll
    for (int i = ks * NW / OPRE; i < (ks + 1) * NW / OPRE; ++i) W[WPR1 + i / 5][i % 5] = ld_tr8(b0 + (WPR1 + i / 5) * 5120 + (i % 5) * 32, 8 * 160);
    if (OSLP) __builtin_amdgcn_s_sleep(OSLP);
    SB();
  }
  __builtin_amdgcn_s_setprio(MPRIO);
  u32 sfa = Y_S + m * 4, sfv = kh + 1;
  SPIN(sfa); SPIN(sfv);
  asm volatile("s_mov_b32 m0, %0" :: "s"(wave * ST_W));
  asm volatile("s_waitcnt vmcnt(%1)" : "+{v[160:163]}"(of[0]) : "i"(OPRE - 1));
  SB();
  u32 fa = sfa, fv = sfv;
  GST(1);
  f4 acc[5];
#pragma unroll
  for (int n = 0; n < 5; ++n) acc[n] = f4{0.f, 0.f, 0.f, 0.f};
  constexpr int NR = (10 - WPRE) * 10;
  u2 Wh[NR];
  auto kn = [](int i, int& ks, int& n) { if (i < 40) { ks = i / 5; n = i % 5; } else { ks = 8 + (i & 1); n = (i - 40) >> 1; } };
  auto rdh = [&](int i) { int ks, n; kn(WPRE * 5 + (i >> 1), ks, n); if (i >= 0 && i < NR) Wh[i] = ld_tr(b0 + ks * 5120 + n * 32 + (i & 1) * 1280); };
  auto put = [&](int j) { asm volatile("ds_write_addtid_b32 %0 offset:%1" :: "v"(acc[j >> 2][j & 3]), "i"((j >> 2) * ST_N + (j & 3) * 256) : "memory"); };
#pragma unroll
  for (int i = 0; i < 50; ++i) {
    int ks, n; kn(i, ks, n);
    acc[n] = mfma(__builtin_bit_cast(bf8, of[ks]), i < WPRE * 5 ? W[ks][n] : mk8(Wh[2 * (i - WPRE * 5)], Wh[2 * (i - WPRE * 5) + 1]), acc[n]);
    const int j = i - RD0;
    rdh(j + j / RDX); if (j >= 0 && j % RDX == RDX - 1) rdh(j + j / RDX + 1);
    if (i == 41) PIN(fa);
    if (i == 42) { PIN(fv); PIN(Wh[NR - 1]); }
    SB();
    if (i >= 43) { put(2 * (i - 43)); put(2 * (i - 43) + 1); SB(); }
    if (!ARR && i % 5 == 4) GST(2 + i / 5);
  }
  put(14); put(15);
  asm volatile("s_nop 3");
#pragma unroll
  for (int j = 16; j < 20; ++j) put(j);
  lds_set(fa, fv);
  GST(12);
  lds_bar();
  GST(13);
}

DEV void epilogue(const Params& p, int lane, int m, int mg, int ng, bool is_main, Epi& ei STM_ARG) {
  const rsrc_t rws = __builtin_amdgcn_make_buffer_rsrc(p.ws.p, 0, WS_END, 0x00020000);
  const u32 tag = __builtin_amdgcn_readfirstlane(ei.sv.y);
  u4 fin = ei.fin;
  float (&gf)[20] = ei.gf;
  f2 xf[10], g2[10];
#pragma unroll
  for (int i = is_main ? 0 : YG; i < (is_main ? GSPLIT : YG2); ++i) ei.gate(i);
  if (__builtin_expect(!(fin.y == tag && fin.w == tag), 0)) {
#pragma nounroll
    for (u32 spin = 0; !(fin.y == tag && fin.w == tag) && spin < SPIN_CAP; ++spin) {
      FENCE();
      __builtin_amdgcn_s_sleep(2);
      fin = bld128_sc1(rws, WS_FIN);
    }
  }
  const u32 k1 = __builtin_amdgcn_readfirstlane(fin.x), k2 = __builtin_amdgcn_readfirstlane(fin.z);
  u32 tk[2], tl[2];
#pragma unroll
  for (int c = 0; c < 2; ++c) { tk[c] = key_tok(c ? k2 : k1); tl[c] = (((tk[c] & 1) << 6) | (tk[c] >> 5)) - mg * 4; }
  const u32 g = lane >> 4, i16 = lane & 15, pt = (i16 * 13) >> 6, qd = (i16 - pt * 5) * 4 + g;
  u32 yo = (WS_YP + (ng * 54 + m * 15 + pt) * 512 + g * 128 + (qd >> 2) * 16) | (pt == 3 ? 0x80000000u : 0u), xo = ng * 160 + qd * 8;
  u32 yo2 = m == 3 ? 0x80000000u : yo + 4608;
  PIN(yo); PIN(xo); PIN(yo2);
  u4 w[6]; u2 xr;
  u32 c = tl[0] > 3;
  u32 co, so_, ro, tc;
  auto sel = [&]() {
    const u32 t = c ? tk[1] : tk[0];
    co = c * (8 * 54 * 512); so_ = WS_ST2 + ((t & 1) * 1024 + (t >> 1)) * 16; ro = t * 1280; tc = c ? tl[1] : tl[0];
    SPIN(co); SPIN(so_); SPIN(ro); SPIN(tc);
  };
  auto fetch = [&]() {
    asm volatile("s_cmp_gt_u32 %[tl], 3\n\ts_cbranch_scc1 1f\n\t"
                 "buffer_load_dwordx4 %0, %[yo], %[ws], %[co] offen sc1\n\tbuffer_load_dwordx4 %1, %[yo], %[ws], %[co] offen offset:1536 sc1\n\t"
                 "buffer_load_dwordx4 %2, %[yo], %[ws], %[co] offen offset:3072 sc1\n\tbuffer_load_dwordx4 %3, %[y2], %[ws], %[co] offen sc1\n\t"
                 "buffer_load_dwordx4 %4, %[y2], %[ws], %[co] offen offset:1536 sc1\n\tbuffer_load_dwordx4 %5, off, %[ws], %[so] sc1\n\t"
                 "buffer_load_dwordx2 %6, %[xo], %[x], %[ro] offen\n1:"
                 : "=&v"(w[0]), "=&v"(w[1]), "=&v"(w[2]), "=&v"(w[3]), "=&v"(w[4]), "=&v"(w[5]), "=&v"(xr)
                 : [yo] "v"(yo), [y2] "v"(yo2), [xo] "v"(xo), [ws] "s"(srd(p.ws)), [x] "s"(srd(p.x)), [co] "s"(co), [so] "s"(so_), [ro] "s"(ro), [tl] "s"(tc) : "scc");
  };
  auto landed = [&]() { asm volatile("s_waitcnt vmcnt(0)" : "+v"(w[0]), "+v"(w[1]), "+v"(w[2]), "+v"(w[3]), "+v"(w[4]), "+v"(w[5]), "+v"(xr) :: "memory"); };
  sel();
  SB();
  wait_vm<0>();
  lds_add(Y_WO, 1);
  STAMP(5);
  fetch();
  SB();
  STAMP(6);
#pragma unroll
  for (int i = is_main ? GSPLIT : YG2; i < (is_main ? GS2 : 20); ++i) ei.gate(i);
  SB();
  landed();
#pragma nounroll
  for (; c < 2 && (c ? tl[1] : tl[0]) < 4; ++c, w[0].y = ~tag) {
    __builtin_amdgcn_s_setprio(3);
    const u32 ya = i16 < 5 ? L_Y + c * 1280 + qd * 16 : L_NONE + lane * 16;
    u32 e0 = pt == 3 ? 0u : tag, e1 = m == 3 ? 0u : e0, uLo = 0x3f80u, uHi = 0x3f800000u;
    PIN(uLo); PIN(uHi);
    auto bad = [&]() {
      u32 b = 0;
#pragma unroll
      for (int j = 0; j < 5; ++j) b |= __builtin_amdgcn_bitop3_b32(w[j].y, w[j].w, j < 3 ? e0 : e1, 0x7e);
      PIN(b);
      return __any(b != 0u);
    };
    if (__builtin_expect(bad(), 0)) {
#pragma nounroll
      for (u32 spin = 0; spin < SPIN_CAP; ++spin) {
        __builtin_amdgcn_s_sleep(4);
        sel(); fetch(); landed();
        if (!bad()) break;
      }
    }
    f4 y = {0.f, 0.f, 0.f, 0.f}, h;
#pragma unroll
    for (int j = 0; j < 5; ++j) { y[0] = dot2(w[j].x, uLo, y[0]); y[1] = dot2(w[j].x, uHi, y[1]); y[2] = dot2(w[j].z, uLo, y[2]); y[3] = dot2(w[j].z, uHi, y[3]); }
#pragma unroll
    for (int e = 0; e < 4; ++e) { float a = y[e] + dppf<0x105>(y[e]); PIN(a); h[e] = a; }
#pragma unroll
    for (int e = 0; e < 4; ++e) { float a = h[e] + dppf<0x10a>(y[e]); PIN(a); y[e] = a; }
    st_f4(ya + m * 320, y);
    lds_add(Y_Y + c * 4, 1);
    if ((c ? tl[1] : tl[0]) != m) continue;
    float sg = __builtin_amdgcn_rcpf(1.f + __expf(-key_r(c ? k2 : k1)));
    const float mu = __uint_as_float(w[5].x), rs = __uint_as_float(w[5].z);
    float xn[4] = {bflo(xr.x), bfhi(xr.x), bflo(xr.y), bfhi(xr.y)};
#define EACH(V, X) _Pragma("unroll") for (int e = 0; e < 4; ++e) { V[e] = X; PIN(V[e]); }
    EACH(xn, xn[e] - mu) EACH(xn, xn[e] * rs)
    PIN(sg);
    lds_wait_ge<0>(Y_Y + c * 4, 4);
    f4 z[4];
#pragma unroll
    for (int j = 0; j < 4; ++j) z[j] = ld_f4(ya + j * 320);
    float a[4], b[4];
    EACH(a, z[0][e] + z[1][e]) EACH(b, z[2][e] + z[3][e]) EACH(a, a[e] + b[e]) EACH(a, fmaf(a[e], sg, xn[e]))
    EACH(b, a[e] * -1.4426950408889634f) EACH(b, __builtin_amdgcn_exp2f(b[e])) EACH(b, 1.f + b[e]) EACH(b, __builtin_amdgcn_rcpf(b[e])) EACH(a, a[e] * b[e])
    y = f4{a[0], a[1], a[2], a[3]};
    st_f4(ya, y);
  }
  STAMP(10);
#pragma unroll
  for (int i = is_main ? GS2 : 20; i < 20; ++i) ei.gate(i);
  const u32 row = Epi::row(lane), odd = ng & 1;
  u32 sadr[2];
  sadr[1] = m * ST_W + (row & 3) * 256 + (row >> 2) * 64;
  sadr[0] = sadr[1] + (odd + ((lane >> 1) & 1)) * ST_N + (lane & 1) * 32;
  sadr[1] += (odd ? 0 : 4 * ST_N) + (lane & 3) * 16;
  PIN(sadr[0]); PIN(sadr[1]);
  SB();
#pragma nounroll
  for (c = 0; c < 2; ++c) {
    if ((c ? tl[1] : tl[0]) != m) continue;
    FENCE();
    const u32 ga = L_Y + c * 1280 + odd * 64 + (lane & 3) * 32, hit = row == (((c ? tk[1] : tk[0]) >> 1) & 15) ? ~0u : 0u;
#pragma unroll
    for (int j = 0; j < 5; ++j) {
      const f4 gr = ld_f4(j < 4 ? ga + (j >> 1) * 128 + (j & 1) * 16 : L_Y + c * 1280 + (odd ? 0 : 256) + (lane & 3) * 16);
#pragma unroll
      for (int e = 0; e < 4; ++e) gf[4 * j + e] = fsel(hit, gr[e], gf[4 * j + e]);
    }
  }
#pragma unroll
  for (int e = 0; e < 10; ++e) { xf[e] = f2{bflo(ei.xw(e)), bfhi(ei.xw(e))}; g2[e] = f2{gf[2 * e], gf[2 * e + 1]}; PIN(xf[e]); PIN(g2[e]); }
  STAMP(7);
  __builtin_amdgcn_s_setprio(LPRIO);
  u4 so = srd(p.out);
  u32 fa = Y_S + m * 4, first;
  SPIN(so); PIN(fa);
  GST(1);
  {
    u32 n = 1u << 20, t, l;
    asm volatile("1:\n\ts_sleep 2\n\tds_read_b32 %1, %4\n\ts_sub_u32 %0, %0, 1\n\ts_cselect_b32 %2, 0, 1\n\ts_waitcnt lgkmcnt(0)\n\t"
                 "v_readfirstlane_b32 %3, %1\n\ts_cmp_lt_u32 %3, %2\n\ts_cbranch_scc1 1b"
                 : "+s"(n), "=&v"(t), "=&s"(l), "=&s"(first) : "v"(fa) : "scc", "memory");
  }
  GST(2);
  f4 pa[5];
  auto rd = [&](u32 a0, u32 a1) {
#pragma unroll
    for (int j = 0; j < 5; ++j) pa[j] = ld_f4(j < 4 ? a0 + (j >> 1) * 2 * ST_N + (j & 1) * 16 : a1);
  };
  u32 o0 = (first - 1) * (4 * ST_W), o1 = 4 * ST_W - o0;
  SPIN(o0); SPIN(o1);
  rd(sadr[0] + o0, sadr[1] + o0);
  SB();
  sadr[0] += o1; sadr[1] += o1;
  PIN(sadr[0]); PIN(sadr[1]);
  SB();
#pragma unroll
  for (int e = 0; e < 20; ++e) {
    float t = xf[e >> 1][e & 1];
    asm volatile("v_fmac_f32 %0, %1, %2" : "+v"(t) : "v"(pa[e >> 2][e & 3]), "v"(g2[e >> 1][e & 1]));
    xf[e >> 1][e & 1] = t;
  }
  GST(3);
  __builtin_amdgcn_s_setprio(3);
  bar();
  GST(4);
  rd(sadr[0], sadr[1]);
  SB();
  auto fin2 = [&](int e) {
    const f2 r = __builtin_elementwise_fma(f2{pa[e >> 1][2 * (e & 1)], pa[e >> 1][2 * (e & 1) + 1]}, g2[e], xf[e]);
    return pk2(r.x, r.y);
  };
  u4 o[2]; u2 oh;
#pragma unroll
  for (int ps = 0; ps < 2; ++ps) {
#pragma unroll
    for (int e = 0; e < 4; ++e) o[ps][e] = fin2(4 * ps + e);
    asm volatile("buffer_store_dwordx4 %0, %1, %2, 0 offen offset:%3 " OUTPOL :: "v"(o[ps]), "v"(ei.goff[0]), "s"(so), "i"(ps * 64) : "memory");
    SB();
  }
  oh.x = fin2(8); oh.y = fin2(9);
  asm volatile("buffer_store_dwordx2 %0, %1, %2, 0 offen " OUTPOL :: "v"(oh), "v"(ei.goff[1]), "s"(so), "v"(o[0]), "v"(o[1]) : "memory");
  GST(5);
  STAMP(8);
}

#define NTW 2
#define NTX 2
#define PUT_STAMPS(m) do {} while (0)
#define STR_(x) #x
#define STR(x) STR_(x)
#define ROLE extern "C" __global__ __launch_bounds__(768, 3) void
#define PAGE __attribute__((aligned(4096)))
#define STAMP0 ""
#define STAMP_INIT() do {} while (0)

#define ROWS3(W, X0, X1) \
    "buffer_load_dwordx4 " W ", v3, s[8:11], s15 offen\n\tbuffer_load_dwordx4 " X0 ", v4, s[4:7], s15 offen\n\t" \
    "buffer_load_dwordx4 " X1 ", v5, s[4:7], s15 offen\n\ts_addk_i32 s15, 256\n\ts_cmpk_eq_u32 s15, 1280\n\ts_cselect_b32 s15, 0, s15\n\t"
ROLE PAGE fused_kernel(Params p) {
  asm volatile(
    STAMP0
    "s_load_dword s94, s[0:1], 0\n\t"
    "s_load_dword s95, s[0:1], 64\n\t"
    "v_readfirstlane_b32 s3, v0\n\t"
    "s_cmpk_ge_u32 s2, 200\n\t"
    "s_cbranch_scc1 3f\n\t"
    "s_cmpk_lt_u32 s3, 256\n\t"
    "s_cbranch_scc1 role_c4\n\t"
    "s_cmpk_lt_u32 s3, 512\n\t"
    "s_cbranch_scc1 role_c3\n\t"
    "s_setprio 3\n\t"
    "s_load_dwordx8 s[4:11], s[0:1], 0\n\t"
    "s_bfe_u32 s12, s3, 0x20006\n\t"
    "s_lshr_b32 s13, s2, 3\n\t"
    "s_mul_i32 s14, s13, 13\n\t"
    "s_lshr_b32 s14, s14, 6\n\t"
    "s_mul_i32 s15, s14, 5\n\t"
    "s_sub_u32 s13, s13, s15\n\t"
    "s_add_u32 s15, s13, s14\n\t"
    "s_sub_u32 s16, s15, 5\n\t"
    "s_min_u32 s15, s15, s16\n\t"
    "s_bitcmp1_b32 s15, 1\n\t"
    "s_cselect_b64 vcc, -1, 0\n\t"
    "s_and_b32 s16, s15, 1\n\t"
    "s_lshl_b32 s17, s2, 1\n\t"
    "s_and_b32 s17, s17, 12\n\t"
    "s_lshr_b32 s17, " STR(KOFF) ", s17\n\t"
    "s_and_b32 s17, s17, 6\n\t"
    "s_add_u32 s16, s16, s17\n\t"
    "s_bitcmp1_b32 s2, 1\n\t"
    "s_cselect_b32 s17, 5, 0\n\t"
    "s_add_u32 s14, s14, s17\n\t"
    "s_bitcmp1_b32 s2, 0\n\t"
    "s_cselect_b32 s17, 5, 0\n\t"
    "s_add_u32 s13, s13, s17\n\t"
    "s_mul_i32 s18, s14, 13\n\t"
    "s_add_u32 s18, s18, s12\n\t"
    "s_sub_u32 s18, s18, 1\n\t"
    "s_mulk_i32 s18, 20480\n\t"
    "s_add_u32 s19, s18, 81920\n\t"
    "s_add_u32 s20, s18, 163840\n\t"
    "s_add_u32 s21, s18, 245760\n\t"
    "s_min_u32 s21, s21, 2600960\n\t"
    "s_max_i32 s18, s18, 0\n\t"
    "s_cmp_lt_u32 s12, 2\n\t"
    "s_cselect_b64 s[22:23], -1, 0\n\t"
    "s_mov_b64 s[24:25], -1\n\t"
    "s_cmp_eq_u32 s12, 0\n\t"
    "s_cmov_b32 s24, 0xffffff00\n\t"
    "s_lshl_b32 s26, s12, 10\n\t"
    "s_add_u32 s26, s26, 116736\n\t"
    "s_mov_b32 s27, 122880\n\t"
    "s_bfe_u32 s17, s2, 0x10002\n\t"
    "s_mul_i32 s17, s17, 10\n\t"
    "s_add_u32 s17, s17, s16\n\t"
    "s_lshl3_add_u32 s28, s16, s12\n\t"
    "s_mulk_i32 s28, 240\n\t"
    "s_add_u32 s28, s28, s13\n\t"
    "s_lshl_b32 s29, s12, 2\n\t"
    "v_bfe_u32 v3, v0, 3, 3\n\t"
    "v_xor_b32 v4, s29, v0\n\t"
    "v_lshrrev_b32 v5, 1, v3\n\t"
    "v_bitop3_b32 v5, v4, v5, 7 bitop3:0x28\n\t"
    "v_and_b32 v6, 2, v3\n\t"
    "v_bitop3_b32 v6, v4, v6, 7 bitop3:0x28\n\t"
    "v_mad_u32_u24 v1, v3, 20, s17\n\t"
    "v_mad_u32_u24 v2, v3, 30, s28\n\t"
    "v_lshl_or_b32 v1, v1, 3, v5\n\t"
    "v_lshl_or_b32 v2, v2, 3, v6\n\t"
    "v_lshlrev_b32 v1, 4, v1\n\t"
    "v_lshlrev_b32 v2, 4, v2\n\t"
    "s_waitcnt lgkmcnt(0)\n\t"
    "s_cbranch_vccnz 5f\n"
    "4:\n\t"
    HALF_A("s26", "v1", "v2", "s[4:7]", "s[8:11]", "s18", "s21", "s[24:25]", "s[22:23]")
    "s_cbranch_vccnz role_ml\n"
    "5:\n\t"
    HALF_B("s26", "v1", "v2", "s[4:7]", "s[8:11]", "s19", "s20", "s27")
    "s_cbranch_vccnz 4b\n\t"
    "s_branch role_ml\n"
    "3:\n\t"
    "s_cmpk_ge_u32 s3, 512\n\t"
    "s_cbranch_scc1 role_al\n\t"
    "s_load_dwordx4 s[4:7], s[0:1], 0\n\t"
    "s_load_dwordx4 s[8:11], s[0:1], 64\n\t"
    "s_load_dwordx2 s[16:17], s[0:1], 32\n\t"
    "s_getpc_b64 s[12:13]\n"
    "2:\n\t"
    "s_lshr_b32 s3, s3, 6\n\t"
    "s_and_b32 s14, s2, -8\n\t"
    "s_add_u32 s14, s14, s3\n\t"
    "s_addk_i32 s14, -200\n\t"
    "s_mulk_i32 s14, 1280\n\t"
    "v_and_b32 v1, 63, v0\n\t"
    "v_min_u32 v2, 9, v1\n\t"
    "v_lshl_add_u32 v2, v2, 7, s14\n\t"
    "v_min_u32 v2, code_end - 2b, v2\n\t"
    "global_load_dword v167, v2, s[12:13]\n\t"
    "s_mul_i32 s3, s3, 7\n\t"
    "s_lshr_b32 s14, s2, 3\n\t"
    "s_add_u32 s3, s3, s14\n\t"
    "s_sub_u32 s3, s3, 25\n\t"
    "s_and_b32 s14, s2, 3\n\t"
    "s_lshl_b32 s14, s14, 5\n\t"
    "s_add_u32 s14, s14, s3\n\t"
    "s_bfe_u32 s15, s2, 0x10002\n\t"
    "s_lshl4_add_u32 s14, s14, s15\n\t"
    "s_mulk_i32 s14, 1280\n\t"
    "s_lshl_b32 s15, s2, 1\n\t"
    "s_and_b32 s15, s15, 12\n\t"
    "s_lshr_b32 s15, " STR(KOFF) ", s15\n\t"
    "s_and_b32 s15, s15, 6\n\t"
    "s_lshl_b32 s15, s15, 7\n\t"
    "s_addk_i32 s15, 256\n\t"
    "v_and_b32 v2, 3, v0\n\t"
    "v_and_b32 v3, 48, v0\n\t"
    "v_lshlrev_b32 v3, 2, v3\n\t"
    "v_lshl_or_b32 v3, v2, 4, v3\n\t"
    "v_bfe_u32 v2, v0, 2, 2\n\t"
    "v_mul_u32_u24 v2, 2560, v2\n\t"
    "v_add3_u32 v4, v2, v3, s14\n\t"
    "v_add_u32 v5, 10240, v4\n\t"
    "s_cmp_ge_u32 s3, 32\n\t"
    "s_waitcnt lgkmcnt(0)\n\t"
    "s_load_dword s94, s[16:17], 0\n\t"
    "s_cbranch_scc1 role_ac\n\t"
    ROWS3("v[100:103]", "v[104:107]", "v[108:111]") ROWS3("v[112:115]", "v[116:119]", "v[120:123]") ROWS3("v[124:127]", "v[128:131]", "v[132:135]")
    ROWS3("v[136:139]", "v[140:143]", "v[144:147]") ROWS3("v[148:151]", "v[152:155]", "v[156:159]")
    "s_branch role_ac"
    :: "s"(__builtin_amdgcn_kernarg_segment_ptr()), "s"(__builtin_amdgcn_workgroup_id_x()), "v"(__builtin_amdgcn_workitem_id_x())
    : "v167", "s99", "vcc", "scc", "memory");
}

struct Ids {
  int lane, wave, xcd, r, mg, ng, ra, rb, rt, j, a7, f, koff;
  DEV Ids() {
    lane = threadIdx.x & 63; wave = __builtin_amdgcn_readfirstlane(threadIdx.x >> 6);
    const int b = blockIdx.x;
    xcd = b & 7; r = b >> 3;
    mg = xcd * 4 + (r >> 3); ng = r & 7;
    ra = (r * 13) >> 6; rb = r - 5 * ra;
    rt = (xcd >> 1) * 5 + ra; j = (xcd & 1) * 5 + rb;
    a7 = r - 25;
    f = a7 < 6 ? 2 * (xcd + 8 * (a7 >> 1)) + (a7 & 1) : 48 + ((FPERM >> (4 * xcd)) & 7);
    koff = (KOFF >> ((xcd >> 1) * 4)) & 6;
  }
};

template <int NS>
DEV void main_wave(Params& p) {
  STAMP_INIT();
  const Ids id;
  const int lane = id.lane, wave = id.wave;
  const rsrc_t rws = __builtin_amdgcn_make_buffer_rsrc(p.ws.p, 0, WS_END, 0x00020000);
  u32 tag;
  G2 g2;
  main_compute<NS>(p, rws, tag, lane, wave, ((id.xcd >> 1) & 1) * 5 + id.ra, id.xcd >> 2, id.rb, id.j, id.mg, id.ng, g2 STM);
  __builtin_amdgcn_s_setprio(0);
  gemm2(p, rws, tag, wave, id.ng, g2 STM);
  PUT_STAMPS(true);
}
ROLE role_ml(Params p) {
  LdIn in; in.take(); SB();
  STAMP_INIT();
  const Ids id;
  const int lane = id.lane, wave = id.wave;
  Epi ei;
  main_loader(p, in, lane, id.xcd >> 2, id.mg, id.ng, id.koff, ei STM);
  epilogue(p, lane, in.lw, id.mg, id.ng, true, ei STM);
  PUT_STAMPS(true);
}
ROLE PAGE role_c3(Params p) { main_wave<3>(p); }
ROLE role_c4(Params p) { main_wave<4>(p); }
ROLE PAGE role_al(Params p) {
  STAMP_INIT();
  const Ids id;
  const int lane = id.lane, wave = id.wave, lw = wave - 8;
  Epi ei;
  aux_loader(p, lane, lw, id.a7, id.f, id.f < 54, id.mg, id.ng, id.koff, ei STM);
  epilogue(p, lane, lw, id.mg, id.ng, false, ei STM);
  PUT_STAMPS(false);
}
ROLE PAGE __attribute__((amdgpu_num_vgpr(160))) role_ac(Params p) {
  RowIn rin; rin.take(); SB();
  STAMP_INIT();
  const Ids id;
  const int lane = id.lane, wave = id.wave;
  const rsrc_t rws = __builtin_amdgcn_make_buffer_rsrc(p.ws.p, 0, WS_END, 0x00020000);
  u32 tag;
  aux_compute(p, rws, tag, lane, wave, id.f, id.f < 54, id.xcd, id.a7, rin STM);
  lds_add(Y_PUB, 1);
  G2 g2;
  g2.init(wave, id.mg, id.ng);
  gemm2(p, rws, tag, wave, id.ng, g2 STM);
  PUT_STAMPS(false);
}
ROLE code_end(Params p) {}

extern "C" void kernel_launch(void* const* d_in, const int* in_sizes, int n_in,
                              void* d_out, int out_size, void* d_ws, size_t ws_size,
                              hipStream_t stream) {
  auto buf = [](void* q, u32 bytes) { Buf r; r.p = (char*)q; r.bytes = bytes; r.flags = 0x00020000u; return r; };
  Params p;
  p.x = buf(d_in[0], S_ * D_ * 2); p.wqkv = buf(d_in[1], D_ * 3 * D_ * 2); p.wo = buf(d_in[2], D_ * D_ * 2); p.wr = buf(d_in[4], D_ * 2);
  p.wg = buf(d_in[5], D_ * F_ * 2); p.wu = buf(d_in[6], D_ * F_ * 2); p.wd = buf(d_in[7], F_ * D_ * 2);
  p.out = buf(d_out, S_ * D_ * 2); p.ws = buf(d_ws, WS_END);
  (void)hipFuncSetAttribute((const void*)fused_kernel, hipFuncAttributeMaxDynamicSharedMemorySize, LDS_BYTES);
  fused_kernel<<<256, 768, LDS_BYTES, stream>>>(p);
}
```

```cpp
#include <hip/hip_runtime.h>
#include <stdint.h>

typedef unsigned short u16;
typedef unsigned int u32;
typedef unsigned long long u64;
typedef __attribute__((ext_vector_type(4))) float f4;
typedef __attribute__((ext_vector_type(2))) float f2;
typedef __attribute__((ext_vector_type(4))) unsigned u4;
typedef __attribute__((ext_vector_type(2))) unsigned u2;
typedef __attribute__((ext_vector_type(8))) __bf16 bf8;
typedef __attribute__((ext_vector_type(2))) __bf16 bf2;
typedef __attribute__((ext_vector_type(4))) short s4;
#define LAS __attribute__((address_space(3)))
#define GAS __attribute__((address_space(1)))
#define DEV __device__ __forceinline__

constexpr int S_ = 2048, D_ = 640, F_ = 1728;

constexpr u32 WS_EPOCH = 0;
constexpr u32 WS_FIN = 256;
constexpr u32 WS_KEY = 24576;
constexpr u32 WS_YP = 8u << 20;
constexpr u32 WS_ST2 = 448u << 10;
constexpr u32 WS_FLAG = 512u << 10;
constexpr u32 WS_BOX = 640u << 10;
constexpr u32 WS_O = 1u << 20;
constexpr u32 WS_XS = 4u << 20;
constexpr u32 WS_END = WS_YP + 16 * 54 * 512;
constexpr u32 WS_STAMP = 16u << 20;

constexpr u32 SLOT = 38912;
constexpr u32 XB = 0, WB = 14336;
constexpr u32 MISC = 4 * SLOT;
constexpr u32 L_CS = MISC;
constexpr u32 L_SYNC = MISC + 768;
constexpr u32 L_MR = MISC + 1024;
constexpr u32 L_DUMP = MISC + 2048;
constexpr u32 L_Y = MISC + 3072;
constexpr u32 L_HV = MISC + 6144;
constexpr u32 LDS_BYTES = 163840;
constexpr u32 Y_Y = L_SYNC;
constexpr u32 Y_PUB = L_SYNC + 8;
constexpr u32 Y_WO = L_SYNC + 12;
constexpr u32 Y_CB = L_SYNC + 16;
constexpr u32 Y_S = L_SYNC + 128;
constexpr u32 Y_GO = L_SYNC + 48;
constexpr u32 Y_F = L_SYNC + 64;
constexpr u32 L_NONE = 0x30000;
constexpr u32 L_X = SLOT;
constexpr u32 L_HK = L_X + 21 * 512;
constexpr u32 L_WO = MISC - 103680;
constexpr u32 ST_N = 1040, ST_W = 5 * ST_N;
static_assert(L_HK + 2048 <= L_WO && 8 * ST_W <= L_WO, "lds map");

#define KD 44
constexpr int KEY_DELAY = KD;
constexpr int HELLO_DELAY = 4;
constexpr int POLL_SLEEP = 1;
#define WOA_ 14
#define WO7_ 5
constexpr int WO7 = WO7_, WOA = WOA_;
#define GSP 5
constexpr int GSPLIT = GSP;
#define OPRE_ 10
#define WPRE 6
#define RD0 5
#define RDX 6
#define STL 5
#define EBAR 1
#define WPR1 6
#define PGAP 1
#define LPRIO 2
#define IPRIO 2
#define BOXW 2
#define MPRIO 0
#define RDLY 10
#define KSLP 6
#define AWO 1
#define AWT 63
#define AW0 63
#define YG2 12
#define DLATE 0
#define GS2 14
#define NFD 40
#define PBR2 1
#define OSLP 0
#define ARR 0
constexpr int OPRE = OPRE_;
#define PADC4 "1"
#define NOTOUCH
#define KOFF 0x6420
#define TX0 10
#define INIT1 1
#define TH0 3
#define TH1 3
#define FFN_DELAY 12
#define WOC 12
#define YG 0
#define SSLP 1
#define FPERM 0x54362107
#define OUTP 0
#define OUTPOL ""
#define PBRK 1
#define WOST 0
#define WOSTA 0
#define WTH 63
#define WTH2 63
#define PACE 0
#define PACE0 1
#define MORD 0
#define REVM 0xc
#define KORD 12
constexpr u64 KT0 = 0x9786453120, KT1 = 0x8975642301, KT2 = KT0, KT3 = KT1, KT4 = 0x9867534210;
#define SCAP 15
constexpr u32 SPIN_CAP = 1u << SCAP;

struct Buf { char* p; u32 bytes, flags; };
struct Params { Buf x, wqkv, ws, wo, wr, out, wg, wu, wd; };

DEV float bflo(u32 v) { return __uint_as_float(v << 16); }
DEV float bfhi(u32 v) { return __uint_as_float(v & 0xffff0000u); }
DEV u32 pk2(float a, float b) {
  bf2 r; r[0] = (__bf16)a; r[1] = (__bf16)b;
  return __builtin_bit_cast(u32, r);
}
DEV float rbf(float a) { return bflo(pk2(a, a) & 0xffffu); }
DEV u4 srd(const Buf& b) { return __builtin_bit_cast(u4, b); }
template <int N> DEV void wait_vm() { asm volatile("s_waitcnt vmcnt(%0)" :: "i"(N) : "memory"); }
template <int N> DEV void wait_bar() { asm volatile("s_waitcnt vmcnt(%0) lgkmcnt(0)\n\ts_barrier" :: "i"(N) : "memory"); }
DEV void lds_bar() { asm volatile("s_waitcnt lgkmcnt(0)\n\ts_barrier" ::: "memory"); }

DEV bf8 ld_b128(u32 a) { return *(const LAS bf8*)(uintptr_t)a; }
DEV u2 ld_u2(u32 a) { return *(const LAS u2*)(uintptr_t)a; }
DEV f4 ld_f4(u32 a) { return *(const LAS f4*)(uintptr_t)a; }
DEV float ld_f(u32 a) { return *(const LAS float*)(uintptr_t)a; }
DEV u32 ld_u(u32 a) { return *(const LAS u32*)(uintptr_t)a; }
DEV void st_u4(u32 a, u4 v) { *(LAS u4*)(uintptr_t)a = v; }
DEV void st_u2(u32 a, u2 v) { *(LAS u2*)(uintptr_t)a = v; }
DEV void st_f4(u32 a, f4 v) { *(LAS f4*)(uintptr_t)a = v; }
DEV void st_f(u32 a, float v) { *(LAS float*)(uintptr_t)a = v; }
DEV void st_u(u32 a, u32 v) { *(LAS u32*)(uintptr_t)a = v; }
DEV u2 ld_tr(u32 a) {
  return __builtin_bit_cast(u2, __builtin_amdgcn_ds_read_tr16_b64_v4i16((LAS s4*)(uintptr_t)a));
}
DEV bf8 mk8(u2 a, u2 b) { u4 r; r.x = a.x; r.y = a.y; r.z = b.x; r.w = b.y; return __builtin_bit_cast(bf8, r); }
DEV bf8 ld_tr8(u32 a, u32 d) { return mk8(ld_tr(a), ld_tr(a + d)); }
DEV f4 mfma(bf8 a, bf8 b, f4 c) { return __builtin_amdgcn_mfma_f32_16x16x32_bf16(a, b, c, 0, 0, 0); }
DEV f4 mfma16(u2 a, u2 b, f4 c) { return __builtin_amdgcn_mfma_f32_16x16x16bf16_1k(__builtin_bit_cast(s4, a), __builtin_bit_cast(s4, b), c, 0, 0, 0); }

#define PIN(x) asm volatile("" : "+v"(x))
#define SB() __builtin_amdgcn_sched_barrier(0)
#define RLX __ATOMIC_RELAXED, __HIP_MEMORY_SCOPE_AGENT
DEV u32 gld32(const char* p) { return __hip_atomic_load((const GAS u32*)p, RLX); }
DEV void gst64(char* p, u64 v) { __hip_atomic_store((GAS u64*)p, v, RLX); }
DEV void gst32(char* p, u32 v) { __hip_atomic_store((GAS u32*)p, v, RLX); }
typedef __amdgpu_buffer_rsrc_t rsrc_t;
DEV u4 bld128_sc1(rsrc_t r, u32 off) { return __builtin_amdgcn_raw_buffer_load_b128(r, off, 0, 16); }
DEV void bst128_sc1(rsrc_t r, u32 off, u4 v) { __builtin_amdgcn_raw_buffer_store_b128(v, r, off, 0, 16); }

#define STAMP(i) do {} while (0)
#define CSTAMP(i) do {} while (0)
#define FST(i) do {} while (0)
#define GST(i) do {} while (0)
#define KST(i) do {} while (0)
#define PST(i) do {} while (0)
#define STM_ARG
#define STM

template <int CTRL> DEV float dppf(float v) {
  return __uint_as_float(__builtin_amdgcn_update_dpp(0u, __float_as_uint(v), CTRL, 0xf, 0xf, true));
}
template <int CTRL> DEV u32 dppu(u32 v) { return __builtin_amdgcn_update_dpp(0u, v, CTRL, 0xf, 0xf, true); }
DEV float row_sum16(float v) {
  v += dppf<0xB1>(v);
  v += dppf<0x4E>(v);
  v += dppf<0x141>(v);
  v += dppf<0x140>(v);
  return v;
}
DEV void top2_merge(u32& a1, u32& a2, u32 b1, u32 b2) {
  u32 lo = min(a1, b1);
  a1 = max(a1, b1);
  a2 = max(lo, max(a2, b2));
}
DEV void top2_wave(u32& k1, u32& k2) {
  top2_merge(k1, k2, dppu<0xB1>(k1), dppu<0xB1>(k2));
  top2_merge(k1, k2, dppu<0x4E>(k1), dppu<0x4E>(k2));
  top2_merge(k1, k2, dppu<0x124>(k1), dppu<0x124>(k2));
  top2_merge(k1, k2, dppu<0x128>(k1), dppu<0x128>(k2));
  {
    auto r1 = __builtin_amdgcn_permlane16_swap(k1, k1, false, false);
    auto r2 = __builtin_amdgcn_permlane16_swap(k2, k2, false, false);
    k1 = r1[0]; k2 = r2[0]; top2_merge(k1, k2, r1[1], r2[1]);
  }
  {
    auto r1 = __builtin_amdgcn_permlane32_swap(k1, k1, false, false);
    auto r2 = __builtin_amdgcn_permlane32_swap(k2, k2, false, false);
    k1 = r1[0]; k2 = r2[0]; top2_merge(k1, k2, r1[1], r2[1]);
  }
}
DEV u32 mk_tag(u32 ep) {
  u32 tag = ep * 0x9E3779B1u + 0x7F4A7C15u;
  tag ^= tag >> 15; tag *= 0x85EBCA77u; tag ^= tag >> 13;
  return tag + (tag == ep);
}
DEV u32 epoch_get(const Params& p) { u32 e; asm volatile("s_load_dword %0, %1, 0" : "=s"(e) : "s"(p.ws.p)); return e; }
DEV u32 epoch_tag(u32 e) { asm volatile("s_waitcnt lgkmcnt(0)" : "+s"(e)); return mk_tag(e); }

DEV float silu_f(float v) { return v * __builtin_amdgcn_rcpf(1.f + __expf(-v)); }

DEV u32 key_tok(u32 k) { return ~k & 2047u; }
DEV float key_r(u32 k) {
  u32 o = k >> 16;
  u32 b = (o & 0x8000u) ? (o & 0x7fffu) : (~o & 0xffffu);
  return bflo(b);
}
DEV float vmax(float a, float b) { return __builtin_elementwise_maximum(a, b); }
DEV void bar() { asm volatile("s_barrier" ::: "memory"); }
#define NEW_PAGE() asm volatile("s_branch 1f\n\t.p2align 12\n1:" ::: "memory")
#define FENCE() asm volatile("" ::: "memory")
DEV u32 lds_peek(u32 a) { return __hip_atomic_load((LAS u32*)(uintptr_t)a, __ATOMIC_RELAXED, __HIP_MEMORY_SCOPE_WORKGROUP); }
DEV void lds_add(u32 a, u32 v) {
  asm volatile("s_mov_b64 exec, 1\n\tds_add_u32 %0, %1\n\ts_mov_b64 exec, -1" :: "v"(a), "v"(v) : "memory");
}
DEV void lds_set(u32 a, u32 v) {
  asm volatile("s_mov_b64 exec, 1\n\tds_write_b32 %0, %1\n\ts_mov_b64 exec, -1" :: "v"(a), "v"(v) : "memory");
}
template <int SLP> DEV void lds_wait_ge(u32 a, u32 v) {
  u32 n = 1u << 20, t, l, r;
  asm volatile(".if %6\n\ts_branch 2f\n\t.endif\n1:\n\t.if %6\n\ts_sleep %6\n\t.endif\n2:\n\t"
               "ds_read_b32 %1, %4\n\ts_sub_u32 %0, %0, 1\n\ts_cselect_b32 %2, 0, %5\n\ts_waitcnt lgkmcnt(0)\n\t"
               "v_readfirstlane_b32 %3, %1\n\ts_cmp_lt_u32 %3, %2\n\ts_cbranch_scc1 1b"
               : "+s"(n), "=&v"(t), "=&s"(l), "=&s"(r) : "v"(a), "s"(v), "i"(SLP) : "scc", "memory");
}
DEV u32 norm2(u32 x, float m, float rs) {
  const u32 d = pk2(bflo(x) - m, bfhi(x) - m);
  return pk2(bflo(d) * rs, bfhi(d) * rs);
}

DEV float dot2(u32 a, u32 b, float c) { return __builtin_amdgcn_fdot2_f32_bf16(__builtin_bit_cast(bf2, a), __builtin_bit_cast(bf2, b), c, false); }
DEV float fsel(u32 m, float a, float b) {
  return __uint_as_float(__builtin_amdgcn_bitop3_b32(m, __float_as_uint(a), __float_as_uint(b), 0xCA));
}
struct RowIn {
  u4 x[2][5], w[5];
  DEV void take() {
    asm volatile("" : "={v[100:103]}"(w[0]), "={v[104:107]}"(x[0][0]), "={v[108:111]}"(x[1][0]), "={v[112:115]}"(w[1]), "={v[116:119]}"(x[0][1]), "={v[120:123]}"(x[1][1]),
                 "={v[124:127]}"(w[2]), "={v[128:131]}"(x[0][2]), "={v[132:135]}"(x[1][2]), "={v[136:139]}"(w[3]), "={v[140:143]}"(x[0][3]), "={v[144:147]}"(x[1][3]),
                 "={v[148:151]}"(w[4]), "={v[152:155]}"(x[0][4]), "={v[156:159]}"(x[1][4]));
  }
  DEV void wait(int i) {
    if (i == 0) asm volatile("s_waitcnt vmcnt(12)" : "+{v[100:103]}"(w[0]), "+{v[104:107]}"(x[0][0]), "+{v[108:111]}"(x[1][0]));
    if (i == 1) asm volatile("s_waitcnt vmcnt(9)" : "+{v[112:115]}"(w[1]), "+{v[116:119]}"(x[0][1]), "+{v[120:123]}"(x[1][1]));
    if (i == 2) asm volatile("s_waitcnt vmcnt(6)" : "+{v[124:127]}"(w[2]), "+{v[128:131]}"(x[0][2]), "+{v[132:135]}"(x[1][2]));
    if (i == 3) asm volatile("s_waitcnt vmcnt(3)" : "+{v[136:139]}"(w[3]), "+{v[140:143]}"(x[0][3]), "+{v[144:147]}"(x[1][3]));
    if (i == 4) asm volatile("s_waitcnt vmcnt(0)" : "+{v[148:151]}"(w[4]), "+{v[152:155]}"(x[0][4]), "+{v[156:159]}"(x[1][4]));
  }
  DEV void stats(const Params& p, rsrc_t rws, u32& tag, int lane, int s8, u32 slot STM_ARG) {
    const u32 ep = epoch_get(p);
    const u32 dq = ((lane >> 2) ^ (lane >> 4)) & 3;
    const int sidx = s8 + (lane >> 4) + dq * 2, tok = 2 * (sidx & 1023) + (sidx >> 10);
    u32 so = ((dq & 1) | (lane & 3)) ? 0x80000000u : WS_ST2 + sidx * 16, ko = lane ? L_NONE + lane * 16 : slot;
    u32 m1 = __builtin_amdgcn_sbfe(lane, 0, 1), m2 = __builtin_amdgcn_sbfe(lane, 1, 1), mz = __builtin_amdgcn_sbfe(dq, 1, 1), mv = ~__builtin_amdgcn_sbfe(dq, 0, 1);
    u4 on; on.x = on.y = on.z = on.w = 0x3f803f80u;
    PIN(so); PIN(ko); PIN(m1); PIN(m2); PIN(mz); PIN(mv); PIN(on);
    const bf8 ones = __builtin_bit_cast(bf8, on);
    const f4 z4 = {0.f, 0.f, 0.f, 0.f};
    f4 ax[2] = {z4, z4}, aq[2] = {z4, z4}, aw[2] = {z4, z4}, a1 = z4;
    SB();
#pragma unroll
    for (int i = 0; i < 5; ++i) {
      wait(i);
      const bf8 wv = __builtin_bit_cast(bf8, w[i]);
#pragma unroll
      for (int z = 0; z < 2; ++z) {
        const bf8 v = __builtin_bit_cast(bf8, x[z][i]);
        aq[z] = mfma(v, v, aq[z]); ax[z] = mfma(v, ones, ax[z]); aw[z] = mfma(v, wv, aw[z]);
      }
      a1 = mfma(wv, ones, a1);
      SB();
      PST(6 + i);
    }
    tag = epoch_tag(ep);
    PST(11);
    const float sw = (a1[0] + a1[1]) + (a1[2] + a1[3]);
    auto quad = [](float v) { v += dppf<0xB1>(v); v += dppf<0x4E>(v); return v; };
    auto diag = [&](f4 d) { return fsel(m2, fsel(m1, d[3], d[2]), fsel(m1, d[1], d[0])); };
    auto both = [&](float a, float b) { return fsel(mz, dppf<0x128>(b), a); };
    const float mean = fsel(mz, (ax[1][0] + ax[1][1]) + (ax[1][2] + ax[1][3]), (ax[0][0] + ax[0][1]) + (ax[0][2] + ax[0][3])) * (1.f / 640.f);
    const float sxw = quad(both(diag(aw[0]), diag(aw[1])));
    float var = fmaf(-mean, mean, quad(both(diag(aq[0]), diag(aq[1]))) * (1.f / 640.f));
    if (__builtin_expect(__any(mv && !(var * 64.f >= mean * mean)), 0)) {
      float q[2];
#pragma unroll
      for (int z = 0; z < 2; ++z) {
        f4 a = z4;
#pragma unroll
        for (int i = 0; i < 5; ++i) a = mfma(ones, __builtin_bit_cast(bf8, x[z][i]), a);
        const float mu = quad(a[0]) * (1.f / 640.f);
        float t = 0.f;
#pragma unroll
        for (int i = 0; i < 5; ++i)
#pragma unroll
          for (int e = 0; e < 4; ++e) { const float c = bflo(x[z][i][e]) - mu, d = bfhi(x[z][i][e]) - mu; t = fmaf(c, c, fmaf(d, d, t)); }
        t = quad(t);
        t += __shfl_xor(t, 16); t += __shfl_xor(t, 32);
        q[z] = t;
      }
      var = both(q[0], q[1]) * (1.f / 640.f);
    }
    const float mb = rbf(mean);
    const float sd = rbf(__builtin_amdgcn_sqrtf(rbf(rbf(var) + 1e-5f)));
    const float rs = __builtin_amdgcn_rcpf(sd);
    u4 st; st.x = __float_as_uint(mb); st.z = __float_as_uint(rs); st.y = st.w = tag;
    PST(12);
    bst128_sc1(rws, so, st);
    PST(13);
    const float rr = fmaf(-mb, sw, sxw) * rs;
    const u32 b = pk2(rr, rr) & 0xffffu;
    const u32 o = (b & 0x8000u) ? (~b & 0xffffu) : (b | 0x8000u);
    u32 k1 = ((o << 16) | (0xffffu - tok)) & mv, k2 = 0;
    top2_merge(k1, k2, dppu<0x128>(k1), 0);
    k1 = max(k1, dppu<0x124>(k1)); k2 = max(k2, dppu<0x124>(k2));
    {
      auto r1 = __builtin_amdgcn_permlane16_swap(k1, k1, false, false);
      auto r2 = __builtin_amdgcn_permlane16_swap(k2, k2, false, false);
      k1 = r1[0]; k2 = r2[0]; top2_merge(k1, k2, r1[1], r2[1]);
    }
    {
      auto r1 = __builtin_amdgcn_permlane32_swap(k1, k1, false, false);
      auto r2 = __builtin_amdgcn_permlane32_swap(k2, k2, false, false);
      k1 = r1[0]; k2 = r2[0]; top2_merge(k1, k2, r1[1], r2[1]);
    }
    u4 kk; kk.x = k1; kk.z = k2; kk.y = kk.w = tag;
    PST(14);
    st_u4(ko, kk);
  }
};

template <int N, int DM, int DS, int E60 = 0, int TH = 63> DEV void dma_run(u32 m0v, u32 voff, u4 srd, u32 so) {
  asm volatile("s_mov_b32 m0, %2\n\t"
               "s_mov_b32 exec_hi, %7\n\t"
               ".rept %4\n\t"
               "buffer_load_dwordx4 %1, %3, %0 offen lds\n\t"
               "s_add_u32 m0, m0, %5\n\t"
               "s_add_u32 %0, %0, %6\n\t"
               ".if %8 < 63\n\ts_waitcnt vmcnt(%8)\n\t.endif\n\t"
               ".endr\n\t"
               "s_mov_b32 exec_hi, -1"
               : "+s"(so) : "v"(voff), "s"(m0v), "s"(srd), "i"(N), "i"(DM), "i"(DS), "i"(E60 ? 0x0fffffff : -1), "i"(TH) : "scc");
}

struct WoDma {
  u32 vo, vol, m, so, m0;
  DEV void init(int lane, int lw, int ng, int start) {
    const int l60 = min(lane, 59);
    vo = (lw * 6 + l60 / 10) * 1280 + (l60 % 10) * 16 + ng * 160;
    vol = min((104 + lw) * 6 + l60 / 10, 639) * 1280 + (l60 % 10) * 16 + ng * 160;
    m0 = L_WO + lw * 960; m = m0 + start * 3840; so = start * (24 * 1280);
    PIN(vo); PIN(vol);
    asm volatile("" : "+s"(m), "+s"(so), "+s"(m0));
  }
  template <int I0, int I1, int TH = 63, int SKIP = 0> DEV void issue(u4 srd, int lw, u32 f = 0) {
    constexpr int E = I1 > 26 ? 26 : I1;
    if (E > I0)
      asm volatile(".if %7\n\ts_cmp_gt_u32 %8, 53\n\ts_cbranch_scc1 1f\n\t.endif\n\t"
                   "s_mov_b32 m0, %0\n\ts_mov_b32 exec_hi, 0x0fffffff\n\t"
                   ".rept %5\n\tbuffer_load_dwordx4 %2, %3, %1 offen lds\n\ts_addk_i32 m0, 3840\n\ts_addk_i32 %1, 30720\n\t"
                   ".if %6 < 63\n\ts_waitcnt vmcnt(%6)\n\t.endif\n\t.endr\n\t"
                   "s_mov_b32 %0, m0\n\ts_mov_b32 exec_hi, -1\n1:"
                   : "+s"(m), "+s"(so) : "v"(vo), "s"(srd), "s"(m0), "i"(E > I0 ? E - I0 : 1), "i"(TH), "i"(SKIP), "s"(f) : "scc");
    if (I1 > 26) dma_run<1, 0, 0, 1>(L_WO + lw * 960 + 26 * 3840, vol, srd, 0);
  }
};

struct Epi {
  u4 xv[2], sv, fin; u2 xh; u32 goff[2], soff;
  float gf[20], c[4];
  static DEV u32 row(u32 lane) { return ((lane >> 1) & 12) | ((lane >> 2) & 1) | ((lane >> 4) & 2); }
  DEV void init(int lane, int m, int mg, int ng) {
    const int mt = mg * 4 + m, parity = mt >> 6;
    const u32 u = (mt & 63) * 16 + row(lane), o = (2 * u + parity) * 1280 + ng * 160;
    goff[0] = o + (ng & 1) * 32 + (lane & 3) * 16; goff[1] = o + ((ng & 1) ? 0 : 128) + (lane & 3) * 8;
    soff = WS_ST2 + (parity * 1024 + u) * 16;
    PIN(goff[0]); PIN(goff[1]); PIN(soff);
  }
  DEV void issue(u4 srdx, u4 srdws, u32 fo = WS_FIN) {
    asm volatile("buffer_load_dwordx4 %0, %1, %2, 0 offen sc1" : "=v"(sv) : "v"(soff), "s"(srdws));
    asm volatile("buffer_load_dwordx4 %0, %1, %2, 0 offen" : "=v"(xv[0]) : "v"(goff[0]), "s"(srdx));
    asm volatile("buffer_load_dwordx4 %0, %1, %2, 0 offen offset:64" : "=v"(xv[1]) : "v"(goff[0]), "s"(srdx));
    asm volatile("buffer_load_dwordx2 %0, %1, %2, 0 offen" : "=v"(xh) : "v"(goff[1]), "s"(srdx));
    asm volatile("buffer_load_dwordx4 %0, off, %1, %2 sc1" : "=v"(fin) : "s"(srdws), "s"(fo));
  }
  template <int N> DEV void wait() {
    asm volatile("s_waitcnt vmcnt(%5)" : "+v"(xv[0]), "+v"(xv[1]), "+v"(xh), "+v"(sv), "+v"(fin) : "i"(N) : "memory");
  }
  DEV void check(char* ws, u32 tag) {
    if (__builtin_expect(__any(__builtin_amdgcn_bitop3_b32(sv.y, sv.w, tag, 0x7e) != 0u), 0)) {
      const rsrc_t rws = __builtin_amdgcn_make_buffer_rsrc(ws, 0, WS_END, 0x00020000);
#pragma nounroll
      for (u32 spin = 0; !__all(sv.y == tag && sv.w == tag) && spin < SPIN_CAP; ++spin) {
        FENCE();
        sv = bld128_sc1(rws, soff);
      }
      __builtin_amdgcn_s_waitcnt(0x0f70);
    }
  }
  DEV u32 xw(int i) const { return i < 8 ? xv[i >> 2][i & 3] : xh[i & 1]; }
  DEV void gate(int i) {
    if (i == 0) {
      c[0] = __uint_as_float(sv.z); c[1] = -__uint_as_float(sv.x) * c[0];
      c[2] = c[0] * -1.4426950408889634f; c[3] = c[1] * -1.4426950408889634f;
    }
    float x = i & 1 ? bfhi(xw(i >> 1)) : bflo(xw(i >> 1));
    PIN(x);
    float z = fmaf(x, c[0], c[1]), t = fmaf(x, c[2], c[3]);
    PIN(z); PIN(t);
    gf[i] = z * __builtin_amdgcn_rcpf(1.f + __builtin_amdgcn_exp2f(t));
    PIN(gf[i]);
  }
};
#define SPIN(x) asm volatile("" : "+s"(x))
#define HALF_A(SB, VX, VW, SX, SW, SA, SC, EA, EC) \
    "s_add_u32 m0, " SB ", 12288\n\ts_mov_b64 exec, " EC "\n\ts_cbranch_execz 7f\n\tbuffer_load_dwordx4 " VX ", " SX ", " SC " offen lds\n7:\n\t" \
    "s_mov_b32 m0, " SB "\n\ts_mov_b64 exec, " EA "\n\tbuffer_load_dwordx4 " VX ", " SX ", " SA " offen lds\n\t" \
    "s_addk_i32 m0, 14336\n\ts_mov_b64 exec, -1\n\tbuffer_load_dwordx4 " VW ", " SW ", 0 offen lds\n\t" \
    "s_addk_i32 m0, 6912\n\ts_nop 0\n\tbuffer_load_dwordx4 " VW ", " SW ", 0 offen offset:1280 lds\n\t" \
    "s_addk_i32 m0, 6912\n\ts_nop 0\n\tbuffer_load_dwordx4 " VW ", " SW ", 0 offen offset:2560 lds\n\t"
#define HALF_B(SB, VX, VW, SX, SW, SB1, SB2, SH) \
    "s_add_u32 m0, " SB ", 4096\n\ts_nop 0\n\tbuffer_load_dwordx4 " VX ", " SX ", " SB1 " offen lds\n\t" \
    "s_addk_i32 m0, 4096\n\ts_nop 0\n\tbuffer_load_dwordx4 " VX ", " SX ", " SB2 " offen lds\n\t" \
    "s_addk_i32 m0, 10240\n\ts_nop 0\n\tbuffer_load_dwordx4 " VW ", " SW ", " SH " offen lds\n\t" \
    "s_addk_i32 m0, 6912\n\ts_nop 0\n\tbuffer_load_dwordx4 " VW ", " SW ", " SH " offen offset:1280 lds\n\t" \
    "s_addk_i32 m0, 6912\n\ts_nop 0\n\tbuffer_load_dwordx4 " VW ", " SW ", " SH " offen offset:2560 lds\n\t"
struct LdIn {
  u4 srdx, srdw; u32 vx, vw, lw, j, tp, cls, kb, sa, sb1, sb2, sc, sb, sh; u64 ec, ea;
  DEV void take() {
    asm volatile("" : "={s[4:7]}"(srdx), "={s[8:11]}"(srdw), "={v1}"(vx), "={v2}"(vw), "={s12}"(lw), "={s13}"(j), "={s14}"(tp), "={s15}"(cls), "={s16}"(kb),
                 "={s18}"(sa), "={s19}"(sb1), "={s20}"(sb2), "={s21}"(sc), "={s[22:23]}"(ec), "={s[24:25]}"(ea), "={s26}"(sb), "={s27}"(sh));
  }
};
DEV void main_loader(Params& p, LdIn& in, int lane, int parity, int mg, int ng, u32 koff, Epi& ei STM_ARG) {
  const int lw = in.lw, tp = in.tp, j = in.j, rt = parity * 10 + tp, u0 = tp * 104 - 8;
  const u4 srdx = in.srdx;
  u64 ktab = KT0;
  ktab = in.cls == 1 ? KT1 : ktab; SPIN(ktab); ktab = in.cls == 2 ? KT2 : ktab; SPIN(ktab); ktab = in.cls == 3 ? KT3 : ktab; SPIN(ktab); ktab = in.cls == 4 ? KT4 : ktab;
  auto next = [&]() {
    ktab >>= 4;
    u32 kb = ((u32)ktab & 15u) + koff;
    kb = min(kb, kb - 10u);
    const int d = kb - in.kb;
    in.kb = kb;
    in.vx += d * 128; in.vw += d * (64 * 3840);
    in.sb = in.sb >= 3 * SLOT ? in.sb - 3 * SLOT : in.sb + SLOT;
    PIN(in.vx); PIN(in.vw); SPIN(in.sb);
  };
  auto issue = [&]() {
    asm volatile("s_bitcmp1_b32 %12, 1\n\ts_cselect_b64 vcc, -1, 0\n\ts_cbranch_vccnz 5f\n4:\n\t"
                 HALF_A("%0", "%1", "%2", "%3", "%4", "%5", "%6", "%7", "%8") "s_cbranch_vccnz 6f\n5:\n\t"
                 HALF_B("%0", "%1", "%2", "%3", "%4", "%9", "%10", "%11") "s_cbranch_vccnz 4b\n6:"
                 :: "s"(in.sb), "v"(in.vx), "v"(in.vw), "s"(in.srdx), "s"(in.srdw), "s"(in.sa), "s"(in.sc), "s"(in.ea), "s"(in.ec),
                    "s"(in.sb1), "s"(in.sb2), "s"(in.sh), "s"(in.cls) : "scc", "vcc");
    next();
  };
#define STAGE_BAR(E) asm volatile("s_cmp_lt_u32 %0, 2\n\ts_cbranch_scc1 1f\n\ts_waitcnt vmcnt(%1)\n1:\n\ts_waitcnt vmcnt(%2)\n\ts_barrier" :: "s"(lw), "i"(9 + E), "i"(10 + E) : "scc", "memory")
  next();
  u32 ep = epoch_get(p);
  const u4 srdws = srd(p.ws);
  const int srow = lw * 28 + min(lane, 27);
  u32 go1 = WS_ST2 + (parity * 1024 + min(max(u0 + srow, 0), 1023)) * 16;
  u32 wa = lw < 2 ? WS_XS + ((rt * 10 + j) * 14 + (lw ? 14 : -7)) * 512 + min(lane, 27) * 128 : (BOXW == 0 || (BOXW == 2 && lane) ? 0x80000000u : WS_BOX + (mg * 8 + ng) * 256 + (lw & 1) * 64);
  u32 ho = (lane | (lw ^ 3)) ? 0x80000000u : WS_FLAG + (rt * 10 + j) * 256 + 128;
  PIN(go1); PIN(wa); PIN(ho);
  WoDma wo; wo.init(lane, lw, ng, (WOST >> (8 * (mg & 3))) & 255);
  ei.init(lane, lw, mg, ng);
  SB();
  PST(4);
  issue();
  u32 tagv = epoch_tag(ep);
  PIN(tagv);
  STAMP(9);
  PST(5);
  STAGE_BAR(0);
  PST(7);
  STAMP(2);
  asm volatile("buffer_store_dword %0, %1, %2, 0 offen sc1" :: "v"(tagv), "v"(ho), "s"(srdws));
#pragma unroll 1
  for (int s = 2; s < 9; ++s) {
    issue();
    STAGE_BAR(0);
  }
  u4 g1;
  asm volatile("buffer_load_dwordx4 %0, %1, %2, 0 offen sc1" : "=v"(g1) : "v"(go1), "s"(srdws));
  asm volatile("s_mov_b32 m0, %0\n\ts_mov_b64 exec, 0x0fffffff\n\tbuffer_load_dword %1, %2, 0 offen sc1 lds\n\t"
               "s_add_u32 m0, m0, 504\n\ts_nop 0\n\tbuffer_load_dword %1, %2, 0 offen offset:8 sc1 lds\n\ts_mov_b64 exec, -1"
               :: "s"(L_MR + lw * 112), "v"(go1), "s"(srdws) : "scc");
  issue();
  STAGE_BAR(3);
  STAMP(1);
  asm volatile("s_waitcnt vmcnt(11)" : "+v"(g1) :: "memory");
  if (__builtin_expect(__any(__builtin_amdgcn_bitop3_b32(g1.y, g1.w, tagv, 0x7e) != 0u), 0)) {
    const rsrc_t rws = __builtin_amdgcn_make_buffer_rsrc(p.ws.p, 0, WS_END, 0x00020000);
#pragma nounroll
    for (u32 spin = 0; spin < SPIN_CAP; ++spin) {
      FENCE();
      g1 = bld128_sc1(rws, go1);
      if (__all(g1.y == tagv && g1.w == tagv)) break;
      __builtin_amdgcn_s_sleep(2);
    }
    __builtin_amdgcn_s_waitcnt(0x0f70);
    st_u(L_MR + srow * 4, g1.x); st_u(L_MR + 512 + srow * 4, g1.z);
  }
  const u4 srdo = srd(p.wo);
  if (BOXW == 2)
    asm volatile("s_mov_b32 m0, %0\n\ts_cmp_lt_u32 %3, 2\n\ts_cbranch_scc1 1f\n\tbuffer_atomic_add %4, %1, %2, 0 offen\n\ts_branch 2f\n"
                 "1:\n\tbuffer_load_dword %1, %2, 0 offen sc1 lds\n2:" :: "s"(L_DUMP + lw * 256), "v"(wa), "s"(srdws), "s"(lw), "v"(0u) : "scc");
  else
  asm volatile("s_mov_b32 m0, %0\n\ts_nop 0\n\tbuffer_load_dword %1, %2, 0 offen sc1 lds" :: "s"(L_DUMP + lw * 256), "v"(wa), "s"(srdws));
  wo.issue<0, WO7>(srdo, lw);
  __builtin_amdgcn_s_setprio(0);
  wait_bar<WO7 + 1>();
  STAMP(3);
  wo.issue<WO7, WOA, WTH>(srdo, lw);
  SB();
  ei.issue(srdx, srdws);
  wo.issue<WOA, 27, WTH2>(srdo, lw);
  ei.wait<27 - WOA>();
  ei.check(p.ws.p, tagv);
  STAMP(4);
}

DEV int fresh_lane() {
  int l;
  asm volatile("v_mbcnt_lo_u32_b32 %0, -1, 0\n\tv_mbcnt_hi_u32_b32 %0, -1, %0" : "=v"(l));
  __builtin_assume(l >= 0 && l < 64);
  return l;
}
struct G2 {
  u32 b0, oo, fo, ga, one, pa, pm;
  DEV void init(int wave, int mg, int ng) {
    const int lane = fresh_lane(), g = lane >> 4, i16 = lane & 15, m = wave & 3, kh = wave >> 2;
    b0 = L_WO + (kh * 320 + 16 * (g >> 1) + 4 * (g & 1) + (i16 >> 2)) * 160 + (lane & 3) * 8;
    oo = WS_O + (mg * 4 + m) * 20480 + kh * 10240 + lane * 16;
    fo = WS_BOX + (mg * 8 + ng) * 256 + kh * 64 + lane * 4; ga = Y_GO + kh * 4 + lane * 4 + min(lane, 1) * 0x10000; one = 1; pa = Y_PUB;
    pm = ((0x12u >> wave) & 1) ? (((0x4b5au >> (mg & 15)) & 1) ? 0x3ffu : 0x1fu) : 0u;
    PIN(b0); PIN(oo); PIN(fo); PIN(ga); PIN(one); PIN(pa); SPIN(pm);
  }
};

constexpr u64 sel_mask(int r) {
  u64 m = 0;
  for (int l = 0; l < 64; ++l) {
    const int i = l & 15, c = 4 * ((l >> 4) & 1) + r;
    if (i < 8 ? c <= i : c > i - 8) m |= 1ull << l;
  }
  return m;
}
constexpr u64 SEL0 = sel_mask(0), SEL1 = sel_mask(1), SEL2 = sel_mask(2), SEL3 = sel_mask(3);
#define LM(m) __builtin_amdgcn_inverse_ballot_w64(m)
DEV u2 and2(u2 a, u2 m) { u2 r; r.x = a.x & m.x; r.y = a.y & m.y; return r; }

struct Att {
  u32 aA0[2], aA1[2], aB[2];
  u32 mOwn, mRot, mV, mLo, so[2], xg, aXw, aXr, aCS, aCV, aST, aMR, aH, aF;
  u4 mS;
  DEV void init_k(u32 ln, u32 n) {
    const u32 a0 = XB + ((ln & 15) << 7) + ((((ln >> 1) & 7) ^ (ln >> 4)) << 4);
    const u32 b0 = WB + ((ln & 48) << 6) + ((ln & 12) << 5) + (((2 * n + ((ln >> 1) & 1)) ^ ((ln >> 2) & 6)) << 4) + ((ln & 1) << 3);
#pragma unroll
    for (int s = 0; s < 2; ++s) {
      aA0[s] = a0 + s * 2 * SLOT; aA1[s] = (a0 ^ 64) + s * 2 * SLOT; aB[s] = b0 + s * 2 * SLOT;
      PIN(aA0[s]); PIN(aA1[s]); PIN(aB[s]);
    }
  }
  template <int NS> DEV void init(u32 ln, int n, int tau, bool edge, int tp, int parity, int j) {
    constexpr int s0 = NS == 4 ? 0 : 4;
    constexpr u32 SGN = 0x80000000u;
    const u32 i16 = ln & 15, g16 = ln & 48, h32 = ln >> 5, i4 = i16 * 4;
    mLo = ~(u32)__builtin_amdgcn_sbfe((int)ln, 3, 1);
    const u32 gt = (u32)(((int)g16 - 16 * tau) >> 31);
    mOwn = mLo & gt; mRot = ~mLo & ~gt;
    mV = (u32)(((int)i16 - 4 * tau) >> 31);
    u32 l0 = (u32)SEL0, l1 = (u32)SEL1, l2 = (u32)SEL2, l3 = (u32)SEL3;
    SPIN(l0); SPIN(l1); SPIN(l2); SPIN(l3);
    mS.x = __builtin_amdgcn_sbfe(l0, ln, 1); mS.y = __builtin_amdgcn_sbfe(l1, ln, 1); mS.z = __builtin_amdgcn_sbfe(l2, ln, 1); mS.w = __builtin_amdgcn_sbfe(l3, ln, 1);
    PIN(mOwn); PIN(mRot); PIN(mV); PIN(mLo); PIN(mS);
    const u32 up = tp * 104 + 8 + s0 * 16 + h32 * 16 + i16;
    const u32 hs = ln << 26;
    so[0] = WS_O + (parity * 64 - 1) * 20480 + (2 * j + (n >> 1)) * 1024 + (n & 1) * 512 + (up >> 4) * 20480 + (((ln & 16) | (up & 15)) << 4);
    so[1] = so[0] + 2 * 20480;
    if (NS == 4) so[0] |= ~hs & mLo & SGN;
    else so[1] |= (hs | (tp == 9 ? SGN : 0u)) & SGN;
    PIN(so[0]); PIN(so[1]);
    const int xl = n > 0 ? L_X + ((n - 1) * 7 + s0) * 512 : L_NONE, xh = n < 3 ? L_X + (n * 7 + s0) * 512 : L_NONE;
    aXr = xl + (int)h32 * (xh - xl) + ln * 4;
    aXw = aXr ^ 128;
    aCS = L_CS + n * 64 + g16;
    aCV = L_CS + 512 + n * 64 + i4;
    if (NS == 4) { aCS += i16 * 64 + min(i16, 1u) * 0x10000; aCV += min(g16, 1u) * 0x10000; }
    aF = Y_F + s0 * 4 + n * 4 + ln * 4 + min(ln, 1u) * 0x10000;
    aST = L_MR + s0 * 64 + i4;
    aMR = L_MR + s0 * 64 + g16;
    aH = n * 512 + ln * 8;
    const u32 xa = WS_XS + (((parity * 10 + tp) * 10 + j) * 14 + (n ? 7 : 0) + s0) * 512 + (edge ? 0u : SGN);
    xg = (xa + (ln & 31) * 16) | ((hs ^ (n ? SGN : 0u)) & SGN);
    PIN(aXw); PIN(aXr); PIN(aCS); PIN(aCV); PIN(aST); PIN(aMR); PIN(aH); PIN(aF); PIN(xg);
  }
};

template <int NS>
DEV void main_compute(const Params& p, rsrc_t rws, u32& tag, int lane, int wave, int tp, int parity, int rb, int j, int mg, int ng, G2& g2 STM_ARG) {
  constexpr int s0 = NS == 4 ? 0 : 4;
  const int n = (wave & 3) ^ (NS == 3);
  const int rt = parity * 10 + tp;
  u32 ep = epoch_get(p);
  Att at;
  at.init_k(lane, n);
  if (NS == 4) st_u(lane < 32 ? wave * SLOT + XB + lane * 4 : L_SYNC + wave * 64 + (lane & 15) * 4, lane < 32 ? 0x3f803f80u : 0u);
  int tau = n - rb; tau += tau < 0 ? 5 : 0;
  const bool edge = ((0x0f00001e >> (n * 8 + tau)) & 1) != 0;
  SB();
  PST(2);
  if (INIT1) at.template init<NS>(lane, n, tau, edge, tp, parity, j);
  SB();
  PST(3);
  tag = epoch_tag(ep);
  const u32 (&aA0)[2] = at.aA0, (&aA1)[2] = at.aA1, (&aB)[2] = at.aB, (&so)[2] = at.so;
  const u32 &mOwn = at.mOwn, &mRot = at.mRot, &mV = at.mV, &mLo = at.mLo, &xg = at.xg, &aXw = at.aXw, &aXr = at.aXr, &aCS = at.aCS, &aCV = at.aCV,
            &aST = at.aST, &aMR = at.aMR, &aH = at.aH, &aF = at.aF;
  const u4& mS = at.mS;
  f4 acc[NS][3];
  u2 m0, mm, mp;
#pragma unroll
  for (int a = 0; a < NS; ++a)
#pragma unroll
    for (int b = 0; b < 3; ++b) acc[a][b] = f4{0.f, 0.f, 0.f, 0.f};
  bf8 A[3][NS], B[3][3];
  auto ldf = [&](int buf, int st, int kk) {
    const int rs = (st + 3) & 3, h = rs >> 1;
    const u32 of = (rs & 1) * SLOT;
    B[buf][0] = ld_tr8(aB[h] + of + kk * 4096, 512);
    A[buf][0] = ld_b128((kk ? aA1[h] : aA0[h]) + of + s0 * 2048);
    B[buf][1] = ld_tr8(aB[h] + of + kk * 4096 + 8192, 512);
    B[buf][2] = ld_tr8(aB[h] + of + kk * 4096 + 16384, 512);
#pragma unroll
    for (int a = 1; a < NS; ++a) A[buf][a] = ld_b128((kk ? aA1[h] : aA0[h]) + of + (s0 + a) * 2048);
  };
  auto mma = [&](int buf, int b0, int b1) {
    if (MORD == 1) {
#pragma unroll
      for (int b = b0; b < b1; ++b)
#pragma unroll
        for (int i = 0; i < NS; ++i) {
          const int a = (b & 1) ? NS - 1 - i : i;
          acc[a][b] = b < 2 ? mfma(B[buf][b], A[buf][a], acc[a][b]) : mfma(A[buf][a], B[buf][b], acc[a][b]);
        }
    } else if (MORD == 2) {
#pragma unroll
      for (int a = 0; a < NS; ++a)
#pragma unroll
        for (int i = 0; i < 2; ++i) { const int b = (a & 1) ? 1 - i : i; acc[a][b] = mfma(B[buf][b], A[buf][a], acc[a][b]); }
      if (b1 == 3) {
#pragma unroll
        for (int a = 0; a < NS; ++a) acc[a][2] = mfma(A[buf][a], B[buf][2], acc[a][2]);
      }
    } else {
#pragma unroll
    for (int a = 0; a < NS; ++a)
#pragma unroll
      for (int b = b0; b < b1; ++b)
        acc[a][b] = b < 2 ? mfma(B[buf][b], A[buf][a], acc[a][b]) : mfma(A[buf][a], B[buf][b], acc[a][b]);
    }
  };
  auto pattern = [&]() {
#pragma unroll
    for (int i = 0; i < NS + 6; ++i) {
      if (i < 3 * NS) __builtin_amdgcn_sched_group_barrier(0x008, 1, 0);
      __builtin_amdgcn_sched_group_barrier(0x100, 1, 0);
    }
    if (3 * NS > NS + 6) __builtin_amdgcn_sched_group_barrier(0x008, 2 * NS - 6, 0);
  };

  STAMP(1);
  PST(4);
  if (PBRK && NS == 4) NEW_PAGE();
  lds_bar();
  PST(5);
  STAMP(2);
  CSTAMP(0);
  ldf(0, 0, 0);
#pragma unroll
  for (int s = 0; s < 9; ++s) {
    SB();
    ldf(1, s, 1);
    mma(0, 0, 3);
    if (s == 8) {
#pragma unroll
      for (int i = 0; i < (NS + 7) / 2; ++i) {
        __builtin_amdgcn_sched_group_barrier(0x008, 1, 0);
        __builtin_amdgcn_sched_group_barrier(0x100, 2, 0);
      }
    } else pattern();
    SB();
    if (s == 0 && !INIT1) { at.template init<NS>(lane, n, tau, edge, tp, parity, j); SB(); }
    if (s == 8) break;
    if (s >= 1) KST(2 * s - 1);
    if (s < 3) PST(6 + 2 * s);
    if (NS == 3 && s >= PACE0 && PACE) asm volatile(".rept %0\n\ts_nop 7\n\t.endr" :: "i"(PACE));
    bar();
    if (PBRK && NS == 3 && s == 7) NEW_PAGE();
    if (s < 3) PST(7 + 2 * s);
    KST(2 * s);
    SB();
    ldf(0, s + 1, 0);
    mma(1, 0, 3);
    pattern();
    SB();
  }
  u32 inner = (n + 1) & 2;
  SPIN(inner);
#define PRIO(PI, PO) asm volatile("s_setprio " #PI "\n\ts_cmp_lg_u32 %0, 0\n\ts_cbranch_scc1 1f\n\ts_setprio " #PO "\n\t1:" :: "s"(inner) : "scc")
  u32 lev = 0;
  auto step = [&]() { ++lev; asm volatile("ds_write_b32 %0, %1" :: "v"(aF), "v"(lev) : "memory"); };
  PRIO(2, 3);
  KST(15);
  if (PBRK && NS == 4) NEW_PAGE();
  lds_bar();
  FST(0);
  SB();
  auto mmqk = [&](int buf) {
    mma(buf, 0, 2);
    if (NS == 4) acc[0][2] = mfma(A[buf][0], B[buf][2], acc[0][2]);
  };
  f2 stt[NS];
  ldf(0, 9, 0);
  ldf(2, 9, 1);
  if (NS == 3) {
#pragma unroll
    for (int a = 0; a < NS; ++a) { stt[a].x = ld_f(aST + a * 64); stt[a].y = ld_f(aST + a * 64 + 512); FENCE(); }
    SB();
    mmqk(1);
    SB();
  } else {
    mmqk(1);
#pragma unroll
    for (int i = 0; i < 7; ++i) {
      __builtin_amdgcn_sched_group_barrier(0x008, 1, 0);
      __builtin_amdgcn_sched_group_barrier(0x100, 3, 0);
    }
    SB();
#pragma unroll
    for (int a = 0; a < NS; ++a) { stt[a].x = ld_f(aST + a * 64); stt[a].y = ld_f(aST + a * 64 + 512); FENCE(); }
    SB();
  }
  FST(1);
  mmqk(0);
  SB();
  f4 cs[2];
  u32 lvl;
  auto reached = [&](u32 v) { return __builtin_expect((u32)__builtin_amdgcn_readfirstlane(lvl) >= v, 1); };
  auto peekcs = [&]() {
    FENCE(); lvl = lds_peek(Y_F + n * 4); FENCE();
    cs[0] = ld_f4(aCS); cs[1] = ld_f4(aCS + 256);
    FENCE();
  };
  if (NS == 4) {
    acc[0][0] = mfma(B[2][0], A[2][0], acc[0][0]); acc[0][1] = mfma(B[2][1], A[2][0], acc[0][1]); acc[0][2] = mfma(A[2][0], B[2][2], acc[0][2]);
#pragma unroll
    for (int a = 1; a < 3; ++a) { acc[a][0] = mfma(B[2][0], A[2][a], acc[a][0]); acc[a][1] = mfma(B[2][1], A[2][a], acc[a][1]); }
#pragma unroll
    for (int a = 0; a < NS; ++a) PIN(stt[a]);
    SB();
    st_f4(aCS, acc[0][0]); st_f4(aCS + 256, acc[0][1]); st_f(aCV, acc[0][2][0]);
    step();
    SB();
    acc[NS - 1][0] = mfma(B[2][0], A[2][NS - 1], acc[NS - 1][0]); acc[NS - 1][1] = mfma(B[2][1], A[2][NS - 1], acc[NS - 1][1]);
#pragma unroll
    for (int b = 1; b >= 0; --b)
#pragma unroll
      for (int r = 0; r < 4; ++r) cs[b][r] = dppf<0x150>(acc[0][b][r]);
    PIN(cs[0]); PIN(cs[1]);
  } else {
    acc[0][0] = mfma(B[2][0], A[2][0], acc[0][0]); acc[0][1] = mfma(B[2][1], A[2][0], acc[0][1]);
    SB();
    peekcs();
    SB();
#pragma unroll
    for (int a = 1; a < NS; ++a) { acc[a][0] = mfma(B[2][0], A[2][a], acc[a][0]); acc[a][1] = mfma(B[2][1], A[2][a], acc[a][1]); }
    SB();
    if (!reached(1)) {
      lds_wait_ge<0>(Y_F + n * 4, 1);
      peekcs();
    }
  }
#pragma unroll
  for (int a = 0; a < NS; ++a) { PIN(acc[a][0]); PIN(acc[a][1]); PIN(acc[a][2]); }
  SB();
  CSTAMP(1);
  STAMP(3);
  FST(2);
  auto bc4 = [](float v) { return f4{v, v, v, v}; };
  auto pk4 = [](f4 v) { u2 r; r.x = pk2(v[0], v[1]); r.y = pk2(v[2], v[3]); return r; };
  u2 q[NS], kLo[NS], kHi[NS + 1];
#pragma unroll
  for (int a = 0; a < NS; ++a) { acc[a][1] = __builtin_elementwise_fma(bc4(-stt[a].x), cs[1], acc[a][1]); PIN(acc[a][1]); }
  SB();
#pragma unroll
  for (int a = 0; a < NS; ++a) { acc[a][1] *= stt[a].y; PIN(acc[a][1]); }
  SB();
  {
    u2 k[NS];
#pragma unroll
    for (int a = 0; a < NS; ++a) { k[a] = pk4(acc[a][1]); PIN(k[a]); }
    SB();
#pragma unroll
    for (int a = 0; a < NS; ++a) { acc[a][0] = __builtin_elementwise_fma(bc4(-stt[a].x), cs[0], acc[a][0]); PIN(acc[a][0]); }
    SB();
#pragma unroll
    for (int i = 0; i < NS; ++i) {
      const int a = NS - 1 - i;
      asm volatile("v_and_b32 %0, %4, %7\n\tv_and_b32 %1, %5, %7\n\t"
                   "v_and_b32_dpp %0, %4, %6 row_ror:8 row_mask:0xf bank_mask:0x3\n\tv_and_b32_dpp %1, %5, %6 row_ror:8 row_mask:0xf bank_mask:0x3\n\t"
                   "v_and_b32 %2, %4, %6\n\tv_and_b32 %3, %5, %6\n\t"
                   "v_and_b32_dpp %2, %4, %7 row_ror:8 row_mask:0xf bank_mask:0xc\n\tv_and_b32_dpp %3, %5, %7 row_ror:8 row_mask:0xf bank_mask:0xc"
                   : "=&v"(kHi[a + 1].x), "=&v"(kHi[a + 1].y), "=&v"(kLo[a].x), "=&v"(kLo[a].y) : "v"(k[a].x), "v"(k[a].y), "v"(mOwn), "v"(mRot));
      if (NS == 4 && i == 0) { st_u2(L_HK + aH, kHi[NS]); step(); }
    }
  }
  if (NS == 3) { FENCE(); lvl = lds_peek(Y_F + n * 4); FENCE(); kHi[0] = ld_u2(L_HK + aH); FENCE(); }
  SB();
#pragma unroll
  for (int a = 0; a < NS; ++a) { q[a] = pk4(acc[a][0]); PIN(q[a]); }
  SB();
  FST(3);
  const f4 z = {0.f, 0.f, 0.f, 0.f};
  f4 t[NS];
  u4 tv[NS];
  {
    f4 d0[NS], d1[NS];
    u4 qq[NS];
    u32 c0 = tp == 0 ? 0xf149f2cau : 0u;
    SPIN(c0);
#pragma unroll
    for (int a = NS - 1; a >= 0; --a) {
      d0[a] = mfma16(kLo[a], q[a], (NS == 4 && a == 0) ? bc4(__uint_as_float(c0)) : z);
      qq[a].x = q[a].x & mLo; qq[a].y = q[a].y & mLo; qq[a].z = q[a].x & ~mLo; qq[a].w = q[a].y & ~mLo;
    }
#pragma unroll
    for (int a = NS - 1; a > 0; --a) { d1[a] = mfma(mk8(kHi[a], kHi[a + 1]), __builtin_bit_cast(bf8, qq[a]), z); tv[a].y = tv[a].w = tag; }
    tv[0].y = tv[0].w = tag;
    if (NS == 4) {
      d1[0] = mfma16(kHi[1], q[0], z);
    } else {
      if (!reached(2)) { lds_wait_ge<0>(Y_F + n * 4, 2); kHi[0] = ld_u2(L_HK + aH); }
      d1[0] = mfma(mk8(kHi[0], kHi[1]), __builtin_bit_cast(bf8, qq[0]), z);
    }
#pragma unroll
    for (int a = NS - 1; a >= 0; --a) {
#pragma unroll
      for (int r = 0; r < 4; ++r) t[a][r] = fsel(mS[r], d0[a][r], d1[a][r]);
      PIN(t[a]);
    }
  }
  SB();
  FST(4);
#pragma unroll
  for (int a = NS - 1; a >= 0; --a) { tv[a].x = pk2(t[a][0], t[a][1]); tv[a].z = pk2(t[a][2], t[a][3]); PIN(tv[a]); }
  if (__builtin_expect(edge, 1)) {
#pragma unroll
    for (int a = NS - 1; a >= 0; --a) __builtin_amdgcn_raw_buffer_store_b128(tv[a], rws, xg + a * 512, 0, 16);
  }
  SB();
#pragma unroll
  for (int a = 0; a < NS; ++a)
    asm volatile("ds_write2st64_b32 %0, %1, %2 offset0:%3 offset1:%4" :: "v"(aXw), "v"(tv[a].x), "v"(tv[a].z), "i"(2 * a), "i"(2 * a + 1) : "memory");
  step();
  STAMP(10);
  FST(5);
  SB();
  PRIO(2, 1);
  f4 vm[NS], vr[NS];
  const float csv = ld_f((aCV & 0xffffu) | (L_CS & ~0xffffu));
#pragma unroll
  for (int a = 0; a < NS; ++a) { vm[a] = ld_f4(aMR + a * 64); vr[a] = ld_f4(aMR + 512 + a * 64); }
  SB();
#pragma unroll
  for (int bi = 0; bi < 3; ++bi)
#pragma unroll
    for (int a = NS - 1; a >= (NS == 4 ? 1 : 0); --a) { const int buf = bi == 0 ? 1 : (bi == 1 ? 0 : 2); acc[a][2] = mfma(A[buf][a], B[buf][2], acc[a][2]); }
  m0.x = (mS.x & 0xffffu) | (mS.y & 0xffff0000u); m0.y = (mS.z & 0xffffu) | (mS.w & 0xffff0000u);
  mm.x = ~m0.x & mLo; mm.y = ~m0.y & mLo; mp.x = ~(m0.x | mLo); mp.y = ~(m0.y | mLo);
  u32 uLo = 0x3f80u, uHi = 0x3f800000u;
  u2 hs; hs.x = hs.y = (lane < 32 ? mV : ~mV) & 0x3f803f80u;
  u32 aFn = Y_F + s0 * 4 + min(max(n + (lane < 32 ? -1 : 1), 0), 3) * 4;
  u32 xr = xg + (n ? 7 * 512 : -7 * 512);
  float cq[NS];
#pragma unroll
  for (int a = 0; a < NS; ++a) { cq[a] = stt[a].y * (0.22360679774997896f * 1.4426950408889634f); PIN(cq[a]); }
  PIN(m0); PIN(mm); PIN(mp); PIN(uLo); PIN(uHi); PIN(hs); PIN(aFn); PIN(xr);
#pragma unroll
  for (int a = 0; a < NS; ++a) PIN(acc[a][2]);
  SB();
  FST(6);
  u4 e[NS];
#pragma unroll
  for (int a = 0; a < NS; ++a) asm volatile("" : "=v"(e[a]));
  auto look = [&]() {
#pragma unroll
    for (int a = 0; a < NS; ++a) e[a] = __builtin_amdgcn_raw_buffer_load_b128(rws, xr + a * 512, 0, 16);
  };
  u2 vLo[NS], vHi[NS + 1];
#pragma unroll
  for (int a = NS - 1; a >= 0; --a) { acc[a][2] = __builtin_elementwise_fma(vm[a], bc4(-csv), acc[a][2]); PIN(acc[a][2]); }
  SB();
#pragma unroll
  for (int a = NS - 1; a >= 0; --a) { acc[a][2] *= vr[a]; PIN(acc[a][2]); }
  SB();
#pragma unroll
  for (int a = NS - 1; a >= 0; --a) { vLo[a] = pk4(acc[a][2]); PIN(vLo[a]); }
  SB();
  if (__builtin_expect(edge, 1)) look();
  SB();
  constexpr u32 XT = NS == 4 ? 3 : 1;
  u2 y[NS];
  u32 cx;
  auto ldy = [&]() {
    FENCE(); cx = lds_peek(aFn); FENCE();
#pragma unroll
    for (int a = 0; a < NS; ++a) { y[a].x = ld_u(aXr + a * 512); y[a].y = ld_u(aXr + a * 512 + 256); }
    FENCE();
  };
  ldy();
  SB();
#pragma unroll
  for (int a = NS - 1; a >= 0; --a) {
    vHi[a + 1].x = vLo[a].x & ~mV; vHi[a + 1].y = vLo[a].y & ~mV; vLo[a].x &= mV; vLo[a].y &= mV;
    PIN(vLo[a]); PIN(vHi[a + 1]);
  }
  SB();
#pragma unroll
  for (int a = NS - 1; a >= 0; --a) {
    auto wx = __builtin_amdgcn_permlane32_swap(vLo[a].x, vHi[a + 1].x, false, false);
    auto wy = __builtin_amdgcn_permlane32_swap(vLo[a].y, vHi[a + 1].y, false, false);
    vLo[a].x = wx[0]; vLo[a].y = wy[0]; vHi[a + 1].x = wx[1]; vHi[a + 1].y = wy[1];
    PIN(vLo[a]); PIN(vHi[a + 1]);
    if (NS == 4 && a == NS - 2) { st_u2(L_HV + aH, vHi[NS]); step(); }
  }
  if (NS == 3) { FENCE(); lvl = lds_peek(Y_F + n * 4); FENCE(); vHi[0] = ld_u2(L_HV + aH); FENCE(); }
  bf8 vH[NS];
#pragma unroll
  for (int a = 1; a < NS; ++a) { vH[a] = mk8(vHi[a], vHi[a + 1]); PIN(vH[a]); }
  SB();
  FST(7);
  if (__any(cx < XT)) {
#pragma nounroll
    for (u32 spin = 0; spin < (1u << 20); ++spin) {
      FENCE();
      if (__all(lds_peek(aFn) >= XT)) break;
    }
    ldy();
  }
  auto addp = [](f4& t, u32 a, u32 b, u32 lo, u32 hi) {
    t[0] = __builtin_amdgcn_fdot2_f32_bf16(__builtin_bit_cast(bf2, a), __builtin_bit_cast(bf2, lo), t[0], false);
    t[1] = __builtin_amdgcn_fdot2_f32_bf16(__builtin_bit_cast(bf2, a), __builtin_bit_cast(bf2, hi), t[1], false);
    t[2] = __builtin_amdgcn_fdot2_f32_bf16(__builtin_bit_cast(bf2, b), __builtin_bit_cast(bf2, lo), t[2], false);
    t[3] = __builtin_amdgcn_fdot2_f32_bf16(__builtin_bit_cast(bf2, b), __builtin_bit_cast(bf2, hi), t[3], false);
  };
#pragma unroll
  for (int a = 0; a < NS; ++a) { addp(t[a], y[a].x, y[a].y, uLo, uHi); PIN(t[a]); }
  if (NS == 3) {
    if (!reached(4)) { lds_wait_ge<0>(Y_F + n * 4, 4); vHi[0] = ld_u2(L_HV + aH); }
    vH[0] = mk8(vHi[0], vHi[1]); PIN(vH[0]);
  }
  PRIO(2, 3);
  STAMP(8);
  FST(8);
  if (__builtin_expect(edge, 1)) {
    u32 etag = (int)xg < 0 ? 0u : tag;
    PIN(etag);
    auto bad = [&]() {
      u32 b = __builtin_amdgcn_bitop3_b32(e[0].y, e[0].w, etag, 0x7e);
#pragma unroll
      for (int a = 1; a < NS; ++a) b |= __builtin_amdgcn_bitop3_b32(e[a].y, e[a].w, etag, 0x7e);
      PIN(b);
      return __any(b != 0u);
    };
    if (bad()) {
#pragma nounroll
      for (u32 spin = 0; spin < SPIN_CAP; ++spin) {
        FENCE();
        look();
        if (!bad()) break;
      }
    }
#pragma unroll
    for (int a = 0; a < NS; ++a) { addp(t[a], e[a].x, e[a].z, uLo, uHi); PIN(t[a]); }
  }
  STAMP(6);
  FST(9);
  SB();
  float mx[4];
#pragma unroll
  for (int a = 0; a < NS; ++a) { mx[a] = vmax(vmax(t[a][0], t[a][1]), t[a][2]); PIN(mx[a]); }
#pragma unroll
  for (int a = 0; a < NS; ++a) { mx[a] = vmax(mx[a], t[a][3]); PIN(mx[a]); }
  if (NS == 3) mx[3] = mx[2];
  SB();
  {
    u32 c[2];
#pragma unroll
    for (int h = 0; h < 2; ++h) {
      auto w = __builtin_amdgcn_permlane16_swap(__float_as_uint(mx[2 * h]), __float_as_uint(mx[2 * h + 1]), false, false);
      mx[2 * h] = __uint_as_float(w[0]); mx[2 * h + 1] = __uint_as_float(w[1]);
    }
    SB();
#pragma unroll
    for (int h = 0; h < 2; ++h) c[h] = __float_as_uint(vmax(mx[2 * h], mx[2 * h + 1]));
    SB();
#pragma unroll
    for (int h = 0; h < 2; ++h) {
      if (NS == 3 && h == 1) { mx[2] = __uint_as_float(c[1]); continue; }
      auto w = __builtin_amdgcn_permlane16_swap(c[h], c[h], false, false);
      mx[2 * h] = __uint_as_float(w[0]); mx[2 * h + 1] = __uint_as_float(w[1]);
    }
    SB();
  }
#pragma unroll
  for (int a = 0; a < NS; ++a) { mx[a] *= -cq[a]; PIN(mx[a]); }
  SB();
#pragma unroll
  for (int a = 0; a < NS; ++a) { t[a] = __builtin_elementwise_fma(t[a], bc4(cq[a]), bc4(mx[a])); PIN(t[a]); }
  SB();
#pragma unroll
  for (int a = 0; a < NS; ++a) {
#pragma unroll
    for (int r = 0; r < 4; ++r) t[a][r] = __builtin_amdgcn_exp2f(t[a][r]);
    PIN(t[a]);
  }
  SB();
  FST(10);
  u2 pp[NS], p0[NS];
  bf8 p1[NS];
#pragma unroll
  for (int a = 0; a < NS; ++a) { pp[a] = pk4(t[a]); PIN(pp[a]); }
  SB();
  f4 o[NS], sm[NS];
#pragma unroll
  for (int a = 0; a < NS; ++a) {
    sm[a] = mfma16(hs, pp[a], z);
    p0[a] = and2(pp[a], m0); PIN(p0[a]);
    SB();
  }
#pragma unroll
  for (int a = 0; a < NS; ++a) {
    o[a] = mfma16(vLo[a], p0[a], z);
    if (NS == 4 && a == 0) { p0[0] = and2(pp[0], mp); PIN(p0[0]); }
    else { p1[a] = mk8(and2(pp[a], mm), and2(pp[a], mp)); PIN(p1[a]); }
    SB();
  }
#pragma unroll
  for (int a = 0; a < NS; ++a) {
    o[a] = (NS == 4 && a == 0) ? mfma16(vHi[1], p0[0], o[a]) : mfma(vH[a], p1[a], o[a]);
    mx[a] = __builtin_amdgcn_rcpf(sm[a][0]); PIN(mx[a]);
    SB();
  }
  u2 ob[4];
#pragma unroll
  for (int a = 0; a < NS; ++a) { o[a] *= mx[a]; PIN(o[a]); }
  SB();
#pragma unroll
  for (int a = 0; a < NS; ++a) { ob[a] = pk4(o[a]); PIN(ob[a]); }
  if (NS == 3) ob[3] = ob[2];
  SB();
#pragma unroll
  for (int h = 0; h < 2; ++h) {
    auto wx = __builtin_amdgcn_permlane32_swap(ob[2 * h].x, ob[2 * h + 1].x, false, false);
    auto wy = __builtin_amdgcn_permlane32_swap(ob[2 * h].y, ob[2 * h + 1].y, false, false);
    u4 v; v.x = wx[0]; v.y = wy[0]; v.z = wx[1]; v.w = wy[1];
    __builtin_amdgcn_raw_buffer_store_b128(v, rws, so[h], 0, 16);
  }
  CSTAMP(2);
  STAMP(4);
  FST(11);
  __builtin_amdgcn_s_setprio(0);
  SB();
  if (PBR2) NEW_PAGE();
  SB();
  {
    const u32 r0 = tp * 104, mq = (r0 >> 6) + (lane >> 3);
    u32 old, tv1 = tag;
    u32 fo = WS_BOX + (parity * 16 + (r0 >> 6)) * 2048 + (j >= 5 ? 64 : 0) + rb * 4 + lane * 256 + (mq * 64 < r0 ? 20 : 0);
    fo |= mq * 64 > min(r0 + 103u, 1023u) ? 0x80000000u : 0u;
    PIN(fo); PIN(tv1);
    g2.init(wave, mg, ng);
    SB();
    __builtin_amdgcn_s_setprio(3);
    asm volatile("s_mov_b64 exec, 1\n\ts_waitcnt vmcnt(0)\n\tds_add_rtn_u32 %0, %1, %2\n\ts_mov_b32 exec_lo, 0xffffff\n\ts_waitcnt lgkmcnt(0)\n\t"
                 "v_readfirstlane_b32 vcc_lo, %0\n\ts_cmp_lg_u32 vcc_lo, 7\n\ts_cbranch_scc1 1f\n\t"
                 "buffer_atomic_swap %3, %4, %5, 0 offen\n1:\n\ts_mov_b64 exec, -1"
                 : "=&v"(old) : "v"(g2.pa), "v"(g2.one), "v"(tv1), "v"(fo), "s"(srd(p.ws)) : "scc", "vcc", "memory");
  }
  STAMP(5);
  FST(12);
}

constexpr u32 A_D = 0, A_G = L_WO, A_U = L_WO + 40960;
constexpr u32 A_XN = 40960;
constexpr u32 A_RED = A_XN + 2560;
constexpr u32 A_H = A_RED + 2048;
constexpr u32 A_TOP = A_H + 1024;

DEV void aux_loader(Params& p, int lane, int lw, int a7, int f, bool ffn, int mg, int ng, int koff, Epi& ei STM_ARG) {
  const u32 ep = epoch_get(p);
  const u4 srdo = srd(p.wo), srdg = srd(p.wg), srdu = srd(p.wu), srdd = srd(p.wd);
  const int rr = lane >> 2;
  u32 vg = (lw * 16 + rr) * (F_ * 2) + f * 64 + (((lane & 3) ^ (((rr >> 3) & 1) << 1)) << 4);
  u32 vd = (f * 32 + lw * 8 + (lane >> 3)) * 1280 + (lane & 7) * 16;
  u32 vt = min((f * 4 + lw) * 30 + min(lane, 29), 6399) * 128;
  u32 ho = (lane | lw) ? 0x80000000u : WS_FLAG + (200 + f) * 256 + 128;
  PIN(vg); PIN(vd); PIN(vt); PIN(ho);
  ei.init(lane, lw, mg, ng);
  WoDma wo; wo.init(lane, lw, ng, WOSTA);
  u32 tag = epoch_tag(ep);
  PIN(tag);
  if (FFN_DELAY) __builtin_amdgcn_s_sleep(FFN_DELAY);
  STAMP(9);
  if (ffn) {
    const u4 s0 = (f & 1) ? srdu : srdg, s1 = (f & 1) ? srdg : srdu;
    const u32 a0 = ((f & 1) ? A_U : A_G) + lw * 1024, a1 = ((f & 1) ? A_G : A_U) + lw * 1024;
    dma_run<10, 4096, 64 * F_ * 2, 0, TH0>(a0, vg, s0, 0);
    asm volatile("s_mov_b32 m0, %0\n\ts_nop 0\n\tbuffer_load_dword %1, %2, 0 offen lds" :: "s"(L_DUMP + lw * 256), "v"(vt), "s"(srdo));
    STAMP(10);
    dma_run<10, 4096, 64 * F_ * 2, 0, TH1>(a1, vg, s1, 0);
    if (!DLATE) dma_run<10, 4096, 128>(A_D + lw * 1024, vd, srdd, 0);
    STAMP(1);
  } else {
    wo.issue<0, 27, TH0>(srdo, lw);
  }
  wait_bar<DLATE ? 0 : 10>();
  if (DLATE && ffn) dma_run<10, 4096, 128>(A_D + lw * 1024, vd, srdd, 0);
  asm volatile("buffer_store_dword %0, %1, %2, 0 offen sc1" :: "v"(tag), "v"(ho), "s"(srd(p.ws)));
  if (BOXW == 2) asm volatile("buffer_atomic_add %0, %1, %2, 0 offen" :: "v"(0u), "v"((lane | (lw & 2)) ? 0x80000000u : WS_BOX + (mg * 8 + ng) * 256 + lw * 64), "s"(srd(p.ws)));
  bar();
  STAMP(2);
  ei.issue(srd(p.x), srd(p.ws), WS_ST2);
  wo.issue<0, WOC, AW0, 1>(srdo, lw, f);
  asm volatile("s_cmp_gt_u32 %0, 53\n\ts_cbranch_scc1 1f\n\ts_waitcnt vmcnt(%1)\n\ts_barrier\n\ts_branch 2f\n1:\n\ts_waitcnt vmcnt(0)\n2:" :: "s"(f), "i"(WOC + 5) : "scc", "memory");
  STAMP(3);
  ei.wait<WOC>();
  ei.check(p.ws.p, tag);
  SB();
#pragma unroll
  for (int i = 0; i < YG; ++i) ei.gate(i);
  SB();
  asm volatile("buffer_load_dwordx4 %0, off, %1, 0 offset:%2 sc1" : "=v"(ei.fin) : "s"(srd(p.ws)), "i"(WS_FIN));
  wo.issue<WOC, 27, AWT, 1>(srdo, lw, f);
  asm volatile("s_cmp_gt_u32 %1, 53\n\ts_cbranch_scc0 1f\n\ts_waitcnt vmcnt(1)\n\ts_sleep %3\n1:\n\ts_waitcnt vmcnt(%2)" : "+v"(ei.fin) : "s"(f), "i"(27 - WOC), "i"(NFD) : "scc");
  STAMP(4);
}

DEV void aux_compute(const Params& p, rsrc_t rws, u32& tag, int lane, int wave, int f, bool ffn, int xcd, int a7, RowIn& rin STM_ARG) {
  const int task = a7 + 7 * wave;
  if (task < 32) rin.stats(p, rws, tag, lane, xcd * 256 + task * 8, A_TOP + wave * 16 STM);
  else tag = epoch_tag(epoch_get(p));
  PST(4);
  const int g = lane >> 4, i16 = lane & 15;
  const int q = i16 >> 2, pp = lane & 3;
  const int t = wave & 1, kq = wave >> 1;
  u32 ab = (kq * 160 + 8 * g + q) * 64 + (((2 * t + (pp >> 1)) ^ ((g & 1) << 1)) << 4) + (pp & 1) * 8;
  u32 xb = A_XN + (i16 & 1) * 1280 + (kq * 160 + 8 * g) * 2;
  u32 ar = i16 < 2 ? A_RED + (wave * 16 + g * 2 + i16) * 16 : L_NONE + lane * 16;
  u32 ad[5], ah[5];
#pragma unroll
  for (int i = 0; i < 5; ++i) {
    const int nt = wave * 5 + i;
    ad[i] = A_D + (8 * g + q) * 128 + pp * 8 + (nt >> 2) * 4096 + (nt & 3) * 32;
    ah[i] = (i16 & 7) == i ? A_H + wave * 128 + (i16 >> 3) * 64 + g * 16 : L_NONE + lane * 16;
    PIN(ad[i]); PIN(ah[i]);
  }
  u32 as = A_RED + (lane >> 5) * 256 + (lane & 31) * 4, aw = A_H + wave * 128 + ((lane >> 2) & 1) * 64 + (4 * (lane >> 3) + (lane & 3)) * 2;
  u32 yo = WS_YP + (((i16 >> 3) * 8 + wave) * 54 + f) * 512 + g * 128 + (i16 & 7) * 16;
  PIN(ab); PIN(xb); PIN(ar); PIN(as); PIN(aw); PIN(yo);
  STAMP(1);
  u32 so = 0x80000000u, xo = 0x80000000u;
  if (wave >= 5) {
    if (wave == 5) st_u(L_SYNC + lane * 4, 0);
    const u32 nt = (a7 < 4 && wave == 5) ? 5 : 4, a = A_TOP + min((u32)lane & 7, nt - 1) * 16;
    u4 k;
#pragma nounroll
    for (u32 spin = 0; spin < (1u << 20); ++spin) {
      FENCE();
      k = __builtin_bit_cast(u4, ld_f4(a));
      if (__all(k.y == tag && k.w == tag)) break;
      __builtin_amdgcn_s_sleep(1);
    }
    if (wave == 5) {
      if ((lane & 7) >= nt) k.x = k.z = 0;
      u32 k1 = k.x, k2 = k.z;
      top2_merge(k1, k2, dppu<0xB1>(k1), dppu<0xB1>(k2));
      top2_merge(k1, k2, dppu<0x4E>(k1), dppu<0x4E>(k2));
      top2_merge(k1, k2, dppu<0x141>(k1), dppu<0x141>(k2));
      k.x = k1; k.z = k2;
      bst128_sc1(rws, lane < 8 ? WS_KEY + f * 128 + lane * 16 : 0x80000000u, k);
    } else {
      __builtin_amdgcn_s_sleep(KEY_DELAY);
#pragma nounroll
      for (u32 spin = 0;; ++spin) {
        FENCE();
        k = bld128_sc1(rws, WS_KEY + min(lane, 55) * 128);
        if (__all(k.y == tag && k.w == tag) || spin > SPIN_CAP) break;
        __builtin_amdgcn_s_sleep(8);
      }
      u32 k1 = lane < 56 ? k.x : 0, k2 = lane < 56 ? k.z : 0;
      top2_wave(k1, k2);
      k.x = k1; k.z = k2;
      if (f == 55) bst128_sc1(rws, (lane | t * 8) < 8 ? WS_FIN + lane * 16 : 0x80000000u, k);
      const int tk = key_tok(__builtin_amdgcn_readfirstlane(t ? k2 : k1));
      xo = tk * 1280; so = WS_ST2 + ((tk & 1) * 1024 + (tk >> 1)) * 16;
    }
  }
  const rsrc_t rx = __builtin_amdgcn_make_buffer_rsrc(p.x.p, 0, S_ * D_ * 2, 0x00020000);
  const u4 xa = __builtin_amdgcn_raw_buffer_load_b128(rx, xo + lane * 16, 0, 0);
  const u32 xc = __builtin_amdgcn_raw_buffer_load_b32(rx, xo + 1024 + lane * 4, 0, 0);
  u4 sv = bld128_sc1(rws, so);
  FENCE();
  STAMP(2);
  NEW_PAGE();
  lds_bar();
  STAMP(3);
  bf8 Wg[5], Wu[5];
#pragma unroll
  for (int ks = 0; ks < 5; ++ks) { Wg[ks] = ld_tr8(A_G + ab + ks * 2048, 256); Wu[ks] = ld_tr8(A_U + ab + ks * 2048, 256); }
#pragma unroll
  for (int ks = 0; ks < 5; ++ks) { PIN(Wg[ks]); PIN(Wu[ks]); }
  bar();
  if (__builtin_expect(wave >= 6, 1)) {
    if (__builtin_expect(__any(__builtin_amdgcn_bitop3_b32(sv.y, sv.w, tag, 0x7e) != 0u), 0)) {
#pragma nounroll
      for (u32 spin = 0; !__all(sv.y == tag && sv.w == tag) && spin < SPIN_CAP; ++spin) {
        FENCE();
        __builtin_amdgcn_s_sleep(1);
        sv = bld128_sc1(rws, so);
      }
    }
    const float m = __uint_as_float(sv.x), r_ = __uint_as_float(sv.z);
    u4 o; o.x = norm2(xa.x, m, r_); o.y = norm2(xa.y, m, r_); o.z = norm2(xa.z, m, r_); o.w = norm2(xa.w, m, r_);
    st_u4(A_XN + t * 1280 + lane * 16, o); st_u(A_XN + t * 1280 + 1024 + lane * 4, norm2(xc, m, r_));
    lds_add(Y_CB, 1);
  }
  if (__builtin_expect(!ffn, 0)) {
    if (f == 55 && wave == 5) {
#pragma nounroll
      for (int i = 0; i < HELLO_DELAY; ++i) __builtin_amdgcn_s_sleep(8);
#pragma nounroll
      for (u32 spin = 0;; ++spin) {
        FENCE();
        u32 bad = 0;
#pragma unroll
        for (int i = 0; i < 4; ++i) bad |= __builtin_amdgcn_raw_buffer_load_b32(rws, WS_FLAG + 128 + (i * 64 + lane) * 256, 0, 16) ^ tag;
        if (__all(bad == 0u) || spin > SPIN_CAP) break;
        __builtin_amdgcn_s_sleep(16);
      }
      if (lane == 0) gst32(p.ws.p + WS_EPOCH, tag);
    }
    return;
  }
  lds_wait_ge<1>(Y_CB, 2);
  STAMP(8);
  {
    bf8 xb8[5];
#pragma unroll
    for (int ks = 0; ks < 5; ++ks) xb8[ks] = ld_b128(xb + ks * 64);
    SB();
    f4 ag = {0.f, 0.f, 0.f, 0.f}, au = {0.f, 0.f, 0.f, 0.f};
#pragma unroll
    for (int ks = 0; ks < 5; ++ks) { ag = mfma(Wg[ks], xb8[ks], ag); au = mfma(Wu[ks], xb8[ks], au); }
    SB();
    st_f4(ar, ag); st_f4(ar + 128, au);
  }
  STAMP(5);
  lds_bar();
  STAMP(6);
  {
    float a[4], b[4];
#pragma unroll
    for (int k = 0; k < 4; ++k) a[k] = ld_f(as + k * 512);
#pragma unroll
    for (int k = 0; k < 4; ++k) b[k] = ld_f(as + k * 512 + 128);
    bf8 Wd[5], hb[5];
#pragma unroll
    for (int i = 0; i < 5; ++i) Wd[i] = ld_tr8(ad[i], 512);
    SB();
    const float hh = rbf(silu_f(rbf((a[0] + a[1]) + (a[2] + a[3])))) * rbf((b[0] + b[1]) + (b[2] + b[3]));
    *(LAS u16*)(uintptr_t)aw = (u16)(pk2(hh, hh) & 0xffffu);
    FENCE();
#pragma unroll
    for (int i = 0; i < 5; ++i) hb[i] = ld_b128(ah[i]);
    SB();
    f4 y = {0.f, 0.f, 0.f, 0.f};
#pragma unroll
    for (int i = 0; i < 5; ++i) y = mfma(Wd[i], hb[i], y);
    u4 v; v.x = pk2(y[0], y[1]); v.z = pk2(y[2], y[3]); v.y = v.w = tag;
    bst128_sc1(rws, yo, v);
  }
  STAMP(4);
}

DEV void gemm2(const Params& p, rsrc_t rws, u32 tag, int wave, int ng, G2& c STM_ARG) {
  const int m = wave & 3, kh = wave >> 2;
  const u32 b0 = c.b0, oo = c.oo, fo = c.fo, ga = c.ga, one = c.one, pa = c.pa, pm = c.pm;
  u4 of[10];
  asm volatile("" : "={v[160:163]}"(of[0]));
#define POLL(R) "v_mov_b32 " R ", 0\n\tbuffer_atomic_add " R ", %[fo], %[srd], 0 offen sc0\n\t"
  GST(14);
  lds_wait_ge<1>(Y_WO, 4);
  asm volatile("s_cmp_eq_u32 %[pm], 0\n\ts_cbranch_scc1 1f\n\ts_mov_b64 exec, %[pm64]\n\t" POLL("v160") "s_mov_b64 exec, -1\n1:"
               : "+{v[160:163]}"(of[0]) : [fo] "v"(fo), [srd] "s"(srd(p.ws)), [pm] "s"(pm), [pm64] "s"((u64)pm) : "scc", "memory");
  bf8 W[WPRE][5];
  auto ldw = [&](int ks) {
#pragma unroll
    for (int n = 0; n < 5; ++n) W[ks][n] = ld_tr8(b0 + ks * 5120 + n * 32, 8 * 160);
  };
#pragma unroll
  for (int ks = 0; ks < WPR1; ++ks) ldw(ks);
  SB();
  GST(15);
  __builtin_amdgcn_s_setprio(IPRIO);
  {
    u32 n = SPIN_CAP, l, r;
#define CHK(R) "s_waitcnt vmcnt(2)\n\tv_cmp_ne_u32 vcc, %[tag], " R "\n\ts_cbranch_vccz 2f\n\t" POLL(R)
#define LSPIN(L, A, V) L ":\n\tds_read_b32 v163, " A "\n\ts_sub_u32 %1, %1, 1\n\ts_cselect_b32 %2, 0, " V "\n\ts_waitcnt lgkmcnt(0)\n\t" \
                 "v_readfirstlane_b32 %3, v163\n\ts_cmp_lt_u32 %3, %2\n\ts_cbranch_scc1 " L "b\n\t"
    asm volatile("s_lshl_b32 %1, %1, 5\n\ts_cmp_eq_u32 %[pm], 0\n\ts_cbranch_scc1 5f\n\t"
                 "s_mov_b64 exec, %[pm64]\n\t" POLL("v161") LSPIN("3", "%[pa]", "8") ".if %[gap]\n\ts_sleep %[gap]\n\t.endif\n\t" POLL("v162")
                 "s_movk_i32 %1, 0x4000\n"
                 "1:\n\t" CHK("v160") CHK("v161") CHK("v162") "s_sub_u32 %1, %1, 1\n\ts_cbranch_scc0 1b\n"
                 "2:\n\tds_write_b32 %[ga], %[one]\n\ts_mov_b64 exec, -1\n\ts_branch 4f\n"
                 LSPIN("5", "%[ga]", "1")
                 "4:\n\tbuffer_load_dwordx4 v[160:163], %[oo], %[srd], 0 offen sc1"
                 : "+{v[160:163]}"(of[0]), "+s"(n), "=&s"(l), "=&s"(r)
                 : [fo] "v"(fo), [srd] "s"(srd(p.ws)), [tag] "s"(tag), [ga] "v"(ga), [one] "v"(one), [pm] "s"(pm), [pm64] "s"((u64)pm), [gap] "i"(PGAP),
                   [pa] "v"(pa), [oo] "v"(oo)
                 : "scc", "vcc", "memory");
  }
  GST(0);
  STAMP(7);
#pragma unroll
  for (int ks = 0; ks < OPRE; ++ks) {
    if (ks) of[ks] = __builtin_amdgcn_raw_buffer_load_b128(rws, oo, ks * 1024, 16);
    SB();
    constexpr int NW = (WPRE - WPR1) * 5;
#pragma unroll
    for (int i = ks * NW / OPRE; i < (ks + 1) * NW / OPRE; ++i) W[WPR1 + i / 5][i % 5] = ld_tr8(b0 + (WPR1 + i / 5) * 5120 + (i % 5) * 32, 8 * 160);
    if (OSLP) __builtin_amdgcn_s_sleep(OSLP);
    SB();
  }
  __builtin_amdgcn_s_setprio(MPRIO);
  u32 sfa = Y_S + m * 4, sfv = kh + 1;
  SPIN(sfa); SPIN(sfv);
  asm volatile("s_mov_b32 m0, %0" :: "s"(wave * ST_W));
  asm volatile("s_waitcnt vmcnt(%1)" : "+{v[160:163]}"(of[0]) : "i"(OPRE - 1));
  SB();
  u32 fa = sfa, fv = sfv;
  GST(1);
  f4 acc[5];
#pragma unroll
  for (int n = 0; n < 5; ++n) acc[n] = f4{0.f, 0.f, 0.f, 0.f};
  constexpr int NR = (10 - WPRE) * 10;
  u2 Wh[NR];
  auto kn = [](int i, int& ks, int& n) { if (i < 40) { ks = i / 5; n = i % 5; } else { ks = 8 + (i & 1); n = (i - 40) >> 1; } };
  auto rdh = [&](int i) { int ks, n; kn(WPRE * 5 + (i >> 1), ks, n); if (i >= 0 && i < NR) Wh[i] = ld_tr(b0 + ks * 5120 + n * 32 + (i & 1) * 1280); };
  auto put = [&](int j) { asm volatile("ds_write_addtid_b32 %0 offset:%1" :: "v"(acc[j >> 2][j & 3]), "i"((j >> 2) * ST_N + (j & 3) * 256) : "memory"); };
#pragma unroll
  for (int i = 0; i < 50; ++i) {
    int ks, n; kn(i, ks, n);
    acc[n] = mfma(__builtin_bit_cast(bf8, of[ks]), i < WPRE * 5 ? W[ks][n] : mk8(Wh[2 * (i - WPRE * 5)], Wh[2 * (i - WPRE * 5) + 1]), acc[n]);
    const int j = i - RD0;
    rdh(j + j / RDX); if (j >= 0 && j % RDX == RDX - 1) rdh(j + j / RDX + 1);
    if (i == 41) PIN(fa);
    if (i == 42) { PIN(fv); PIN(Wh[NR - 1]); }
    SB();
    if (i >= 43) { put(2 * (i - 43)); put(2 * (i - 43) + 1); SB(); }
    if (!ARR && i % 5 == 4) GST(2 + i / 5);
  }
  put(14); put(15);
  asm volatile("s_nop 3");
#pragma unroll
  for (int j = 16; j < 20; ++j) put(j);
  lds_set(fa, fv);
  GST(12);
  lds_bar();
  GST(13);
}

DEV void epilogue(const Params& p, int lane, int m, int mg, int ng, bool is_main, Epi& ei STM_ARG) {
  const rsrc_t rws = __builtin_amdgcn_make_buffer_rsrc(p.ws.p, 0, WS_END, 0x00020000);
  const u32 tag = __builtin_amdgcn_readfirstlane(ei.sv.y);
  u4 fin = ei.fin;
  float (&gf)[20] = ei.gf;
  f2 xf[10], g2[10];
#pragma unroll
  for (int i = is_main ? 0 : YG; i < (is_main ? GSPLIT : YG2); ++i) ei.gate(i);
  if (__builtin_expect(!(fin.y == tag && fin.w == tag), 0)) {
#pragma nounroll
    for (u32 spin = 0; !(fin.y == tag && fin.w == tag) && spin < SPIN_CAP; ++spin) {
      FENCE();
      __builtin_amdgcn_s_sleep(2);
      fin = bld128_sc1(rws, WS_FIN);
    }
  }
  const u32 k1 = __builtin_amdgcn_readfirstlane(fin.x), k2 = __builtin_amdgcn_readfirstlane(fin.z);
  u32 tk[2], tl[2];
#pragma unroll
  for (int c = 0; c < 2; ++c) { tk[c] = key_tok(c ? k2 : k1); tl[c] = (((tk[c] & 1) << 6) | (tk[c] >> 5)) - mg * 4; }
  const u32 g = lane >> 4, i16 = lane & 15, pt = (i16 * 13) >> 6, qd = (i16 - pt * 5) * 4 + g;
  u32 yo = (WS_YP + (ng * 54 + m * 15 + pt) * 512 + g * 128 + (qd >> 2) * 16) | (pt == 3 ? 0x80000000u : 0u), xo = ng * 160 + qd * 8;
  u32 yo2 = m == 3 ? 0x80000000u : yo + 4608;
  PIN(yo); PIN(xo); PIN(yo2);
  u4 w[6]; u2 xr;
  u32 c = tl[0] > 3;
  u32 co, so_, ro, tc;
  auto sel = [&]() {
    const u32 t = c ? tk[1] : tk[0];
    co = c * (8 * 54 * 512); so_ = WS_ST2 + ((t & 1) * 1024 + (t >> 1)) * 16; ro = t * 1280; tc = c ? tl[1] : tl[0];
    SPIN(co); SPIN(so_); SPIN(ro); SPIN(tc);
  };
  auto fetch = [&]() {
    asm volatile("s_cmp_gt_u32 %[tl], 3\n\ts_cbranch_scc1 1f\n\t"
                 "buffer_load_dwordx4 %0, %[yo], %[ws], %[co] offen sc1\n\tbuffer_load_dwordx4 %1, %[yo], %[ws], %[co] offen offset:1536 sc1\n\t"
                 "buffer_load_dwordx4 %2, %[yo], %[ws], %[co] offen offset:3072 sc1\n\tbuffer_load_dwordx4 %3, %[y2], %[ws], %[co] offen sc1\n\t"
                 "buffer_load_dwordx4 %4, %[y2], %[ws], %[co] offen offset:1536 sc1\n\tbuffer_load_dwordx4 %5, off, %[ws], %[so] sc1\n\t"
                 "buffer_load_dwordx2 %6, %[xo], %[x], %[ro] offen\n1:"
                 : "=&v"(w[0]), "=&v"(w[1]), "=&v"(w[2]), "=&v"(w[3]), "=&v"(w[4]), "=&v"(w[5]), "=&v"(xr)
                 : [yo] "v"(yo), [y2] "v"(yo2), [xo] "v"(xo), [ws] "s"(srd(p.ws)), [x] "s"(srd(p.x)), [co] "s"(co), [so] "s"(so_), [ro] "s"(ro), [tl] "s"(tc) : "scc");
  };
  auto landed = [&]() { asm volatile("s_waitcnt vmcnt(0)" : "+v"(w[0]), "+v"(w[1]), "+v"(w[2]), "+v"(w[3]), "+v"(w[4]), "+v"(w[5]), "+v"(xr) :: "memory"); };
  sel();
  SB();
  wait_vm<0>();
  lds_add(Y_WO, 1);
  STAMP(5);
  fetch();
  SB();
  STAMP(6);
#pragma unroll
  for (int i = is_main ? GSPLIT : YG2; i < (is_main ? GS2 : 20); ++i) ei.gate(i);
  SB();
  landed();
#pragma nounroll
  for (; c < 2 && (c ? tl[1] : tl[0]) < 4; ++c, w[0].y = ~tag) {
    __builtin_amdgcn_s_setprio(3);
    const u32 ya = i16 < 5 ? L_Y + c * 1280 + qd * 16 : L_NONE + lane * 16;
    u32 e0 = pt == 3 ? 0u : tag, e1 = m == 3 ? 0u : e0, uLo = 0x3f80u, uHi = 0x3f800000u;
    PIN(uLo); PIN(uHi);
    auto bad = [&]() {
      u32 b = 0;
#pragma unroll
      for (int j = 0; j < 5; ++j) b |= __builtin_amdgcn_bitop3_b32(w[j].y, w[j].w, j < 3 ? e0 : e1, 0x7e);
      PIN(b);
      return __any(b != 0u);
    };
    if (__builtin_expect(bad(), 0)) {
#pragma nounroll
      for (u32 spin = 0; spin < SPIN_CAP; ++spin) {
        __builtin_amdgcn_s_sleep(4);
        sel(); fetch(); landed();
        if (!bad()) break;
      }
    }
    f4 y = {0.f, 0.f, 0.f, 0.f}, h;
#pragma unroll
    for (int j = 0; j < 5; ++j) { y[0] = dot2(w[j].x, uLo, y[0]); y[1] = dot2(w[j].x, uHi, y[1]); y[2] = dot2(w[j].z, uLo, y[2]); y[3] = dot2(w[j].z, uHi, y[3]); }
#pragma unroll
    for (int e = 0; e < 4; ++e) { float a = y[e] + dppf<0x105>(y[e]); PIN(a); h[e] = a; }
#pragma unroll
    for (int e = 0; e < 4; ++e) { float a = h[e] + dppf<0x10a>(y[e]); PIN(a); y[e] = a; }
    st_f4(ya + m * 320, y);
    lds_add(Y_Y + c * 4, 1);
    if ((c ? tl[1] : tl[0]) != m) continue;
    float sg = __builtin_amdgcn_rcpf(1.f + __expf(-key_r(c ? k2 : k1)));
    const float mu = __uint_as_float(w[5].x), rs = __uint_as_float(w[5].z);
    float xn[4] = {bflo(xr.x), bfhi(xr.x), bflo(xr.y), bfhi(xr.y)};
#define EACH(V, X) _Pragma("unroll") for (int e = 0; e < 4; ++e) { V[e] = X; PIN(V[e]); }
    EACH(xn, xn[e] - mu) EACH(xn, xn[e] * rs)
    PIN(sg);
    lds_wait_ge<0>(Y_Y + c * 4, 4);
    f4 z[4];
#pragma unroll
    for (int j = 0; j < 4; ++j) z[j] = ld_f4(ya + j * 320);
    float a[4], b[4];
    EACH(a, z[0][e] + z[1][e]) EACH(b, z[2][e] + z[3][e]) EACH(a, a[e] + b[e]) EACH(a, fmaf(a[e], sg, xn[e]))
    EACH(b, a[e] * -1.4426950408889634f) EACH(b, __builtin_amdgcn_exp2f(b[e])) EACH(b, 1.f + b[e]) EACH(b, __builtin_amdgcn_rcpf(b[e])) EACH(a, a[e] * b[e])
    y = f4{a[0], a[1], a[2], a[3]};
    st_f4(ya, y);
  }
  STAMP(10);
#pragma unroll
  for (int i = is_main ? GS2 : 20; i < 20; ++i) ei.gate(i);
  const u32 row = Epi::row(lane), odd = ng & 1;
  u32 sadr[2];
  sadr[1] = m * ST_W + (row & 3) * 256 + (row >> 2) * 64;
  sadr[0] = sadr[1] + (odd + ((lane >> 1) & 1)) * ST_N + (lane & 1) * 32;
  sadr[1] += (odd ? 0 : 4 * ST_N) + (lane & 3) * 16;
  PIN(sadr[0]); PIN(sadr[1]);
  SB();
#pragma nounroll
  for (c = 0; c < 2; ++c) {
    if ((c ? tl[1] : tl[0]) != m) continue;
    FENCE();
    const u32 ga = L_Y + c * 1280 + odd * 64 + (lane & 3) * 32, hit = row == (((c ? tk[1] : tk[0]) >> 1) & 15) ? ~0u : 0u;
#pragma unroll
    for (int j = 0; j < 5; ++j) {
      const f4 gr = ld_f4(j < 4 ? ga + (j >> 1) * 128 + (j & 1) * 16 : L_Y + c * 1280 + (odd ? 0 : 256) + (lane & 3) * 16);
#pragma unroll
      for (int e = 0; e < 4; ++e) gf[4 * j + e] = fsel(hit, gr[e], gf[4 * j + e]);
    }
  }
#pragma unroll
  for (int e = 0; e < 10; ++e) { xf[e] = f2{bflo(ei.xw(e)), bfhi(ei.xw(e))}; g2[e] = f2{gf[2 * e], gf[2 * e + 1]}; PIN(xf[e]); PIN(g2[e]); }
  STAMP(7);
  __builtin_amdgcn_s_setprio(LPRIO);
  u4 so = srd(p.out);
  u32 fa = Y_S + m * 4, first;
  SPIN(so); PIN(fa);
  GST(1);
  {
    u32 n = 1u << 20, t, l;
    asm volatile("1:\n\ts_sleep 2\n\tds_read_b32 %1, %4\n\ts_sub_u32 %0, %0, 1\n\ts_cselect_b32 %2, 0, 1\n\ts_waitcnt lgkmcnt(0)\n\t"
                 "v_readfirstlane_b32 %3, %1\n\ts_cmp_lt_u32 %3, %2\n\ts_cbranch_scc1 1b"
                 : "+s"(n), "=&v"(t), "=&s"(l), "=&s"(first) : "v"(fa) : "scc", "memory");
  }
  GST(2);
  f4 pa[5], pb[5];
  auto rd = [&](f4 (&d)[5], u32 a0, u32 a1) {
#pragma unroll
    for (int j = 0; j < 5; ++j) d[j] = ld_f4(j < 4 ? a0 + (j >> 1) * 2 * ST_N + (j & 1) * 16 : a1);
  };
  u32 o0 = (first - 1) * (4 * ST_W), o1 = 4 * ST_W - o0;
  SPIN(o0); SPIN(o1);
  rd(pa, sadr[0] + o0, sadr[1] + o0);
  SB();
  sadr[0] += o1; sadr[1] += o1;
  PIN(sadr[0]); PIN(sadr[1]);
#pragma unroll
  for (int j = 0; j < 5; ++j) PIN(pa[j]);
  GST(3);
  __builtin_amdgcn_s_setprio(3);
  bar();
  GST(4);
  rd(pb, sadr[0], sadr[1]);
  SB();
  auto fin2 = [&](int e) {
    const f4 s4 = pa[e >> 1] + pb[e >> 1];
    const f2 r = __builtin_elementwise_fma(f2{s4[2 * (e & 1)], s4[2 * (e & 1) + 1]}, g2[e], xf[e]);
    return pk2(r.x, r.y);
  };
  u4 o[2]; u2 oh;
#pragma unroll
  for (int ps = 0; ps < 2; ++ps) {
#pragma unroll
    for (int e = 0; e < 4; ++e) o[ps][e] = fin2(4 * ps + e);
    asm volatile("buffer_store_dwordx4 %0, %1, %2, 0 offen offset:%3 " OUTPOL :: "v"(o[ps]), "v"(ei.goff[0]), "s"(so), "i"(ps * 64) : "memory");
    SB();
  }
  oh.x = fin2(8); oh.y = fin2(9);
  asm volatile("buffer_store_dwordx2 %0, %1, %2, 0 offen " OUTPOL :: "v"(oh), "v"(ei.goff[1]), "s"(so), "v"(o[0]), "v"(o[1]) : "memory");
  GST(5);
  STAMP(8);
}

#define NTW 2
#define NTX 2
#define PUT_STAMPS(m) do {} while (0)
#define STR_(x) #x
#define STR(x) STR_(x)
#define ROLE extern "C" __global__ __launch_bounds__(768, 3) void
#define PAGE __attribute__((aligned(4096)))
#define STAMP0 ""
#define STAMP_INIT() do {} while (0)

#define ROWS3(W, X0, X1) \
    "buffer_load_dwordx4 " W ", v3, s[8:11], s15 offen\n\tbuffer_load_dwordx4 " X0 ", v4, s[4:7], s15 offen\n\t" \
    "buffer_load_dwordx4 " X1 ", v5, s[4:7], s15 offen\n\ts_addk_i32 s15, 256\n\ts_cmpk_eq_u32 s15, 1280\n\ts_cselect_b32 s15, 0, s15\n\t"
ROLE PAGE fused_kernel(Params p) {
  asm volatile(
    STAMP0
    "s_load_dword s94, s[0:1], 0\n\t"
    "s_load_dword s95, s[0:1], 64\n\t"
    "v_readfirstlane_b32 s3, v0\n\t"
    "s_cmpk_ge_u32 s2, 200\n\t"
    "s_cbranch_scc1 3f\n\t"
    "s_cmpk_lt_u32 s3, 256\n\t"
    "s_cbranch_scc1 role_c4\n\t"
    "s_cmpk_lt_u32 s3, 512\n\t"
    "s_cbranch_scc1 role_c3\n\t"
    "s_setprio 3\n\t"
    "s_load_dwordx8 s[4:11], s[0:1], 0\n\t"
    "s_bfe_u32 s12, s3, 0x20006\n\t"
    "s_lshr_b32 s13, s2, 3\n\t"
    "s_mul_i32 s14, s13, 13\n\t"
    "s_lshr_b32 s14, s14, 6\n\t"
    "s_mul_i32 s15, s14, 5\n\t"
    "s_sub_u32 s13, s13, s15\n\t"
    "s_add_u32 s15, s13, s14\n\t"
    "s_sub_u32 s16, s15, 5\n\t"
    "s_min_u32 s15, s15, s16\n\t"
    "s_bitcmp1_b32 s15, 1\n\t"
    "s_cselect_b64 vcc, -1, 0\n\t"
    "s_and_b32 s16, s15, 1\n\t"
    "s_lshl_b32 s17, s2, 1\n\t"
    "s_and_b32 s17, s17, 12\n\t"
    "s_lshr_b32 s17, " STR(KOFF) ", s17\n\t"
    "s_and_b32 s17, s17, 6\n\t"
    "s_add_u32 s16, s16, s17\n\t"
    "s_bitcmp1_b32 s2, 1\n\t"
    "s_cselect_b32 s17, 5, 0\n\t"
    "s_add_u32 s14, s14, s17\n\t"
    "s_bitcmp1_b32 s2, 0\n\t"
    "s_cselect_b32 s17, 5, 0\n\t"
    "s_add_u32 s13, s13, s17\n\t"
    "s_mul_i32 s18, s14, 13\n\t"
    "s_add_u32 s18, s18, s12\n\t"
    "s_sub_u32 s18, s18, 1\n\t"
    "s_mulk_i32 s18, 20480\n\t"
    "s_add_u32 s19, s18, 81920\n\t"
    "s_add_u32 s20, s18, 163840\n\t"
    "s_add_u32 s21, s18, 245760\n\t"
    "s_min_u32 s21, s21, 2600960\n\t"
    "s_max_i32 s18, s18, 0\n\t"
    "s_cmp_lt_u32 s12, 2\n\t"
    "s_cselect_b64 s[22:23], -1, 0\n\t"
    "s_mov_b64 s[24:25], -1\n\t"
    "s_cmp_eq_u32 s12, 0\n\t"
    "s_cmov_b32 s24, 0xffffff00\n\t"
    "s_lshl_b32 s26, s12, 10\n\t"
    "s_add_u32 s26, s26, 116736\n\t"
    "s_mov_b32 s27, 122880\n\t"
    "s_bfe_u32 s17, s2, 0x10002\n\t"
    "s_mul_i32 s17, s17, 10\n\t"
    "s_add_u32 s17, s17, s16\n\t"
    "s_lshl3_add_u32 s28, s16, s12\n\t"
    "s_mulk_i32 s28, 240\n\t"
    "s_add_u32 s28, s28, s13\n\t"
    "s_lshl_b32 s29, s12, 2\n\t"
    "v_bfe_u32 v3, v0, 3, 3\n\t"
    "v_xor_b32 v4, s29, v0\n\t"
    "v_lshrrev_b32 v5, 1, v3\n\t"
    "v_bitop3_b32 v5, v4, v5, 7 bitop3:0x28\n\t"
    "v_and_b32 v6, 2, v3\n\t"
    "v_bitop3_b32 v6, v4, v6, 7 bitop3:0x28\n\t"
    "v_mad_u32_u24 v1, v3, 20, s17\n\t"
    "v_mad_u32_u24 v2, v3, 30, s28\n\t"
    "v_lshl_or_b32 v1, v1, 3, v5\n\t"
    "v_lshl_or_b32 v2, v2, 3, v6\n\t"
    "v_lshlrev_b32 v1, 4, v1\n\t"
    "v_lshlrev_b32 v2, 4, v2\n\t"
    "s_waitcnt lgkmcnt(0)\n\t"
    "s_cbranch_vccnz 5f\n"
    "4:\n\t"
    HALF_A("s26", "v1", "v2", "s[4:7]", "s[8:11]", "s18", "s21", "s[24:25]", "s[22:23]")
    "s_cbranch_vccnz role_ml\n"
    "5:\n\t"
    HALF_B("s26", "v1", "v2", "s[4:7]", "s[8:11]", "s19", "s20", "s27")
    "s_cbranch_vccnz 4b\n\t"
    "s_branch role_ml\n"
    "3:\n\t"
    "s_cmpk_ge_u32 s3, 512\n\t"
    "s_cbranch_scc1 role_al\n\t"
    "s_load_dwordx4 s[4:7], s[0:1], 0\n\t"
    "s_load_dwordx4 s[8:11], s[0:1], 64\n\t"
    "s_load_dwordx2 s[16:17], s[0:1], 32\n\t"
    "s_getpc_b64 s[12:13]\n"
    "2:\n\t"
    "s_lshr_b32 s3, s3, 6\n\t"
    "s_and_b32 s14, s2, -8\n\t"
    "s_add_u32 s14, s14, s3\n\t"
    "s_addk_i32 s14, -200\n\t"
    "s_mulk_i32 s14, 1280\n\t"
    "v_and_b32 v1, 63, v0\n\t"
    "v_min_u32 v2, 9, v1\n\t"
    "v_lshl_add_u32 v2, v2, 7, s14\n\t"
    "v_min_u32 v2, code_end - 2b, v2\n\t"
    "global_load_dword v167, v2, s[12:13]\n\t"
    "s_mul_i32 s3, s3, 7\n\t"
    "s_lshr_b32 s14, s2, 3\n\t"
    "s_add_u32 s3, s3, s14\n\t"
    "s_sub_u32 s3, s3, 25\n\t"
    "s_and_b32 s14, s2, 3\n\t"
    "s_lshl_b32 s14, s14, 5\n\t"
    "s_add_u32 s14, s14, s3\n\t"
    "s_bfe_u32 s15, s2, 0x10002\n\t"
    "s_lshl4_add_u32 s14, s14, s15\n\t"
    "s_mulk_i32 s14, 1280\n\t"
    "s_lshl_b32 s15, s2, 1\n\t"
    "s_and_b32 s15, s15, 12\n\t"
    "s_lshr_b32 s15, " STR(KOFF) ", s15\n\t"
    "s_and_b32 s15, s15, 6\n\t"
    "s_lshl_b32 s15, s15, 7\n\t"
    "s_addk_i32 s15, 256\n\t"
    "v_and_b32 v2, 3, v0\n\t"
    "v_and_b32 v3, 48, v0\n\t"
    "v_lshlrev_b32 v3, 2, v3\n\t"
    "v_lshl_or_b32 v3, v2, 4, v3\n\t"
    "v_bfe_u32 v2, v0, 2, 2\n\t"
    "v_mul_u32_u24 v2, 2560, v2\n\t"
    "v_add3_u32 v4, v2, v3, s14\n\t"
    "v_add_u32 v5, 10240, v4\n\t"
    "s_cmp_ge_u32 s3, 32\n\t"
    "s_waitcnt lgkmcnt(0)\n\t"
    "s_load_dword s94, s[16:17], 0\n\t"
    "s_cbranch_scc1 role_ac\n\t"
    ROWS3("v[100:103]", "v[104:107]", "v[108:111]") ROWS3("v[112:115]", "v[116:119]", "v[120:123]") ROWS3("v[124:127]", "v[128:131]", "v[132:135]")
    ROWS3("v[136:139]", "v[140:143]", "v[144:147]") ROWS3("v[148:151]", "v[152:155]", "v[156:159]")
    "s_branch role_ac"
    :: "s"(__builtin_amdgcn_kernarg_segment_ptr()), "s"(__builtin_amdgcn_workgroup_id_x()), "v"(__builtin_amdgcn_workitem_id_x())
    : "v167", "s99", "vcc", "scc", "memory");
}

struct Ids {
  int lane, wave, xcd, r, mg, ng, ra, rb, rt, j, a7, f, koff;
  DEV Ids() {
    lane = threadIdx.x & 63; wave = __builtin_amdgcn_readfirstlane(threadIdx.x >> 6);
    const int b = blockIdx.x;
    xcd = b & 7; r = b >> 3;
    mg = xcd * 4 + (r >> 3); ng = r & 7;
    ra = (r * 13) >> 6; rb = r - 5 * ra;
    rt = (xcd >> 1) * 5 + ra; j = (xcd & 1) * 5 + rb;
    a7 = r - 25;
    f = a7 < 6 ? 2 * (xcd + 8 * (a7 >> 1)) + (a7 & 1) : 48 + ((FPERM >> (4 * xcd)) & 7);
    koff = (KOFF >> ((xcd >> 1) * 4)) & 6;
  }
};

template <int NS>
DEV void main_wave(Params& p) {
  STAMP_INIT();
  const Ids id;
  const int lane = id.lane, wave = id.wave;
  const rsrc_t rws = __builtin_amdgcn_make_buffer_rsrc(p.ws.p, 0, WS_END, 0x00020000);
  u32 tag;
  G2 g2;
  main_compute<NS>(p, rws, tag, lane, wave, ((id.xcd >> 1) & 1) * 5 + id.ra, id.xcd >> 2, id.rb, id.j, id.mg, id.ng, g2 STM);
  __builtin_amdgcn_s_setprio(0);
  gemm2(p, rws, tag, wave, id.ng, g2 STM);
  PUT_STAMPS(true);
}
ROLE role_ml(Params p) {
  LdIn in; in.take(); SB();
  STAMP_INIT();
  const Ids id;
  const int lane = id.lane, wave = id.wave;
  Epi ei;
  main_loader(p, in, lane, id.xcd >> 2, id.mg, id.ng, id.koff, ei STM);
  epilogue(p, lane, in.lw, id.mg, id.ng, true, ei STM);
  PUT_STAMPS(true);
}
ROLE PAGE role_c3(Params p) { main_wave<3>(p); }
ROLE role_c4(Params p) { main_wave<4>(p); }
ROLE PAGE role_al(Params p) {
  STAMP_INIT();
  const Ids id;
  const int lane = id.lane, wave = id.wave, lw = wave - 8;
  Epi ei;
  aux_loader(p, lane, lw, id.a7, id.f, id.f < 54, id.mg, id.ng, id.koff, ei STM);
  epilogue(p, lane, lw, id.mg, id.ng, false, ei STM);
  PUT_STAMPS(false);
}
ROLE PAGE __attribute__((amdgpu_num_vgpr(160))) role_ac(Params p) {
  RowIn rin; rin.take(); SB();
  STAMP_INIT();
  const Ids id;
  const int lane = id.lane, wave = id.wave;
  const rsrc_t rws = __builtin_amdgcn_make_buffer_rsrc(p.ws.p, 0, WS_END, 0x00020000);
  u32 tag;
  aux_compute(p, rws, tag, lane, wave, id.f, id.f < 54, id.xcd, id.a7, rin STM);
  lds_add(Y_PUB, 1);
  G2 g2;
  g2.init(wave, id.mg, id.ng);
  gemm2(p, rws, tag, wave, id.ng, g2 STM);
  PUT_STAMPS(false);
}
ROLE code_end(Params p) {}

extern "C" void kernel_launch(void* const* d_in, const int* in_sizes, int n_in,
                              void* d_out, int out_size, void* d_ws, size_t ws_size,
                              hipStream_t stream) {
  auto buf = [](void* q, u32 bytes) { Buf r; r.p = (char*)q; r.bytes = bytes; r.flags = 0x00020000u; return r; };
  Params p;
  p.x = buf(d_in[0], S_ * D_ * 2); p.wqkv = buf(d_in[1], D_ * 3 * D_ * 2); p.wo = buf(d_in[2], D_ * D_ * 2); p.wr = buf(d_in[4], D_ * 2);
  p.wg = buf(d_in[5], D_ * F_ * 2); p.wu = buf(d_in[6], D_ * F_ * 2); p.wd = buf(d_in[7], F_ * D_ * 2);
  p.out = buf(d_out, S_ * D_ * 2); p.ws = buf(d_ws, WS_END);
  (void)hipFuncSetAttribute((const void*)fused_kernel, hipFuncAttributeMaxDynamicSharedMemorySize, LDS_BYTES);
  fused_kernel<<<256, 768, LDS_BYTES, stream>>>(p);
}
```

```cpp
#include <hip/hip_runtime.h>
#include <stdint.h>

typedef unsigned short u16;
typedef unsigned int u32;
typedef unsigned long long u64;
typedef __attribute__((ext_vector_type(4))) float f4;
typedef __attribute__((ext_vector_type(2))) float f2;
typedef __attribute__((ext_vector_type(4))) unsigned u4;
typedef __attribute__((ext_vector_type(2))) unsigned u2;
typedef __attribute__((ext_vector_type(8))) __bf16 bf8;
typedef __attribute__((ext_vector_type(2))) __bf16 bf2;
typedef __attribute__((ext_vector_type(4))) short s4;
#define LAS __attribute__((address_space(3)))
#define GAS __attribute__((address_space(1)))
#define DEV __device__ __forceinline__

constexpr int S_ = 2048, D_ = 640, F_ = 1728;

constexpr u32 WS_EPOCH = 0;
constexpr u32 WS_FIN = 256;
constexpr u32 WS_KEY = 24576;
constexpr u32 WS_GR = 40960;
constexpr u32 WS_YP = 8u << 20;
constexpr u32 WS_ST2 = 448u << 10;
constexpr u32 WS_FLAG = 512u << 10;
constexpr u32 WS_BOX = 640u << 10;
constexpr u32 WS_O = 1u << 20;
constexpr u32 WS_XS = 4u << 20;
constexpr u32 WS_END = WS_YP + 40 * 56 * 128;
constexpr u32 WS_STAMP = 16u << 20;

constexpr u32 SLOT = 38912;
constexpr u32 XB = 0, WB = 14336;
constexpr u32 MISC = 4 * SLOT;
constexpr u32 L_CS = MISC;
constexpr u32 L_SYNC = MISC + 768;
constexpr u32 L_MR = MISC + 1024;
constexpr u32 L_DUMP = MISC + 2048;
constexpr u32 L_Y = MISC + 3072;
constexpr u32 L_HV = MISC + 6144;
constexpr u32 LDS_BYTES = 163840;
constexpr u32 Y_Y = L_SYNC;
constexpr u32 Y_PUB = L_SYNC + 8;
constexpr u32 Y_WO = L_SYNC + 12;
constexpr u32 Y_CB = L_SYNC + 16;
constexpr u32 Y_ST = L_SYNC + 32;
constexpr u32 Y_S = L_SYNC + 128;
constexpr u32 Y_F = L_SYNC + 64;
constexpr u32 L_NONE = 0x30000;
constexpr u32 L_X = SLOT;
constexpr u32 L_HK = L_X + 21 * 512;
constexpr u32 L_CQ = L_HK + 2048;
constexpr u32 L_WO = L_CQ + 512;
constexpr u32 ST_N = 1040, ST_W = 5 * ST_N;
static_assert(L_WO + 103680 == MISC + 256 && 8 * ST_W <= L_WO, "lds map");

#define KD 44
constexpr int KEY_DELAY = KD;
constexpr int HELLO_DELAY = 4;
constexpr int POLL_SLEEP = 1;
#define WOA_ 14
#define WO7_ 5
constexpr int WO7 = WO7_, WOA = WOA_;
#define GSP 0
constexpr int GSPLIT = GSP;
#define OPRE_ 10
#define WPRE 6
#define RD0 5
#define RDX 6
#define STL 5
#define EBAR 1
#define WPR1 6
#define PGAP 2
#define LPRIO 2
#define IPRIO 2
#define BOXW 2
#define MPRIO 0
#define RDLY 0
#define KSLP 6
#define AWO 1
#define AWT 63
#define AW0 63
#define YG2 12
#define DLATE 0
#define GS2 0
#define NFD 0
#define PBR2 1
#define WEXP 0
#define WDLY 0
#define GWT 0
#define GW0 4
#define PWM 0x21
#define PR3 2
#define PR2 2
#define Q2I 2
#define Q2O 2
#define Q3I 2
#define Q3O 2
#define PFAIR 0
#define PK0 1
#define PK1 3
#define PK2 5
#define WOB 20
#define PPR 3
#define RPM 3
#define RPM4 2
#define KSC 1
#define QSC 0
#define PFT 0
#define PFL 0
#define LDP 0
#define STP 1
#define S8D 3
#define XST 1
#define EFT 1
#define WOF 22
#define YTCH 1
#define PCI 4
#define STR_(x) #x
#define STR(x) STR_(x)
#define LGAP 7
#define PDL 0
#define PC0 6
#define AWW 4
#define LPOLL 1
#define WOP 27
#define WLD 10
#define GW 3
#define OSLP 0
#define ARR 0
constexpr int OPRE = OPRE_;
#define PADC4 "1"
#define NOTOUCH
#define KOFF 0x6420
#define TX0 10
#define INIT1 1
#define TH0 3
#define TH1 3
#define FFN_DELAY 12
#define WOC 11
#define YG 0
#define SSLP 1
#define FPERM 0x54362107
#define OUTP 0
#define OUTPOL ""
#define PBRK 1
#define WOST 0
#define WOSTA 0
#define WTH 6
#define WTH2 4
#define PACE 0
#define PACE0 1
#define MORD 0
#define REVM 0xc
#define KORD 12
constexpr u64 KT0 = 0x9786453120, KT1 = 0x8975642301, KT2 = KT0, KT3 = KT1, KT4 = 0x9867534210;
#define SCAP 15
constexpr u32 SPIN_CAP = 1u << SCAP;

struct Buf { char* p; u32 bytes, flags; };
struct Params { Buf x, wqkv, ws, wo, wr, out, wg, wu, wd; };

DEV float bflo(u32 v) { return __uint_as_float(v << 16); }
DEV float bfhi(u32 v) { return __uint_as_float(v & 0xffff0000u); }
DEV u32 pk2(float a, float b) {
  bf2 r; r[0] = (__bf16)a; r[1] = (__bf16)b;
  return __builtin_bit_cast(u32, r);
}
DEV float rbf(float a) { return bflo(pk2(a, a) & 0xffffu); }
DEV u4 srd(const Buf& b) { return __builtin_bit_cast(u4, b); }
template <int N> DEV void wait_vm() { asm volatile("s_waitcnt vmcnt(%0)" :: "i"(N) : "memory"); }
template <int N> DEV void wait_bar() { asm volatile("s_waitcnt vmcnt(%0) lgkmcnt(0)\n\ts_barrier" :: "i"(N) : "memory"); }
DEV void lds_bar() { asm volatile("s_waitcnt lgkmcnt(0)\n\ts_barrier" ::: "memory"); }

DEV bf8 ld_b128(u32 a) { return *(const LAS bf8*)(uintptr_t)a; }
DEV u2 ld_u2(u32 a) { return *(const LAS u2*)(uintptr_t)a; }
DEV f4 ld_f4(u32 a) { return *(const LAS f4*)(uintptr_t)a; }
DEV float ld_f(u32 a) { return *(const LAS float*)(uintptr_t)a; }
DEV u32 ld_u(u32 a) { return *(const LAS u32*)(uintptr_t)a; }
DEV void st_u4(u32 a, u4 v) { *(LAS u4*)(uintptr_t)a = v; }
DEV void st_u2(u32 a, u2 v) { *(LAS u2*)(uintptr_t)a = v; }
DEV void st_f4(u32 a, f4 v) { *(LAS f4*)(uintptr_t)a = v; }
DEV void st_f(u32 a, float v) { *(LAS float*)(uintptr_t)a = v; }
DEV void st_u(u32 a, u32 v) { *(LAS u32*)(uintptr_t)a = v; }
DEV u2 ld_tr(u32 a) {
  return __builtin_bit_cast(u2, __builtin_amdgcn_ds_read_tr16_b64_v4i16((LAS s4*)(uintptr_t)a));
}
DEV bf8 mk8(u2 a, u2 b) { u4 r; r.x = a.x; r.y = a.y; r.z = b.x; r.w = b.y; return __builtin_bit_cast(bf8, r); }
DEV bf8 ld_tr8(u32 a, u32 d) { return mk8(ld_tr(a), ld_tr(a + d)); }
DEV f4 mfma(bf8 a, bf8 b, f4 c) { return __builtin_amdgcn_mfma_f32_16x16x32_bf16(a, b, c, 0, 0, 0); }
DEV f4 mfma16(u2 a, u2 b, f4 c) { return __builtin_amdgcn_mfma_f32_16x16x16bf16_1k(__builtin_bit_cast(s4, a), __builtin_bit_cast(s4, b), c, 0, 0, 0); }

#define PIN(x) asm volatile("" : "+v"(x))
#define SB() __builtin_amdgcn_sched_barrier(0)
#define RLX __ATOMIC_RELAXED, __HIP_MEMORY_SCOPE_AGENT
DEV u32 gld32(const char* p) { return __hip_atomic_load((const GAS u32*)p, RLX); }
DEV void gst64(char* p, u64 v) { __hip_atomic_store((GAS u64*)p, v, RLX); }
DEV void gst32(char* p, u32 v) { __hip_atomic_store((GAS u32*)p, v, RLX); }
typedef __amdgpu_buffer_rsrc_t rsrc_t;
DEV u4 bld128_sc1(rsrc_t r, u32 off) { return __builtin_amdgcn_raw_buffer_load_b128(r, off, 0, 16); }
DEV void bst128_sc1(rsrc_t r, u32 off, u4 v) { __builtin_amdgcn_raw_buffer_store_b128(v, r, off, 0, 16); }

#define FSTAMPS 0
#define STAMP(i) do {} while (0)
#define CSTAMP(i) do {} while (0)
#define FST(i) do {} while (0)
#define GST(i) do {} while (0)
#define KST(i) do {} while (0)
#define PST(i) do {} while (0)
#define EST(i) do {} while (0)
#define STM_ARG
#define STM

template <int CTRL> DEV float dppf(float v) {
  return __uint_as_float(__builtin_amdgcn_update_dpp(0u, __float_as_uint(v), CTRL, 0xf, 0xf, true));
}
template <int CTRL> DEV u32 dppu(u32 v) { return __builtin_amdgcn_update_dpp(0u, v, CTRL, 0xf, 0xf, true); }
DEV float row_sum16(float v) {
  v += dppf<0xB1>(v);
  v += dppf<0x4E>(v);
  v += dppf<0x141>(v);
  v += dppf<0x140>(v);
  return v;
}
DEV void top2_merge(u32& a1, u32& a2, u32 b1, u32 b2) {
  u32 lo = min(a1, b1);
  a1 = max(a1, b1);
  a2 = max(lo, max(a2, b2));
}
DEV void top2_wave(u32& k1, u32& k2) {
  top2_merge(k1, k2, dppu<0xB1>(k1), dppu<0xB1>(k2));
  top2_merge(k1, k2, dppu<0x4E>(k1), dppu<0x4E>(k2));
  top2_merge(k1, k2, dppu<0x124>(k1), dppu<0x124>(k2));
  top2_merge(k1, k2, dppu<0x128>(k1), dppu<0x128>(k2));
  {
    auto r1 = __builtin_amdgcn_permlane16_swap(k1, k1, false, false);
    auto r2 = __builtin_amdgcn_permlane16_swap(k2, k2, false, false);
    k1 = r1[0]; k2 = r2[0]; top2_merge(k1, k2, r1[1], r2[1]);
  }
  {
    auto r1 = __builtin_amdgcn_permlane32_swap(k1, k1, false, false);
    auto r2 = __builtin_amdgcn_permlane32_swap(k2, k2, false, false);
    k1 = r1[0]; k2 = r2[0]; top2_merge(k1, k2, r1[1], r2[1]);
  }
}
DEV u32 mk_tag(u32 ep) {
  u32 tag = ep * 0x9E3779B1u + 0x7F4A7C15u;
  tag ^= tag >> 15; tag *= 0x85EBCA77u; tag ^= tag >> 13;
  return tag + (tag == ep);
}
DEV u32 epoch_get(const Params& p) { u32 e; asm volatile("s_load_dword %0, %1, 0" : "=s"(e) : "s"(p.ws.p)); return e; }
DEV u32 epoch_tag(u32 e) { asm volatile("s_waitcnt lgkmcnt(0)" : "+s"(e)); return mk_tag(e); }

DEV float silu_f(float v) { return v * __builtin_amdgcn_rcpf(1.f + __expf(-v)); }

DEV u32 key_tok(u32 k) { return ~k & 2047u; }
DEV float key_r(u32 k) {
  u32 o = k >> 16;
  u32 b = (o & 0x8000u) ? (o & 0x7fffu) : (~o & 0xffffu);
  return bflo(b);
}
DEV float vmax(float a, float b) { return __builtin_elementwise_maximum(a, b); }
DEV void bar() { asm volatile("s_barrier" ::: "memory"); }
#define NEW_PAGE() asm volatile("s_branch 1f\n\t.p2align 12\n1:" ::: "memory")
#define FENCE() asm volatile("" ::: "memory")
DEV u32 lds_peek(u32 a) { return __hip_atomic_load((LAS u32*)(uintptr_t)a, __ATOMIC_RELAXED, __HIP_MEMORY_SCOPE_WORKGROUP); }
DEV void lds_add(u32 a, u32 v) {
  asm volatile("s_mov_b64 exec, 1\n\tds_add_u32 %0, %1\n\ts_mov_b64 exec, -1" :: "v"(a), "v"(v) : "memory");
}
DEV void lds_set(u32 a, u32 v) {
  asm volatile("s_mov_b64 exec, 1\n\tds_write_b32 %0, %1\n\ts_mov_b64 exec, -1" :: "v"(a), "v"(v) : "memory");
}
template <int SLP> DEV void lds_wait_ge(u32 a, u32 v) {
  u32 n = 1u << 20, t, l, r;
  asm volatile(".if %6\n\ts_branch 2f\n\t.endif\n1:\n\t.if %6\n\ts_sleep %6\n\t.endif\n2:\n\t"
               "ds_read_b32 %1, %4\n\ts_sub_u32 %0, %0, 1\n\ts_cselect_b32 %2, 0, %5\n\ts_waitcnt lgkmcnt(0)\n\t"
               "v_readfirstlane_b32 %3, %1\n\ts_cmp_lt_u32 %3, %2\n\ts_cbranch_scc1 1b"
               : "+s"(n), "=&v"(t), "=&s"(l), "=&s"(r) : "v"(a), "s"(v), "i"(SLP) : "scc", "memory");
}
template <int SLP> DEV void lds_wait_bit(u32 a, u32 v) {
  u32 n = 1u << 20, t, l, r;
  asm volatile("s_branch 2f\n1:\n\ts_sleep %6\n2:\n\tds_read_b32 %1, %4\n\ts_sub_u32 %0, %0, 1\n\ts_cselect_b32 %2, -1, 0\n\ts_waitcnt lgkmcnt(0)\n\t"
               "v_readfirstlane_b32 %3, %1\n\ts_or_b32 %3, %3, %2\n\ts_and_b32 %3, %3, %5\n\ts_cbranch_scc0 1b"
               : "+s"(n), "=&v"(t), "=&s"(l), "=&s"(r) : "v"(a), "s"(v), "i"(SLP) : "scc", "memory");
}
DEV u32 norm2(u32 x, float m, float rs) {
  const u32 d = pk2(bflo(x) - m, bfhi(x) - m);
  return pk2(bflo(d) * rs, bfhi(d) * rs);
}

DEV float dot2(u32 a, u32 b, float c) { return __builtin_amdgcn_fdot2_f32_bf16(__builtin_bit_cast(bf2, a), __builtin_bit_cast(bf2, b), c, false); }
DEV float fsel(u32 m, float a, float b) {
  return __uint_as_float(__builtin_amdgcn_bitop3_b32(m, __float_as_uint(a), __float_as_uint(b), 0xCA));
}
struct RowIn {
  u4 x[2][5], w[5];
  DEV void take() {
    asm volatile("" : "={v[100:103]}"(w[0]), "={v[104:107]}"(x[0][0]), "={v[108:111]}"(x[1][0]), "={v[112:115]}"(w[1]), "={v[116:119]}"(x[0][1]), "={v[120:123]}"(x[1][1]),
                 "={v[124:127]}"(w[2]), "={v[128:131]}"(x[0][2]), "={v[132:135]}"(x[1][2]), "={v[136:139]}"(w[3]), "={v[140:143]}"(x[0][3]), "={v[144:147]}"(x[1][3]),
                 "={v[148:151]}"(w[4]), "={v[152:155]}"(x[0][4]), "={v[156:159]}"(x[1][4]));
  }
  DEV void wait(int i) {
    if (i == 0) asm volatile("s_waitcnt vmcnt(12)" : "+{v[100:103]}"(w[0]), "+{v[104:107]}"(x[0][0]), "+{v[108:111]}"(x[1][0]));
    if (i == 1) asm volatile("s_waitcnt vmcnt(9)" : "+{v[112:115]}"(w[1]), "+{v[116:119]}"(x[0][1]), "+{v[120:123]}"(x[1][1]));
    if (i == 2) asm volatile("s_waitcnt vmcnt(6)" : "+{v[124:127]}"(w[2]), "+{v[128:131]}"(x[0][2]), "+{v[132:135]}"(x[1][2]));
    if (i == 3) asm volatile("s_waitcnt vmcnt(3)" : "+{v[136:139]}"(w[3]), "+{v[140:143]}"(x[0][3]), "+{v[144:147]}"(x[1][3]));
    if (i == 4) asm volatile("s_waitcnt vmcnt(0)" : "+{v[148:151]}"(w[4]), "+{v[152:155]}"(x[0][4]), "+{v[156:159]}"(x[1][4]));
  }
  DEV void stats(const Params& p, rsrc_t rws, u32& tag, int lane, int s8, u32 slot STM_ARG) {
    const u32 ep = epoch_get(p);
    const u32 dq = ((lane >> 2) ^ (lane >> 4)) & 3;
    const int sidx = s8 + (lane >> 4) + dq * 2, tok = 2 * (sidx & 1023) + (sidx >> 10);
    u32 so = ((dq & 1) | (lane & 3)) ? 0x80000000u : WS_ST2 + sidx * 16, ko = lane ? L_NONE + lane * 16 : slot;
    u32 m1 = __builtin_amdgcn_sbfe(lane, 0, 1), m2 = __builtin_amdgcn_sbfe(lane, 1, 1), mz = __builtin_amdgcn_sbfe(dq, 1, 1), mv = ~__builtin_amdgcn_sbfe(dq, 0, 1);
    u4 on; on.x = on.y = on.z = on.w = 0x3f803f80u;
    PIN(so); PIN(ko); PIN(m1); PIN(m2); PIN(mz); PIN(mv); PIN(on);
    const bf8 ones = __builtin_bit_cast(bf8, on);
    const f4 z4 = {0.f, 0.f, 0.f, 0.f};
    f4 ax[2] = {z4, z4}, aq[2] = {z4, z4}, aw[2] = {z4, z4}, a1 = z4;
    SB();
#pragma unroll
    for (int i = 0; i < 5; ++i) {
      wait(i);
      const bf8 wv = __builtin_bit_cast(bf8, w[i]);
#pragma unroll
      for (int z = 0; z < 2; ++z) {
        const bf8 v = __builtin_bit_cast(bf8, x[z][i]);
        aq[z] = mfma(v, v, aq[z]); ax[z] = mfma(v, ones, ax[z]); aw[z] = mfma(v, wv, aw[z]);
      }
      a1 = mfma(wv, ones, a1);
      SB();
      PST(6 + i);
    }
    tag = epoch_tag(ep);
    PST(11);
    const float sw = (a1[0] + a1[1]) + (a1[2] + a1[3]);
    auto quad = [](float v) { v += dppf<0xB1>(v); v += dppf<0x4E>(v); return v; };
    auto diag = [&](f4 d) { return fsel(m2, fsel(m1, d[3], d[2]), fsel(m1, d[1], d[0])); };
    auto both = [&](float a, float b) { return fsel(mz, dppf<0x128>(b), a); };
    const float mean = fsel(mz, (ax[1][0] + ax[1][1]) + (ax[1][2] + ax[1][3]), (ax[0][0] + ax[0][1]) + (ax[0][2] + ax[0][3])) * (1.f / 640.f);
    const float sxw = quad(both(diag(aw[0]), diag(aw[1])));
    float var = fmaf(-mean, mean, quad(both(diag(aq[0]), diag(aq[1]))) * (1.f / 640.f));
    if (__builtin_expect(__any(mv && !(var * 64.f >= mean * mean)), 0)) {
      float q[2];
#pragma unroll
      for (int z = 0; z < 2; ++z) {
        f4 a = z4;
#pragma unroll
        for (int i = 0; i < 5; ++i) a = mfma(ones, __builtin_bit_cast(bf8, x[z][i]), a);
        const float mu = quad(a[0]) * (1.f / 640.f);
        float t = 0.f;
#pragma unroll
        for (int i = 0; i < 5; ++i)
#pragma unroll
          for (int e = 0; e < 4; ++e) { const float c = bflo(x[z][i][e]) - mu, d = bfhi(x[z][i][e]) - mu; t = fmaf(c, c, fmaf(d, d, t)); }
        t = quad(t);
        t += __shfl_xor(t, 16); t += __shfl_xor(t, 32);
        q[z] = t;
      }
      var = both(q[0], q[1]) * (1.f / 640.f);
    }
    const float mb = rbf(mean);
    const float sd = rbf(__builtin_amdgcn_sqrtf(rbf(rbf(var) + 1e-5f)));
    const float rs = __builtin_amdgcn_rcpf(sd);
    u4 st; st.x = __float_as_uint(mb); st.z = __float_as_uint(rs); st.y = st.w = tag;
    PST(12);
    bst128_sc1(rws, so, st);
    PST(13);
    const float rr = fmaf(-mb, sw, sxw) * rs;
    const u32 b = pk2(rr, rr) & 0xffffu;
    const u32 o = (b & 0x8000u) ? (~b & 0xffffu) : (b | 0x8000u);
    u32 k1 = ((o << 16) | (0xffffu - tok)) & mv, k2 = 0;
    top2_merge(k1, k2, dppu<0x128>(k1), 0);
    k1 = max(k1, dppu<0x124>(k1)); k2 = max(k2, dppu<0x124>(k2));
    {
      auto r1 = __builtin_amdgcn_permlane16_swap(k1, k1, false, false);
      auto r2 = __builtin_amdgcn_permlane16_swap(k2, k2, false, false);
      k1 = r1[0]; k2 = r2[0]; top2_merge(k1, k2, r1[1], r2[1]);
    }
    {
      auto r1 = __builtin_amdgcn_permlane32_swap(k1, k1, false, false);
      auto r2 = __builtin_amdgcn_permlane32_swap(k2, k2, false, false);
      k1 = r1[0]; k2 = r2[0]; top2_merge(k1, k2, r1[1], r2[1]);
    }
    u4 kk; kk.x = k1; kk.z = k2; kk.y = kk.w = tag;
    PST(14);
    st_u4(ko, kk);
  }
};

template <int N, int DM, int DS, int E60 = 0, int TH = 63> DEV void dma_run(u32 m0v, u32 voff, u4 srd, u32 so) {
  asm volatile("s_mov_b32 m0, %2\n\t"
               "s_mov_b32 exec_hi, %7\n\t"
               ".rept %4\n\t"
               "buffer_load_dwordx4 %1, %3, %0 offen lds\n\t"
               "s_add_u32 m0, m0, %5\n\t"
               "s_add_u32 %0, %0, %6\n\t"
               ".if %8 < 63\n\ts_waitcnt vmcnt(%8)\n\t.endif\n\t"
               ".endr\n\t"
               "s_mov_b32 exec_hi, -1"
               : "+s"(so) : "v"(voff), "s"(m0v), "s"(srd), "i"(N), "i"(DM), "i"(DS), "i"(E60 ? 0x0fffffff : -1), "i"(TH) : "scc");
}

struct WoDma {
  u32 vo, vol, m, so, m0;
  DEV void init(int lane, int lw, int ng, int start, int last) {
    const int l60 = min(lane, 59);
    vo = (lw * 6 + l60 / 10) * 1280 + (l60 % 10) * 16 + ng * 160;
    if (WEXP == 1) vo = ng * 3840 + lw * 960 + l60 * 16;
    if (WEXP == 2) vo = 0x80000000u;
    vol = min(last * 6 + l60 / 10, 639) * 1280 + (l60 % 10) * 16 + ng * 160;
    m0 = L_WO + lw * 960; m = m0 + start * 3840; so = start * (24 * 1280);
    PIN(vo); PIN(vol);
    asm volatile("" : "+s"(m), "+s"(so), "+s"(m0));
  }
  template <int I0, int I1, int TH = 63, int SKIP = 0, int NL = 4> DEV void issue(u4 srd, int lw, u32 f = 0) {
    constexpr int E = I1 > 26 ? 26 : I1;
    if (E > I0)
      asm volatile(".if %7\n\ts_cmp_gt_u32 %8, 53\n\ts_cbranch_scc1 1f\n\t.endif\n\t"
                   "s_mov_b32 m0, %0\n\ts_mov_b32 exec_hi, 0x0fffffff\n\t"
                   ".rept %5\n\tbuffer_load_dwordx4 %2, %3, %1 offen lds\n\ts_addk_i32 m0, 960 * %9\n\ts_addk_i32 %1, 7680 * %9\n\t"
                   ".if %6 < 63\n\ts_waitcnt vmcnt(%6)\n\t.endif\n\t.endr\n\t"
                   "s_mov_b32 %0, m0\n\ts_mov_b32 exec_hi, -1\n1:"
                   : "+s"(m), "+s"(so) : "v"(vo), "s"(srd), "s"(m0), "i"(E > I0 ? (E - I0) * 4 / NL : 1), "i"(TH), "i"(SKIP), "s"(f), "i"(NL) : "scc");
    if (I1 > 26) dma_run<1, 0, 0, 1>(L_WO + lw * 960 + 26 * 3840, vol, srd, 0);
  }
};

struct Epi {
  u4 xv[2], sv, fin; u2 xh; u32 goff[2], soff, tag;
  float gf[20], c[4];
  static DEV u32 row(u32 lane) { return ((lane >> 1) & 12) | ((lane >> 2) & 1) | ((lane >> 4) & 2); }
  DEV void init(int lane, int m, int mg, int ng) {
    const int mt = mg * 4 + m, parity = mt >> 6;
    const u32 u = (mt & 63) * 16 + row(lane), o = (2 * u + parity) * 1280 + ng * 160;
    goff[0] = o + (ng & 1) * 32 + (lane & 3) * 16; goff[1] = o + ((ng & 1) ? 0 : 128) + (lane & 3) * 8;
    soff = WS_ST2 + (parity * 1024 + u) * 16;
    PIN(goff[0]); PIN(goff[1]); PIN(soff);
  }
  DEV void issue(u4 srdx, u4 srdws, u32 fo = WS_FIN) {
    asm volatile("buffer_load_dwordx4 %0, %1, %2, 0 offen sc1" : "=v"(sv) : "v"(soff), "s"(srdws));
    asm volatile("buffer_load_dwordx4 %0, %1, %2, 0 offen" : "=v"(xv[0]) : "v"(goff[0]), "s"(srdx));
    asm volatile("buffer_load_dwordx4 %0, %1, %2, 0 offen offset:64" : "=v"(xv[1]) : "v"(goff[0]), "s"(srdx));
    asm volatile("buffer_load_dwordx2 %0, %1, %2, 0 offen" : "=v"(xh) : "v"(goff[1]), "s"(srdx));
    asm volatile("buffer_load_dwordx4 %0, off, %1, %2 sc1" : "=v"(fin) : "s"(srdws), "s"(fo));
  }
  template <int N> DEV void wait() {
    asm volatile("s_waitcnt vmcnt(%5)" : "+v"(xv[0]), "+v"(xv[1]), "+v"(xh), "+v"(sv), "+v"(fin) : "i"(N) : "memory");
  }
  DEV void check(char* ws, u32 tag) {
    if (__builtin_expect(__any(__builtin_amdgcn_bitop3_b32(sv.y, sv.w, tag, 0x7e) != 0u), 0)) {
      const rsrc_t rws = __builtin_amdgcn_make_buffer_rsrc(ws, 0, WS_END, 0x00020000);
#pragma nounroll
      for (u32 spin = 0; !__all(sv.y == tag && sv.w == tag) && spin < SPIN_CAP; ++spin) {
        FENCE();
        sv = bld128_sc1(rws, soff);
      }
      __builtin_amdgcn_s_waitcnt(0x0f70);
    }
  }
  DEV u32 xw(int i) const { return i < 8 ? xv[i >> 2][i & 3] : xh[i & 1]; }
  DEV void gate(int i) {
    if (i == 0) {
      c[0] = __uint_as_float(sv.z); c[1] = -__uint_as_float(sv.x) * c[0];
      c[2] = c[0] * -1.4426950408889634f; c[3] = c[1] * -1.4426950408889634f;
    }
    float x = i & 1 ? bfhi(xw(i >> 1)) : bflo(xw(i >> 1));
    PIN(x);
    float z = fmaf(x, c[0], c[1]), t = fmaf(x, c[2], c[3]);
    PIN(z); PIN(t);
    gf[i] = z * __builtin_amdgcn_rcpf(1.f + __builtin_amdgcn_exp2f(t));
    PIN(gf[i]);
  }
};
#define SPIN(x) asm volatile("" : "+s"(x))
#define HALF_A(SB, VX, VW, SX, SW, SA, SC, EA, EC) \
    "s_add_u32 m0, " SB ", 12288\n\ts_mov_b64 exec, " EC "\n\ts_cbranch_execz 7f\n\tbuffer_load_dwordx4 " VX ", " SX ", " SC " offen lds\n7:\n\t" \
    "s_mov_b32 m0, " SB "\n\ts_mov_b64 exec, " EA "\n\tbuffer_load_dwordx4 " VX ", " SX ", " SA " offen lds\n\t" \
    "s_addk_i32 m0, 14336\n\ts_mov_b64 exec, -1\n\tbuffer_load_dwordx4 " VW ", " SW ", 0 offen lds\n\t" \
    "s_addk_i32 m0, 6912\n\ts_nop 0\n\tbuffer_load_dwordx4 " VW ", " SW ", 0 offen offset:1280 lds\n\t" \
    "s_addk_i32 m0, 6912\n\ts_nop 0\n\tbuffer_load_dwordx4 " VW ", " SW ", 0 offen offset:2560 lds\n\t"
#define HALF_B(SB, VX, VW, SX, SW, SB1, SB2, SH) \
    "s_add_u32 m0, " SB ", 4096\n\ts_nop 0\n\tbuffer_load_dwordx4 " VX ", " SX ", " SB1 " offen lds\n\t" \
    "s_addk_i32 m0, 4096\n\ts_nop 0\n\tbuffer_load_dwordx4 " VX ", " SX ", " SB2 " offen lds\n\t" \
    "s_addk_i32 m0, 10240\n\ts_nop 0\n\tbuffer_load_dwordx4 " VW ", " SW ", " SH " offen lds\n\t" \
    "s_addk_i32 m0, 6912\n\ts_nop 0\n\tbuffer_load_dwordx4 " VW ", " SW ", " SH " offen offset:1280 lds\n\t" \
    "s_addk_i32 m0, 6912\n\ts_nop 0\n\tbuffer_load_dwordx4 " VW ", " SW ", " SH " offen offset:2560 lds\n\t"
struct LdIn {
  u4 srdx, srdw; u32 vx, vw, lw, j, tp, cls, kb, sa, sb1, sb2, sc, sb, sh; u64 ec, ea;
  DEV void take() {
    asm volatile("" : "={s[4:7]}"(srdx), "={s[8:11]}"(srdw), "={v1}"(vx), "={v2}"(vw), "={s12}"(lw), "={s13}"(j), "={s14}"(tp), "={s15}"(cls), "={s16}"(kb),
                 "={s18}"(sa), "={s19}"(sb1), "={s20}"(sb2), "={s21}"(sc), "={s[22:23]}"(ec), "={s[24:25]}"(ea), "={s26}"(sb), "={s27}"(sh));
  }
};
DEV void main_loader(Params& p, LdIn& in, int lane, int parity, int mg, int ng, u32 koff, Epi& ei STM_ARG) {
  const int lw = in.lw, tp = in.tp, j = in.j, rt = parity * 10 + tp, u0 = tp * 104 - 8;
  const u4 srdx = in.srdx;
  u64 ktab = KT0;
  ktab = in.cls == 1 ? KT1 : ktab; SPIN(ktab); ktab = in.cls == 2 ? KT2 : ktab; SPIN(ktab); ktab = in.cls == 3 ? KT3 : ktab; SPIN(ktab); ktab = in.cls == 4 ? KT4 : ktab;
  auto next = [&]() {
    ktab >>= 4;
    u32 kb = ((u32)ktab & 15u) + koff;
    kb = min(kb, kb - 10u);
    const int d = kb - in.kb;
    in.kb = kb;
    in.vx += d * 128; in.vw += d * (64 * 3840);
    in.sb = in.sb >= 3 * SLOT ? in.sb - 3 * SLOT : in.sb + SLOT;
    PIN(in.vx); PIN(in.vw); SPIN(in.sb);
  };
  auto issue = [&]() {
    asm volatile("s_bitcmp1_b32 %12, 1\n\ts_cselect_b64 vcc, -1, 0\n\ts_cbranch_vccnz 5f\n4:\n\t"
                 HALF_A("%0", "%1", "%2", "%3", "%4", "%5", "%6", "%7", "%8") "s_cbranch_vccnz 6f\n5:\n\t"
                 HALF_B("%0", "%1", "%2", "%3", "%4", "%9", "%10", "%11") "s_cbranch_vccnz 4b\n6:"
                 :: "s"(in.sb), "v"(in.vx), "v"(in.vw), "s"(in.srdx), "s"(in.srdw), "s"(in.sa), "s"(in.sc), "s"(in.ea), "s"(in.ec),
                    "s"(in.sb1), "s"(in.sb2), "s"(in.sh), "s"(in.cls) : "scc", "vcc");
    next();
  };
#define STAGE_BAR(E) asm volatile("s_cmp_lt_u32 %0, 2\n\ts_cbranch_scc1 1f\n\ts_waitcnt vmcnt(%1)\n1:\n\ts_waitcnt vmcnt(%2)\n\ts_barrier" :: "s"(lw), "i"(9 + (E)), "i"(10 + (E)) : "scc", "memory")
  next();
  u32 ep = epoch_get(p);
  const u4 srdws = srd(p.ws);
  const int srow = lw * 28 + min(lane, 27);
  u32 go1 = WS_ST2 + (parity * 1024 + min(max(u0 + srow, 0), 1023)) * 16;
  u32 wa = lw < 2 ? (XST ? 0u : 0x80000000u) + WS_XS + ((rt * 10 + j) * 14 + (lw ? 14 : -7)) * 512 + min(lane, 27) * 128 : (BOXW == 0 || (BOXW == 2 && lane) ? 0x80000000u : WS_BOX + (mg * 8 + ng) * 256 + (lw & 1) * 64);
  u32 ho = (lane | (lw ^ 3)) ? 0x80000000u : WS_FLAG + (rt * 10 + j) * 256 + 128;
  PIN(go1); PIN(wa); PIN(ho);
  WoDma wo; wo.init(lane, lw, ng, 0, lw == 2 ? 52 : 106);
  ei.init(lane, lw, mg, ng);
  SB();
  PST(4);
  issue();
  u32 tagv = epoch_tag(ep);
  PIN(tagv);
  STAMP(9);
  PST(5);
  STAGE_BAR(0);
  PST(7);
  STAMP(2);
  asm volatile("buffer_store_dword %0, %1, %2, 0 offen sc1" :: "v"(tagv), "v"(ho), "s"(srdws));
#pragma unroll 1
  for (int s = 2; s < 9; ++s) {
    issue();
    STAGE_BAR(0);
  }
  EST(2);
  u4 g1;
  auto stats = [&]() {
    asm volatile("buffer_load_dwordx4 %0, %1, %2, 0 offen sc1" : "=v"(g1) : "v"(go1), "s"(srdws));
    asm volatile("s_mov_b32 m0, %0\n\ts_mov_b64 exec, 0x0fffffff\n\tbuffer_load_dword %1, %2, 0 offen sc1 lds\n\t"
                 "s_add_u32 m0, m0, 504\n\ts_nop 0\n\tbuffer_load_dword %1, %2, 0 offen offset:8 sc1 lds\n\ts_mov_b64 exec, -1"
                 :: "s"(L_MR + lw * 112), "v"(go1), "s"(srdws) : "scc");
  };
  issue();
  if (STP == 0) stats();
  EST(3);
  STAGE_BAR(STP == 0 ? 3 : 0);
  EST(4);
  STAMP(1);
  if (STP == 1) stats();
  const u4 srdo = srd(p.wo);
  asm volatile("s_mov_b32 m0, %0\n\ts_cmp_lt_u32 %3, 2\n\ts_cbranch_scc1 1f\n\tbuffer_atomic_add %4, %1, %2, 0 offen\n\ts_branch 2f\n"
               "1:\n\tbuffer_load_dword %1, %2, 0 offen sc1 lds\n2:" :: "s"(L_DUMP + lw * 256), "v"(wa), "s"(srdws), "s"(lw), "v"(0u) : "scc");
  wo.issue<0, WO7>(srdo, lw);
  EST(5);
  asm volatile("s_waitcnt vmcnt(%1)" : "+v"(g1) : "i"(WO7 + 3) : "memory");
  EST(7);
  if (__builtin_expect(__any(__builtin_amdgcn_bitop3_b32(g1.y, g1.w, tagv, 0x7e) != 0u), 0)) {
    const rsrc_t rws = __builtin_amdgcn_make_buffer_rsrc(p.ws.p, 0, WS_END, 0x00020000);
#pragma nounroll
    for (u32 spin = 0; spin < SPIN_CAP; ++spin) {
      FENCE();
      g1 = bld128_sc1(rws, go1);
      if (__all(g1.y == tagv && g1.w == tagv)) break;
      __builtin_amdgcn_s_sleep(2);
    }
    __builtin_amdgcn_s_waitcnt(0x0f70);
    st_u(L_MR + srow * 4, g1.x); st_u(L_MR + 512 + srow * 4, g1.z);
  }
  __builtin_amdgcn_s_setprio(LDP);
  if (FSTAMPS + 0 == 6) { wait_vm<WO7 + 1>(); EST(8); }
  wait_bar<WO7 + 1>();
  EST(6);
  STAMP(3);
  if (WDLY) __builtin_amdgcn_s_sleep(WDLY);
  static_assert((32 - 4 * WO7) % 2 == 0, "");
  ei.tag = tagv;
  if (lw < 2) {
    dma_run<16 - 2 * WO7, 1920, 12 * 1280, 1, WTH>(L_WO + (4 * WO7 + lw) * 960, wo.vo, srdo, 4 * WO7 * 7680);
    dma_run<17, 1920, 12 * 1280, 1, WTH>(L_WO + (53 + lw) * 960, wo.vo, srdo, 53 * 7680);
  }
  ei.issue(srdx, srdws);
  ei.wait<5>();
  lds_add(Y_WO, 1);
  STAMP(6);
  ei.wait<0>();
  if (lw >= 2) {
    __builtin_amdgcn_s_sleep(WLD);
    dma_run<20, 960, 7680, 1, WTH2>(L_WO + (lw == 2 ? 32 : 86) * 960, wo.vo, srdo, (lw == 2 ? 30 : 83) * 7680);
    dma_run<1, 0, 0, 1>(L_WO + (lw == 2 ? 52 : 106) * 960, wo.vol, srdo, 0);
  }
  STAMP(4);
}

DEV int fresh_lane() {
  int l;
  asm volatile("v_mbcnt_lo_u32_b32 %0, -1, 0\n\tv_mbcnt_hi_u32_b32 %0, -1, %0" : "=v"(l));
  __builtin_assume(l >= 0 && l < 64);
  return l;
}
struct G2 {
  u32 b0, oo, fo, gb, one, pa, pm;
  DEV void init(int wave, int mg, int ng) {
    const int lane = fresh_lane(), g = lane >> 4, i16 = lane & 15, m = wave & 3, kh = wave >> 2;
    b0 = L_WO + (kh * 320 + 16 * (g >> 1) + 4 * (g & 1) + (i16 >> 2)) * 160 + (lane & 3) * 8;
    oo = WS_O + (mg * 4 + m) * 20480 + kh * 10240 + lane * 16;
    fo = WS_BOX + (mg * 8 + ng) * 256 + kh * 64 + lane * 4; gb = 256u << kh; one = 1; pa = Y_PUB;
    pm = ((PWM >> wave) & 1) ? (((0x4b5au >> (mg & 15)) & 1) ? 0x3ffu : 0x1fu) : 0u;
    PIN(b0); PIN(oo); PIN(fo); PIN(gb); PIN(one); PIN(pa); SPIN(pm);
  }
};

constexpr u64 sel_mask(int r) {
  u64 m = 0;
  for (int l = 0; l < 64; ++l) {
    const int i = l & 15, c = 4 * ((l >> 4) & 1) + r;
    if (i < 8 ? c <= i : c > i - 8) m |= 1ull << l;
  }
  return m;
}
constexpr u64 SEL0 = sel_mask(0), SEL1 = sel_mask(1), SEL2 = sel_mask(2), SEL3 = sel_mask(3);
#define LM(m) __builtin_amdgcn_inverse_ballot_w64(m)
DEV u2 and2(u2 a, u2 m) { u2 r; r.x = a.x & m.x; r.y = a.y & m.y; return r; }

struct Att {
  u32 aA0[2], aA1[2], aB[2];
  u32 mOwn, mRot, mV, mLo, so[2], xg, aXw, aXr, aCS, aCV, aST, aMR, aH, aF;
  u4 mS;
  DEV void init_k(u32 ln, u32 n) {
    const u32 a0 = XB + ((ln & 15) << 7) + ((((ln >> 1) & 7) ^ (ln >> 4)) << 4);
    const u32 b0 = WB + ((ln & 48) << 6) + ((ln & 12) << 5) + (((2 * n + ((ln >> 1) & 1)) ^ ((ln >> 2) & 6)) << 4) + ((ln & 1) << 3);
#pragma unroll
    for (int s = 0; s < 2; ++s) {
      aA0[s] = a0 + s * 2 * SLOT; aA1[s] = (a0 ^ 64) + s * 2 * SLOT; aB[s] = b0 + s * 2 * SLOT;
      PIN(aA0[s]); PIN(aA1[s]); PIN(aB[s]);
    }
  }
  template <int NS> DEV void init(u32 ln, int n, int tau, bool edge, int tp, int parity, int j) {
    constexpr int s0 = NS == 4 ? 0 : 4;
    constexpr u32 SGN = 0x80000000u;
    const u32 i16 = ln & 15, g16 = ln & 48, h32 = ln >> 5, i4 = i16 * 4;
    mLo = ~(u32)__builtin_amdgcn_sbfe((int)ln, 3, 1);
    const u32 gt = (u32)(((int)g16 - 16 * tau) >> 31);
    mOwn = mLo & gt; mRot = ~mLo & ~gt;
    mV = (u32)(((int)i16 - 4 * tau) >> 31);
    u32 l0 = (u32)SEL0, l1 = (u32)SEL1, l2 = (u32)SEL2, l3 = (u32)SEL3;
    SPIN(l0); SPIN(l1); SPIN(l2); SPIN(l3);
    mS.x = __builtin_amdgcn_sbfe(l0, ln, 1); mS.y = __builtin_amdgcn_sbfe(l1, ln, 1); mS.z = __builtin_amdgcn_sbfe(l2, ln, 1); mS.w = __builtin_amdgcn_sbfe(l3, ln, 1);
    PIN(mOwn); PIN(mRot); PIN(mV); PIN(mLo); PIN(mS);
    const u32 up = tp * 104 + 8 + s0 * 16 + h32 * 16 + i16;
    const u32 hs = ln << 26;
    so[0] = WS_O + (parity * 64 - 1) * 20480 + (2 * j + (n >> 1)) * 1024 + (n & 1) * 512 + (up >> 4) * 20480 + (((ln & 16) | (up & 15)) << 4);
    so[1] = so[0] + 2 * 20480;
    if (NS == 4) so[0] |= ~hs & mLo & SGN;
    else so[1] |= (hs | (tp == 9 ? SGN : 0u)) & SGN;
    PIN(so[0]); PIN(so[1]);
    const int xl = n > 0 ? L_X + ((n - 1) * 7 + s0) * 512 : L_NONE, xh = n < 3 ? L_X + (n * 7 + s0) * 512 : L_NONE;
    aXr = xl + (int)h32 * (xh - xl) + ln * 4;
    aXw = aXr ^ 128;
    aCS = L_CQ + (n >> 1) * 256 + (n & 1) * 32 + g16 * 4;
    aCV = L_CS + 512 + n * 64 + i4;
    if (NS == 4) aCV += min(g16, 1u) * 0x10000;
    aF = Y_F + s0 * 4 + n * 4 + ln * 4 + min(ln, 1u) * 0x10000;
    aST = L_MR + s0 * 64 + i4;
    aMR = L_MR + s0 * 64 + g16;
    aH = n * 512 + ln * 8;
    const u32 xa = WS_XS + (((parity * 10 + tp) * 10 + j) * 14 + (n ? 7 : 0) + s0) * 512 + (edge ? 0u : SGN);
    xg = (xa + (ln & 31) * 16) | ((hs ^ (n ? SGN : 0u)) & SGN);
    PIN(aXw); PIN(aXr); if (NS == 3) PIN(aCS); PIN(aCV); PIN(aST); PIN(aMR); PIN(aH); PIN(aF); PIN(xg);
  }
};

template <int NS>
DEV void main_compute(const Params& p, rsrc_t rws, u32& tag, int lane, int wave, int tp, int parity, int rb, int j, int mg, int ng, G2& g2 STM_ARG) {
  constexpr int s0 = NS == 4 ? 0 : 4;
  const int n = (wave & 3) ^ (NS == 3);
  const int rt = parity * 10 + tp;
  u32 ep = epoch_get(p);
  Att at;
  at.init_k(lane, n);
  if (NS == 4) st_u(lane < 32 ? wave * SLOT + XB + lane * 4 : L_SYNC + wave * 64 + (lane & 15) * 4, lane < 32 ? 0xbf80bf80u : 0u);
  int tau = n - rb; tau += tau < 0 ? 5 : 0;
  const bool edge = ((0x0f00001e >> (n * 8 + tau)) & 1) != 0;
  SB();
  PST(2);
  if (INIT1) at.template init<NS>(lane, n, tau, edge, tp, parity, j);
  SB();
  PST(3);
  tag = epoch_tag(ep);
  const u32 (&aA0)[2] = at.aA0, (&aA1)[2] = at.aA1, (&aB)[2] = at.aB, (&so)[2] = at.so;
  const u32 &mOwn = at.mOwn, &mRot = at.mRot, &mV = at.mV, &mLo = at.mLo, &xg = at.xg, &aXw = at.aXw, &aXr = at.aXr, &aCS = at.aCS, &aCV = at.aCV,
            &aST = at.aST, &aMR = at.aMR, &aH = at.aH, &aF = at.aF;
  const u4& mS = at.mS;
  f4 acc[NS][3];
  u2 m0, mm, mp;
#pragma unroll
  for (int a = 0; a < NS; ++a)
#pragma unroll
    for (int b = 0; b < 3; ++b) acc[a][b] = f4{0.f, 0.f, 0.f, 0.f};
  bf8 A[3][NS], B[3][3];
  auto ldf = [&](int buf, int st, int kk) {
    const int rs = (st + 3) & 3, h = rs >> 1;
    const u32 of = (rs & 1) * SLOT;
    B[buf][0] = ld_tr8(aB[h] + of + kk * 4096, 512);
    A[buf][0] = ld_b128((kk ? aA1[h] : aA0[h]) + of + s0 * 2048);
    B[buf][1] = ld_tr8(aB[h] + of + kk * 4096 + 8192, 512);
    B[buf][2] = ld_tr8(aB[h] + of + kk * 4096 + 16384, 512);
#pragma unroll
    for (int a = 1; a < NS; ++a) A[buf][a] = ld_b128((kk ? aA1[h] : aA0[h]) + of + (s0 + a) * 2048);
  };
  auto mma = [&](int buf, int b0, int b1) {
    if (MORD == 1) {
#pragma unroll
      for (int b = b0; b < b1; ++b)
#pragma unroll
        for (int i = 0; i < NS; ++i) {
          const int a = (b & 1) ? NS - 1 - i : i;
          acc[a][b] = b < 2 ? mfma(B[buf][b], A[buf][a], acc[a][b]) : mfma(A[buf][a], B[buf][b], acc[a][b]);
        }
    } else if (MORD == 2) {
#pragma unroll
      for (int a = 0; a < NS; ++a)
#pragma unroll
        for (int i = 0; i < 2; ++i) { const int b = (a & 1) ? 1 - i : i; acc[a][b] = mfma(B[buf][b], A[buf][a], acc[a][b]); }
      if (b1 == 3) {
#pragma unroll
        for (int a = 0; a < NS; ++a) acc[a][2] = mfma(A[buf][a], B[buf][2], acc[a][2]);
      }
    } else {
#pragma unroll
    for (int a = 0; a < NS; ++a)
#pragma unroll
      for (int b = b0; b < b1; ++b)
        acc[a][b] = b < 2 ? mfma(B[buf][b], A[buf][a], acc[a][b]) : mfma(A[buf][a], B[buf][b], acc[a][b]);
    }
  };
  auto pattern = [&]() {
#pragma unroll
    for (int i = 0; i < NS + 6; ++i) {
      if (i < 3 * NS) __builtin_amdgcn_sched_group_barrier(0x008, 1, 0);
      __builtin_amdgcn_sched_group_barrier(0x100, 1, 0);
    }
    if (3 * NS > NS + 6) __builtin_amdgcn_sched_group_barrier(0x008, 2 * NS - 6, 0);
  };

  STAMP(1);
  PST(4);
  if (PBRK && NS == 4) NEW_PAGE();
  lds_bar();
  PST(5);
  STAMP(2);
  CSTAMP(0);
  ldf(0, 0, 0);
#pragma unroll
  for (int s = 0; s < 9; ++s) {
    SB();
    ldf(1, s, 1);
    mma(0, 0, 3);
    if (s == 8 && NS == 3) {
#pragma unroll
      for (int i = 0; i < 9 - S8D; ++i) {
        __builtin_amdgcn_sched_group_barrier(0x008, 1, 0);
        if (i < S8D) __builtin_amdgcn_sched_group_barrier(0x100, 2, 0); else __builtin_amdgcn_sched_group_barrier(0x100, 1, 0);
      }
    } else pattern();
    SB();
    if (s == 0 && !INIT1) { at.template init<NS>(lane, n, tau, edge, tp, parity, j); SB(); }
    if (s == 8) break;
    if (s >= 1) KST(2 * s - 1);
    if (s >= 6) EST(2 * s - 11);
    if (s < 3) PST(6 + 2 * s);
    if (NS == 3 && s >= PACE0 && PACE) asm volatile(".rept %0\n\ts_nop 7\n\t.endr" :: "i"(PACE));
    bar();
    if (PBRK && NS == 3 && s == 7) NEW_PAGE();
    if (s < 3) PST(7 + 2 * s);
    KST(2 * s);
    if (s >= 5) EST(2 * s - 10);
    SB();
    ldf(0, s + 1, 0);
    mma(1, 0, 3);
    pattern();
    SB();
  }
  u32 inner = (n + 1) & 2;
  SPIN(inner);
#define PRIO(PI, PO) asm volatile("s_setprio %1\n\t.if %1 != %2\n\ts_cmp_lg_u32 %0, 0\n\ts_cbranch_scc1 1f\n\ts_setprio %2\n\t1:\n\t.endif" :: "s"(inner), "i"(PI), "i"(PO) : "scc")
  u32 lev = 0;
  auto step = [&]() { ++lev; asm volatile("ds_write_b32 %0, %1" :: "v"(aF), "v"(lev) : "memory"); };
  PRIO(2, 3);
  KST(15);
  EST(5);
  if (NS == 4)
    asm volatile("s_mov_b32 m0, %8\n\ts_mov_b32 exec_lo, 0x10001\n\ts_mov_b32 exec_hi, 0x10001\n\t"
                 "ds_write_addtid_b32 %0\n\tds_write_addtid_b32 %1 offset:4\n\tds_write_addtid_b32 %2 offset:8\n\tds_write_addtid_b32 %3 offset:12\n\t"
                 "ds_write_addtid_b32 %4 offset:16\n\tds_write_addtid_b32 %5 offset:20\n\tds_write_addtid_b32 %6 offset:24\n\tds_write_addtid_b32 %7 offset:28\n\t"
                 "s_mov_b64 exec, -1"
                 :: "v"(acc[0][0][0]), "v"(acc[0][0][1]), "v"(acc[0][0][2]), "v"(acc[0][0][3]), "v"(acc[0][1][0]), "v"(acc[0][1][1]), "v"(acc[0][1][2]), "v"(acc[0][1][3]),
                    "s"(L_CQ + (n >> 1) * 256 + (n & 1) * 32) : "memory");
  SB();
  if (PBRK && NS == 4) NEW_PAGE();
  u4 on; on.x = on.y = on.z = on.w = 0xbf80bf80u;
  if (NS == 3) PIN(on);
  const bf8 ones = __builtin_bit_cast(bf8, on);
  SB();
  __builtin_amdgcn_s_waitcnt(0xc07f);
  EST(7);
  bar();
  EST(6);
  FST(0);
  SB();
  f2 stt[NS];
  f4 cs[2];
  auto ldqk = [&](int buf, int kk) {
    B[buf][0] = ld_tr8(aB[0] + kk * 4096, 512);
    A[buf][0] = ld_b128((kk ? aA1[0] : aA0[0]) + s0 * 2048);
    B[buf][1] = ld_tr8(aB[0] + kk * 4096 + 8192, 512);
#pragma unroll
    for (int a = 1; a < NS; ++a) A[buf][a] = ld_b128((kk ? aA1[0] : aA0[0]) + (s0 + a) * 2048);
    FENCE();
  };
  auto mmcs = [&](int buf) { if (NS == 3) { cs[1] = mfma(B[buf][1], ones, cs[1]); cs[0] = mfma(B[buf][0], ones, cs[0]); } };
  if (NS == 3) { cs[1] = ld_f4(aCS + 16); cs[0] = ld_f4(aCS); FENCE(); }
  ldqk(0, 0);
  ldqk(2, 1);
#pragma unroll
  for (int a = 0; a < NS; ++a) { stt[a].x = ld_f(aST + a * 64); stt[a].y = ld_f(aST + a * 64 + 512); FENCE(); }
  B[0][2] = ld_tr8(aB[0] + 16384, 512); B[2][2] = ld_tr8(aB[0] + 4096 + 16384, 512);
  mma(1, 0, 3);
#pragma unroll
  for (int i = 0; i < 3 * NS; ++i) {
    __builtin_amdgcn_sched_group_barrier(0x008, 1, 0);
    __builtin_amdgcn_sched_group_barrier(0x100, NS == 4 ? RPM4 : RPM, 0);
  }
  SB();
  mmcs(1);
  SB();
  FST(1);
  mma(0, 0, 2);
  mmcs(0);
  SB();
  u32 lvl;
  auto reached = [&](u32 v) { return __builtin_expect((u32)__builtin_amdgcn_readfirstlane(lvl) >= v, 1); };
  mmcs(2);
  SB();
  acc[0][1] = mfma(B[2][1], A[2][0], acc[0][1]); acc[0][0] = mfma(B[2][0], A[2][0], acc[0][0]);
  SB();
#pragma unroll
  for (int a = 1; a < NS; ++a) { acc[a][1] = mfma(B[2][1], A[2][a], acc[a][1]); SB(); }
#pragma unroll
  for (int i = 0; i < 5; ++i) {
    if (i + 1 < NS) acc[i + 1][0] = mfma(B[2][0], A[2][i + 1], acc[i + 1][0]);
    if (NS == 4) {
      if (i >= 3) acc[0][2] = mfma(A[2 * (i - 3)][0], B[2 * (i - 3)][2], acc[0][2]);
      SB();
      if (i < 4) {
#pragma unroll
        for (int r = 2 * (i & 1); r < 2 * (i & 1) + 2; ++r) { float v = dppf<0x150>(acc[0][1 - (i >> 1)][r]); PIN(v); cs[1 - (i >> 1)][r] = v; }
      }
    }
    SB();
  }
  PIN(cs[0]); PIN(cs[1]);
#pragma unroll
  for (int a = 0; a < NS; ++a) PIN(stt[a]);
#pragma unroll
  for (int a = 0; a < NS; ++a) { PIN(acc[a][0]); PIN(acc[a][1]); PIN(acc[a][2]); }
  SB();
  CSTAMP(1);
  STAMP(3);
  FST(2);
  auto bc4 = [](float v) { return f4{v, v, v, v}; };
  auto pk4 = [](f4 v) { u2 r; r.x = pk2(v[0], v[1]); r.y = pk2(v[2], v[3]); return r; };
  u2 q[NS], kLo[NS], kHi[NS + 1];
  auto lnq = [&]() {
#pragma unroll
    for (int a = 0; a < NS; ++a) {
      if (QSC) {
#pragma unroll
        for (int r = 0; r < 4; ++r) { float t = fmaf(stt[a].x, cs[0][r], acc[a][0][r]); PIN(t); acc[a][0][r] = t; }
      } else { acc[a][0] = __builtin_elementwise_fma(bc4(stt[a].x), cs[0], acc[a][0]); PIN(acc[a][0]); }
    }
    SB();
  };
  {
    u2 k[NS];
    if (KSC) {
#pragma unroll
      for (int a = 0; a < NS; ++a)
#pragma unroll
        for (int r = 0; r < 4; ++r) { float t = fmaf(stt[a].x, cs[1][r], acc[a][1][r]); PIN(t); acc[a][1][r] = t; }
      SB();
#pragma unroll
      for (int a = 0; a < NS; ++a)
#pragma unroll
        for (int r = 0; r < 4; ++r) { float t = acc[a][1][r] * stt[a].y; PIN(t); acc[a][1][r] = t; }
      SB();
    } else {
#pragma unroll
      for (int a = 0; a < NS; ++a) { acc[a][1] = __builtin_elementwise_fma(bc4(stt[a].x), cs[1], acc[a][1]); PIN(acc[a][1]); }
      SB();
#pragma unroll
      for (int a = 0; a < NS; ++a) { acc[a][1] *= stt[a].y; PIN(acc[a][1]); }
      SB();
    }
#pragma unroll
    for (int a = NS - 1; a >= 0; --a) { k[a] = pk4(acc[a][1]); PIN(k[a]); }
    SB();
#pragma unroll
    for (int i = 0; i < NS; ++i) {
      const int a = NS - 1 - i;
      asm volatile("v_and_b32 %0, %4, %7\n\tv_and_b32 %1, %5, %7\n\t"
                   "v_and_b32_dpp %0, %4, %6 row_ror:8 row_mask:0xf bank_mask:0x3\n\tv_and_b32_dpp %1, %5, %6 row_ror:8 row_mask:0xf bank_mask:0x3\n\t"
                   "v_and_b32 %2, %4, %6\n\tv_and_b32 %3, %5, %6\n\t"
                   "v_and_b32_dpp %2, %4, %7 row_ror:8 row_mask:0xf bank_mask:0xc\n\tv_and_b32_dpp %3, %5, %7 row_ror:8 row_mask:0xf bank_mask:0xc"
                   : "=&v"(kHi[a + 1].x), "=&v"(kHi[a + 1].y), "=&v"(kLo[a].x), "=&v"(kLo[a].y) : "v"(k[a].x), "v"(k[a].y), "v"(mOwn), "v"(mRot));
      if (NS == 4 && i == 0) { st_u2(L_HK + aH, kHi[NS]); st_f(aCV, acc[0][2][0]); step(); }
    }
  }
  SB();
  lnq();
  if (NS == 3) { FENCE(); lvl = lds_peek(Y_F + n * 4); FENCE(); kHi[0] = ld_u2(L_HK + aH); FENCE(); }
  SB();
#pragma unroll
  for (int a = 0; a < NS; ++a) { q[a] = pk4(acc[a][0]); PIN(q[a]); }
  SB();
  FST(3);
  const f4 z = {0.f, 0.f, 0.f, 0.f};
  f4 t[NS];
  u4 tv[NS];
  {
    f4 d0[NS], d1[NS];
    u4 qq[NS];
    u32 c0 = tp == 0 ? 0xf149f2cau : 0u;
    SPIN(c0);
#pragma unroll
    for (int a = NS - 1; a >= 0; --a) {
      d0[a] = mfma16(kLo[a], q[a], (NS == 4 && a == 0) ? bc4(__uint_as_float(c0)) : z);
      qq[a].x = q[a].x & mLo; qq[a].y = q[a].y & mLo; qq[a].z = q[a].x & ~mLo; qq[a].w = q[a].y & ~mLo;
    }
#pragma unroll
    for (int a = NS - 1; a > 0; --a) { d1[a] = mfma(mk8(kHi[a], kHi[a + 1]), __builtin_bit_cast(bf8, qq[a]), z); tv[a].y = tv[a].w = tag; }
    tv[0].y = tv[0].w = tag;
    if (NS == 4) {
      d1[0] = mfma16(kHi[1], q[0], z);
    } else {
      if (!reached(1)) { lds_wait_ge<0>(Y_F + n * 4, 1); kHi[0] = ld_u2(L_HK + aH); }
      d1[0] = mfma(mk8(kHi[0], kHi[1]), __builtin_bit_cast(bf8, qq[0]), z);
    }
#pragma unroll
    for (int a = NS - 1; a >= 0; --a) {
#pragma unroll
      for (int r = 0; r < 4; ++r) t[a][r] = fsel(mS[r], d0[a][r], d1[a][r]);
      PIN(t[a]);
    }
  }
  SB();
  FST(4);
#pragma unroll
  for (int a = NS - 1; a >= 0; --a) { tv[a].x = pk2(t[a][0], t[a][1]); tv[a].z = pk2(t[a][2], t[a][3]); PIN(tv[a]); }
  if (__builtin_expect(edge, 1)) {
#pragma unroll
    for (int a = NS - 1; a >= 0; --a) __builtin_amdgcn_raw_buffer_store_b128(tv[a], rws, xg + a * 512, 0, 16);
  }
  SB();
#pragma unroll
  for (int a = 0; a < NS; ++a)
    asm volatile("ds_write2st64_b32 %0, %1, %2 offset0:%3 offset1:%4" :: "v"(aXw), "v"(tv[a].x), "v"(tv[a].z), "i"(2 * a), "i"(2 * a + 1) : "memory");
  step();
  STAMP(10);
  FST(5);
  SB();
  if (NS == 3) PRIO(Q2I, Q2O); else PRIO(2, PR2);
  f4 vm[NS], vr[NS];
  const float csv = ld_f((aCV & 0xffffu) | (L_CS & ~0xffffu));
#pragma unroll
  for (int a = 0; a < NS; ++a) { vm[a] = ld_f4(aMR + a * 64); vr[a] = ld_f4(aMR + 512 + a * 64); }
  SB();
#pragma unroll
  for (int bi = 1; bi < 3; ++bi)
#pragma unroll
    for (int a = NS - 1; a >= (NS == 4 ? 1 : 0); --a) { const int buf = bi == 0 ? 1 : (bi == 1 ? 0 : 2); acc[a][2] = mfma(A[buf][a], B[buf][2], acc[a][2]); }
  m0.x = (mS.x & 0xffffu) | (mS.y & 0xffff0000u); m0.y = (mS.z & 0xffffu) | (mS.w & 0xffff0000u);
  mm.x = ~m0.x & mLo; mm.y = ~m0.y & mLo; mp.x = ~(m0.x | mLo); mp.y = ~(m0.y | mLo);
  u32 uLo = 0x3f80u, uHi = 0x3f800000u;
  u2 hs; hs.x = hs.y = (lane < 32 ? mV : ~mV) & 0x3f803f80u;
  u32 aFn = Y_F + s0 * 4 + min(max(n + (lane < 32 ? -1 : 1), 0), 3) * 4;
  u32 xr = xg + (n ? 7 * 512 : -7 * 512);
  float cq[NS];
#pragma unroll
  for (int a = 0; a < NS; ++a) { cq[a] = stt[a].y * (0.22360679774997896f * 1.4426950408889634f); PIN(cq[a]); }
  PIN(m0); PIN(mm); PIN(mp); PIN(uLo); PIN(uHi); PIN(hs); PIN(aFn); PIN(xr);
#pragma unroll
  for (int a = 0; a < NS; ++a) PIN(acc[a][2]);
  SB();
  FST(6);
  u4 e[NS];
#pragma unroll
  for (int a = 0; a < NS; ++a) asm volatile("" : "=v"(e[a]));
  auto look = [&]() {
#pragma unroll
    for (int a = 0; a < NS; ++a) e[a] = __builtin_amdgcn_raw_buffer_load_b128(rws, xr + a * 512, 0, 16);
  };
  u2 vLo[NS], vHi[NS + 1];
#pragma unroll
  for (int a = NS - 1; a >= 0; --a) { acc[a][2] = __builtin_elementwise_fma(vm[a], bc4(csv), acc[a][2]); PIN(acc[a][2]); }
  SB();
#pragma unroll
  for (int a = NS - 1; a >= 0; --a) { acc[a][2] *= vr[a]; PIN(acc[a][2]); }
  SB();
#pragma unroll
  for (int a = NS - 1; a >= 0; --a) { vLo[a] = pk4(acc[a][2]); PIN(vLo[a]); }
  SB();
  if (__builtin_expect(edge, 1)) look();
  SB();
  constexpr u32 XT = NS == 4 ? 2 : 1;
  u2 y[NS];
  u32 cx;
  auto ldy = [&]() {
    FENCE(); cx = lds_peek(aFn); FENCE();
#pragma unroll
    for (int a = 0; a < NS; ++a) { y[a].x = ld_u(aXr + a * 512); y[a].y = ld_u(aXr + a * 512 + 256); }
    FENCE();
  };
  ldy();
  SB();
#pragma unroll
  for (int a = NS - 1; a >= 0; --a) {
    vHi[a + 1].x = vLo[a].x & ~mV; vHi[a + 1].y = vLo[a].y & ~mV; vLo[a].x &= mV; vLo[a].y &= mV;
    PIN(vLo[a]); PIN(vHi[a + 1]);
  }
  SB();
#pragma unroll
  for (int a = NS - 1; a >= 0; --a) {
    auto wx = __builtin_amdgcn_permlane32_swap(vLo[a].x, vHi[a + 1].x, false, false);
    auto wy = __builtin_amdgcn_permlane32_swap(vLo[a].y, vHi[a + 1].y, false, false);
    vLo[a].x = wx[0]; vLo[a].y = wy[0]; vHi[a + 1].x = wx[1]; vHi[a + 1].y = wy[1];
    PIN(vLo[a]); PIN(vHi[a + 1]);
    if (NS == 4 && a == NS - 2) { st_u2(L_HV + aH, vHi[NS]); step(); }
  }
  if (NS == 3) { FENCE(); lvl = lds_peek(Y_F + n * 4); FENCE(); vHi[0] = ld_u2(L_HV + aH); FENCE(); }
  bf8 vH[NS];
#pragma unroll
  for (int a = 1; a < NS; ++a) { vH[a] = mk8(vHi[a], vHi[a + 1]); PIN(vH[a]); }
  SB();
  FST(7);
  if (__any(cx < XT)) {
#pragma nounroll
    for (u32 spin = 0; spin < (1u << 20); ++spin) {
      FENCE();
      if (__all(lds_peek(aFn) >= XT)) break;
    }
    ldy();
  }
  auto addp = [](f4& t, u32 a, u32 b, u32 lo, u32 hi) {
    t[0] = __builtin_amdgcn_fdot2_f32_bf16(__builtin_bit_cast(bf2, a), __builtin_bit_cast(bf2, lo), t[0], false);
    t[1] = __builtin_amdgcn_fdot2_f32_bf16(__builtin_bit_cast(bf2, a), __builtin_bit_cast(bf2, hi), t[1], false);
    t[2] = __builtin_amdgcn_fdot2_f32_bf16(__builtin_bit_cast(bf2, b), __builtin_bit_cast(bf2, lo), t[2], false);
    t[3] = __builtin_amdgcn_fdot2_f32_bf16(__builtin_bit_cast(bf2, b), __builtin_bit_cast(bf2, hi), t[3], false);
  };
#pragma unroll
  for (int a = 0; a < NS; ++a) { addp(t[a], y[a].x, y[a].y, uLo, uHi); PIN(t[a]); }
  if (NS == 3) {
    if (!reached(3)) { lds_wait_ge<0>(Y_F + n * 4, 3); vHi[0] = ld_u2(L_HV + aH); }
    vH[0] = mk8(vHi[0], vHi[1]); PIN(vH[0]);
  }
  if (NS == 3) { if (Q3I != Q2I || Q3O != Q2O) PRIO(Q3I, Q3O); } else if (PR3 != PR2) PRIO(2, PR3);
  STAMP(8);
  FST(8);
  if (__builtin_expect(edge, 1)) {
    u32 etag = (int)xg < 0 ? 0u : tag;
    PIN(etag);
    auto bad = [&]() {
      u32 b = __builtin_amdgcn_bitop3_b32(e[0].y, e[0].w, etag, 0x7e);
#pragma unroll
      for (int a = 1; a < NS; ++a) b |= __builtin_amdgcn_bitop3_b32(e[a].y, e[a].w, etag, 0x7e);
      PIN(b);
      return __any(b != 0u);
    };
    if (bad()) {
#pragma nounroll
      for (u32 spin = 0; spin < SPIN_CAP; ++spin) {
        FENCE();
        look();
        if (!bad()) break;
      }
    }
#pragma unroll
    for (int a = 0; a < NS; ++a) { addp(t[a], e[a].x, e[a].z, uLo, uHi); PIN(t[a]); }
  }
  STAMP(6);
  FST(9);
  SB();
  float mx[4];
#pragma unroll
  for (int a = 0; a < NS; ++a) { mx[a] = vmax(vmax(t[a][0], t[a][1]), t[a][2]); PIN(mx[a]); }
#pragma unroll
  for (int a = 0; a < NS; ++a) { mx[a] = vmax(mx[a], t[a][3]); PIN(mx[a]); }
  if (NS == 3) mx[3] = mx[2];
  SB();
  {
    u32 c[2];
#pragma unroll
    for (int h = 0; h < 2; ++h) {
      auto w = __builtin_amdgcn_permlane16_swap(__float_as_uint(mx[2 * h]), __float_as_uint(mx[2 * h + 1]), false, false);
      mx[2 * h] = __uint_as_float(w[0]); mx[2 * h + 1] = __uint_as_float(w[1]);
    }
    SB();
#pragma unroll
    for (int h = 0; h < 2; ++h) c[h] = __float_as_uint(vmax(mx[2 * h], mx[2 * h + 1]));
    SB();
#pragma unroll
    for (int h = 0; h < 2; ++h) {
      if (NS == 3 && h == 1) { mx[2] = __uint_as_float(c[1]); continue; }
      auto w = __builtin_amdgcn_permlane16_swap(c[h], c[h], false, false);
      mx[2 * h] = __uint_as_float(w[0]); mx[2 * h + 1] = __uint_as_float(w[1]);
    }
    SB();
  }
#pragma unroll
  for (int a = 0; a < NS; ++a) { mx[a] *= -cq[a]; PIN(mx[a]); }
  SB();
#pragma unroll
  for (int a = 0; a < NS; ++a) { t[a] = __builtin_elementwise_fma(t[a], bc4(cq[a]), bc4(mx[a])); PIN(t[a]); }
  SB();
#pragma unroll
  for (int a = 0; a < NS; ++a) {
#pragma unroll
    for (int r = 0; r < 4; ++r) t[a][r] = __builtin_amdgcn_exp2f(t[a][r]);
    PIN(t[a]);
  }
  SB();
  FST(10);
  u2 pp[NS], p0[NS];
  bf8 p1[NS];
#pragma unroll
  for (int a = 0; a < NS; ++a) { pp[a] = pk4(t[a]); PIN(pp[a]); }
  SB();
  f4 o[NS], sm[NS];
#pragma unroll
  for (int a = 0; a < NS; ++a) {
    sm[a] = mfma16(hs, pp[a], z);
    p0[a] = and2(pp[a], m0); PIN(p0[a]);
    SB();
  }
#pragma unroll
  for (int a = 0; a < NS; ++a) {
    o[a] = mfma16(vLo[a], p0[a], z);
    if (NS == 4 && a == 0) { p0[0] = and2(pp[0], mp); PIN(p0[0]); }
    else { p1[a] = mk8(and2(pp[a], mm), and2(pp[a], mp)); PIN(p1[a]); }
    SB();
  }
#pragma unroll
  for (int a = 0; a < NS; ++a) {
    o[a] = (NS == 4 && a == 0) ? mfma16(vHi[1], p0[0], o[a]) : mfma(vH[a], p1[a], o[a]);
    mx[a] = __builtin_amdgcn_rcpf(sm[a][0]); PIN(mx[a]);
    SB();
  }
  u2 ob[4];
#pragma unroll
  for (int a = 0; a < NS; ++a) { o[a] *= mx[a]; PIN(o[a]); }
  SB();
#pragma unroll
  for (int a = 0; a < NS; ++a) { ob[a] = pk4(o[a]); PIN(ob[a]); }
  if (NS == 3) ob[3] = ob[2];
  SB();
#pragma unroll
  for (int h = 0; h < 2; ++h) {
    auto wx = __builtin_amdgcn_permlane32_swap(ob[2 * h].x, ob[2 * h + 1].x, false, false);
    auto wy = __builtin_amdgcn_permlane32_swap(ob[2 * h].y, ob[2 * h + 1].y, false, false);
    u4 v; v.x = wx[0]; v.y = wy[0]; v.z = wx[1]; v.w = wy[1];
    __builtin_amdgcn_raw_buffer_store_b128(v, rws, so[h], 0, 16);
  }
  CSTAMP(2);
  STAMP(4);
  FST(11);
  __builtin_amdgcn_s_setprio(0);
  SB();
  if (PBR2) NEW_PAGE();
  if (GWT) lds_add(Y_ST, 1);
  SB();
  {
    const u32 r0 = tp * 104, mq = (r0 >> 6) + (lane >> 3);
    u32 old, tv1 = tag;
    u32 fo = WS_BOX + (parity * 16 + (r0 >> 6)) * 2048 + (j >= 5 ? 64 : 0) + rb * 4 + lane * 256 + (mq * 64 < r0 ? 20 : 0);
    fo |= mq * 64 > min(r0 + 103u, 1023u) ? 0x80000000u : 0u;
    PIN(fo); PIN(tv1);
    g2.init(wave, mg, ng);
    SB();
    __builtin_amdgcn_s_setprio(3);
    asm volatile("s_mov_b64 exec, 1\n\ts_waitcnt vmcnt(0)\n\tds_add_rtn_u32 %0, %1, %2\n\ts_mov_b32 exec_lo, 0xffffff\n\ts_waitcnt lgkmcnt(0)\n\t"
                 "v_readfirstlane_b32 vcc_lo, %0\n\ts_cmp_lg_u32 vcc_lo, 7\n\ts_cbranch_scc1 1f\n\t"
                 "buffer_atomic_swap %3, %4, %5, 0 offen\n1:\n\ts_mov_b64 exec, -1"
                 : "=&v"(old) : "v"(g2.pa), "v"(g2.one), "v"(tv1), "v"(fo), "s"(srd(p.ws)) : "scc", "vcc", "memory");
  }
  STAMP(5);
  FST(12);
}

constexpr u32 A_D = 0, A_G = L_WO, A_U = L_WO + 40960;
constexpr u32 A_XN = 40960;
constexpr u32 A_RED = A_XN + 2560;
constexpr u32 A_H = A_RED + 2048;
constexpr u32 A_TOP = A_H + 1024;

DEV void aux_loader(Params& p, int lane, int lw, int a7, int f, bool ffn, int mg, int ng, int koff, Epi& ei STM_ARG) {
  const u32 ep = epoch_get(p);
  const u4 srdo = srd(p.wo), srdg = srd(p.wg), srdu = srd(p.wu), srdd = srd(p.wd);
  const int rr = lane >> 2;
  u32 vg = (lw * 16 + rr) * (F_ * 2) + f * 64 + (((lane & 3) ^ (((rr >> 3) & 1) << 1)) << 4);
  u32 vd = (f * 32 + lw * 8 + (lane >> 3)) * 1280 + (lane & 7) * 16;
  u32 vt = min((f * 4 + lw) * 30 + min(lane, 29), 6399) * 128;
  u32 ho = (lane | lw) ? 0x80000000u : WS_FLAG + (200 + f) * 256 + 128;
  PIN(vg); PIN(vd); PIN(vt); PIN(ho);
  ei.init(lane, lw, mg, ng);
  WoDma wo; wo.init(lane, lw, ng, 0, 104 + lw);
  u32 tag = epoch_tag(ep);
  PIN(tag);
  if (FFN_DELAY) __builtin_amdgcn_s_sleep(FFN_DELAY);
  STAMP(9);
  if (ffn) {
    const u4 s0 = (f & 1) ? srdu : srdg, s1 = (f & 1) ? srdg : srdu;
    const u32 a0 = ((f & 1) ? A_U : A_G) + lw * 1024, a1 = ((f & 1) ? A_G : A_U) + lw * 1024;
    dma_run<10, 4096, 64 * F_ * 2, 0, TH0>(a0, vg, s0, 0);
    asm volatile("s_mov_b32 m0, %0\n\ts_nop 0\n\tbuffer_load_dword %1, %2, 0 offen lds" :: "s"(L_DUMP + lw * 256), "v"(vt), "s"(srdo));
    STAMP(10);
    dma_run<10, 4096, 64 * F_ * 2, 0, TH1>(a1, vg, s1, 0);
    if (!DLATE) dma_run<10, 4096, 128>(A_D + lw * 1024, vd, srdd, 0);
    STAMP(1);
  } else {
    wo.issue<0, 27, TH0>(srdo, lw);
  }
  wait_bar<DLATE ? 0 : 10>();
  if (DLATE && ffn) dma_run<10, 4096, 128>(A_D + lw * 1024, vd, srdd, 0);
  asm volatile("buffer_store_dword %0, %1, %2, 0 offen sc1" :: "v"(tag), "v"(ho), "s"(srd(p.ws)));
  if (BOXW == 2) asm volatile("buffer_atomic_add %0, %1, %2, 0 offen" :: "v"(0u), "v"((lane | (lw & 2)) ? 0x80000000u : WS_BOX + (mg * 8 + ng) * 256 + lw * 64), "s"(srd(p.ws)));
  bar();
  STAMP(2);
  ei.issue(srd(p.x), srd(p.ws), WS_ST2);
  wo.issue<0, WOC, AW0, 1>(srdo, lw, f);
  asm volatile("s_cmp_gt_u32 %0, 53\n\ts_cbranch_scc1 1f\n\ts_waitcnt vmcnt(%1)\n\ts_barrier\n\ts_branch 2f\n1:\n\ts_waitcnt vmcnt(0)\n2:" :: "s"(f), "i"(WOC + 5) : "scc", "memory");
  STAMP(3);
  ei.wait<WOC>();
  ei.check(p.ws.p, tag);
  ei.tag = tag;
  SB();
#pragma unroll
  for (int i = 0; i < YG; ++i) ei.gate(i);
  SB();
  asm volatile("buffer_load_dwordx4 %0, off, %1, 0 offset:%2 sc1" : "=v"(ei.fin) : "s"(srd(p.ws)), "i"(WS_FIN));
  static_assert((26 - WOC) % 3 == 0, "");
  const u32 skip = lw == 3 ? 99 : f;
  wo.issue<WOC, 27, AWT, 1, 3>(srdo, lw, skip);
  asm volatile("s_cmp_gt_u32 %1, 53\n\ts_cbranch_scc0 1f\n\ts_waitcnt vmcnt(1)\n1:\n\ts_waitcnt vmcnt(%2)" : "+v"(ei.fin) : "s"(skip), "i"((26 - WOC) * 4 / 3 + 1) : "scc");
  STAMP(4);
}

DEV void aux_compute(const Params& p, rsrc_t rws, u32& tag, int lane, int wave, int f, bool ffn, int xcd, int a7, RowIn& rin STM_ARG) {
  const int task = a7 + 7 * wave;
  if (task < 32) rin.stats(p, rws, tag, lane, xcd * 256 + task * 8, A_TOP + wave * 16 STM);
  else tag = epoch_tag(epoch_get(p));
  PST(4);
  const int g = lane >> 4, i16 = lane & 15;
  const int q = i16 >> 2, pp = lane & 3;
  const int t = wave & 1, kq = wave >> 1;
  u32 ab = (kq * 160 + 8 * g + q) * 64 + (((2 * t + (pp >> 1)) ^ ((g & 1) << 1)) << 4) + (pp & 1) * 8;
  u32 xb = A_XN + (i16 & 1) * 1280 + (kq * 160 + 8 * g) * 2;
  u32 ar = i16 < 2 ? A_RED + (wave * 16 + g * 2 + i16) * 16 : L_NONE + lane * 16;
  u32 ad[5], ah[5];
#pragma unroll
  for (int i = 0; i < 5; ++i) {
    const int nt = wave * 5 + i;
    ad[i] = A_D + (8 * g + q) * 128 + pp * 8 + (nt >> 2) * 4096 + (nt & 3) * 32;
    ah[i] = (i16 & 7) == i ? A_H + wave * 128 + (i16 >> 3) * 64 + g * 16 : L_NONE + lane * 16;
    PIN(ad[i]); PIN(ah[i]);
  }
  u32 as = A_RED + (lane >> 5) * 256 + (lane & 31) * 4, aw = A_H + wave * 128 + ((lane >> 2) & 1) * 64 + (4 * (lane >> 3) + (lane & 3)) * 2;
  u32 yo = (i16 & 7) < 5 ? WS_YP + ((wave * 5 + (i16 & 7)) * 56 + f) * 128 + (i16 >> 3) * 64 + g * 16 : 0x80000000u;
  PIN(ab); PIN(xb); PIN(ar); PIN(as); PIN(aw); PIN(yo);
  STAMP(1);
  u32 so = 0x80000000u, xo = 0x80000000u;
  if (wave >= 5) {
    if (wave == 5) st_u(L_SYNC + lane * 4, 0);
    const u32 nt = (a7 < 4 && wave == 5) ? 5 : 4, a = A_TOP + min((u32)lane & 7, nt - 1) * 16;
    u4 k;
#pragma nounroll
    for (u32 spin = 0; spin < (1u << 20); ++spin) {
      FENCE();
      k = __builtin_bit_cast(u4, ld_f4(a));
      if (__all(k.y == tag && k.w == tag)) break;
      __builtin_amdgcn_s_sleep(1);
    }
    if (wave == 5) {
      if ((lane & 7) >= nt) k.x = k.z = 0;
      u32 k1 = k.x, k2 = k.z;
      top2_merge(k1, k2, dppu<0xB1>(k1), dppu<0xB1>(k2));
      top2_merge(k1, k2, dppu<0x4E>(k1), dppu<0x4E>(k2));
      top2_merge(k1, k2, dppu<0x141>(k1), dppu<0x141>(k2));
      k.x = k1; k.z = k2;
      bst128_sc1(rws, lane < 8 ? WS_KEY + f * 128 + lane * 16 : 0x80000000u, k);
    } else {
      if (YTCH && wave == 7) { u32 d = __builtin_amdgcn_raw_buffer_load_b32(rws, f >= 40 ? 0x80000000u : lane < 54 ? WS_YP + (f * 56 + lane) * 128 : WS_GR + f * 128, 0, 16); PIN(d); }
      __builtin_amdgcn_s_sleep(KEY_DELAY - (YTCH ? 16 : 0));
      if (YTCH && wave == 6) __builtin_amdgcn_s_sleep(16);
#pragma nounroll
      for (u32 spin = 0;; ++spin) {
        FENCE();
        k = bld128_sc1(rws, WS_KEY + min(lane, 55) * 128);
        if (__all(k.y == tag && k.w == tag) || spin > SPIN_CAP) break;
        __builtin_amdgcn_s_sleep(8);
      }
      u32 k1 = lane < 56 ? k.x : 0, k2 = lane < 56 ? k.z : 0;
      top2_wave(k1, k2);
      k.x = k1; k.z = k2;
      if (f == 55) bst128_sc1(rws, (lane | t * 8) < 8 ? WS_FIN + lane * 16 : 0x80000000u, k);
      const int tk = key_tok(__builtin_amdgcn_readfirstlane(t ? k2 : k1));
      xo = tk * 1280; so = WS_ST2 + ((tk & 1) * 1024 + (tk >> 1)) * 16;
    }
  }
  const rsrc_t rx = __builtin_amdgcn_make_buffer_rsrc(p.x.p, 0, S_ * D_ * 2, 0x00020000);
  const u4 xa = __builtin_amdgcn_raw_buffer_load_b128(rx, xo + lane * 16, 0, 0);
  const u32 xc = __builtin_amdgcn_raw_buffer_load_b32(rx, xo + 1024 + lane * 4, 0, 0);
  u4 sv = bld128_sc1(rws, so);
  FENCE();
  STAMP(2);
  NEW_PAGE();
  lds_bar();
  STAMP(3);
  bf8 Wg[5], Wu[5];
#pragma unroll
  for (int ks = 0; ks < 5; ++ks) { Wg[ks] = ld_tr8(A_G + ab + ks * 2048, 256); Wu[ks] = ld_tr8(A_U + ab + ks * 2048, 256); }
#pragma unroll
  for (int ks = 0; ks < 5; ++ks) { PIN(Wg[ks]); PIN(Wu[ks]); }
  bar();
  if (__builtin_expect(wave >= 6, 1)) {
    if (__builtin_expect(__any(__builtin_amdgcn_bitop3_b32(sv.y, sv.w, tag, 0x7e) != 0u), 0)) {
#pragma nounroll
      for (u32 spin = 0; !__all(sv.y == tag && sv.w == tag) && spin < SPIN_CAP; ++spin) {
        FENCE();
        __builtin_amdgcn_s_sleep(1);
        sv = bld128_sc1(rws, so);
      }
    }
    const float m = __uint_as_float(sv.x), r_ = __uint_as_float(sv.z);
    u4 o; o.x = norm2(xa.x, m, r_); o.y = norm2(xa.y, m, r_); o.z = norm2(xa.z, m, r_); o.w = norm2(xa.w, m, r_);
    st_u4(A_XN + t * 1280 + lane * 16, o); st_u(A_XN + t * 1280 + 1024 + lane * 4, norm2(xc, m, r_));
    lds_add(Y_CB, 1);
  }
  if (__builtin_expect(!ffn, 0)) {
    if (f == 55 && wave == 5) {
#pragma nounroll
      for (int i = 0; i < HELLO_DELAY; ++i) __builtin_amdgcn_s_sleep(8);
#pragma nounroll
      for (u32 spin = 0;; ++spin) {
        FENCE();
        u32 bad = 0;
#pragma unroll
        for (int i = 0; i < 4; ++i) bad |= __builtin_amdgcn_raw_buffer_load_b32(rws, WS_FLAG + 128 + (i * 64 + lane) * 256, 0, 16) ^ tag;
        if (__all(bad == 0u) || spin > SPIN_CAP) break;
        __builtin_amdgcn_s_sleep(16);
      }
      if (lane == 0) gst32(p.ws.p + WS_EPOCH, tag);
    }
    lds_add(Y_PUB, 1);
    return;
  }
  lds_wait_ge<1>(Y_CB, 2);
  STAMP(8);
  {
    bf8 xb8[5];
#pragma unroll
    for (int ks = 0; ks < 5; ++ks) xb8[ks] = ld_b128(xb + ks * 64);
    SB();
    f4 ag = {0.f, 0.f, 0.f, 0.f}, au = {0.f, 0.f, 0.f, 0.f};
#pragma unroll
    for (int ks = 0; ks < 5; ++ks) { ag = mfma(Wg[ks], xb8[ks], ag); au = mfma(Wu[ks], xb8[ks], au); }
    SB();
    st_f4(ar, ag); st_f4(ar + 128, au);
  }
  STAMP(5);
  lds_bar();
  STAMP(6);
  {
    float a[4], b[4];
#pragma unroll
    for (int k = 0; k < 4; ++k) a[k] = ld_f(as + k * 512);
#pragma unroll
    for (int k = 0; k < 4; ++k) b[k] = ld_f(as + k * 512 + 128);
    bf8 Wd[5], hb[5];
#pragma unroll
    for (int i = 0; i < 5; ++i) Wd[i] = ld_tr8(ad[i], 512);
    SB();
    const float hh = rbf(silu_f(rbf((a[0] + a[1]) + (a[2] + a[3])))) * rbf((b[0] + b[1]) + (b[2] + b[3]));
    *(LAS u16*)(uintptr_t)aw = (u16)(pk2(hh, hh) & 0xffffu);
    FENCE();
#pragma unroll
    for (int i = 0; i < 5; ++i) hb[i] = ld_b128(ah[i]);
    SB();
    f4 y = {0.f, 0.f, 0.f, 0.f};
#pragma unroll
    for (int i = 0; i < 5; ++i) y = mfma(Wd[i], hb[i], y);
    u4 v; v.x = pk2(y[0], y[1]); v.z = pk2(y[2], y[3]); v.y = v.w = tag;
    bst128_sc1(rws, yo, v);
  }
  STAMP(4);
  lds_add(Y_PUB, 1);
}

DEV void gemm2(const Params& p, rsrc_t rws, u32 tag, int wave, int ng, G2& c STM_ARG) {
  const int m = wave & 3, kh = wave >> 2;
  const u32 b0 = c.b0, oo = c.oo, fo = c.fo, gb = c.gb, pa = c.pa;
  u32 pm = c.pm;
  u64 pm64 = pm;
  SPIN(pm64);
  u4 of[10];
#define LG "s_nop " STR(LGAP) "\n\t"
#define LOADS "buffer_load_dwordx4 v[124:127], %[oo], %[srd], 0 offen offset:0 sc1\n\t" LG \
              "buffer_load_dwordx4 v[128:131], %[oo], %[srd], 0 offen offset:1024 sc1\n\t" LG \
              "buffer_load_dwordx4 v[132:135], %[oo], %[srd], 0 offen offset:2048 sc1\n\t" LG \
              "buffer_load_dwordx4 v[136:139], %[oo], %[srd], 0 offen offset:3072 sc1\n\t" LG \
              "buffer_load_dwordx4 v[140:143], %[oo], %[srd], %[s4] offen offset:0 sc1\n\t" LG \
              "buffer_load_dwordx4 v[144:147], %[oo], %[srd], %[s4] offen offset:1024 sc1\n\t" LG \
              "buffer_load_dwordx4 v[148:151], %[oo], %[srd], %[s4] offen offset:2048 sc1\n\t" LG \
              "buffer_load_dwordx4 v[152:155], %[oo], %[srd], %[s4] offen offset:3072 sc1\n\t" LG \
              "buffer_load_dwordx4 v[156:159], %[oo], %[srd], %[s8] offen offset:0 sc1\n\t" LG \
              "buffer_load_dwordx4 v[160:163], %[oo], %[srd], %[s8] offen offset:1024 sc1\n\t" LG \
              ""
#define OFS(C) C "{v[124:127]}"(of[0]), C "{v[128:131]}"(of[1]), C "{v[132:135]}"(of[2]), C "{v[136:139]}"(of[3]), C "{v[140:143]}"(of[4]), C "{v[144:147]}"(of[5]), C "{v[148:151]}"(of[6]), C "{v[152:155]}"(of[7]), C "{v[156:159]}"(of[8]), C "{v[160:163]}"(of[9])
#define POLL(R) "v_mov_b32 " R ", 0\n\tbuffer_atomic_add " R ", %[fo], %[srd], 0 offen sc0\n\t"
#define CHK(R) "s_waitcnt vmcnt(2)\n\tv_cmp_ne_u32 vcc, %[tag], " R "\n\ts_cbranch_vccz 2f\n\t" POLL(R)
#define LSPIN(L, O, V) L ":\n\tds_read_b32 %[t], %[pa] offset:" O "\n\ts_sub_u32 %[n], %[n], 1\n\ts_cselect_b32 %[l], -1, 0\n\ts_waitcnt lgkmcnt(0)\n\t" \
                 "v_readfirstlane_b32 %[r], %[t]\n\ts_or_b32 %[r], %[r], %[l]\n\ts_and_b32 %[l], %[r], " V "\n\ts_cbranch_scc0 " L "b\n\t"
  static_assert(Y_WO == Y_PUB + 4, "");
  u32 n = SPIN_CAP << 5, got, t, l, sg = 256u << kh;
  GST(14);
  asm volatile("" : OFS("="));
  lds_wait_bit<1>(Y_WO, 4);
  GST(15);
#define POLL1(R) asm volatile("s_cmp_eq_u32 %[pm], 0\n\ts_cbranch_scc1 1f\n\ts_mov_b64 exec, %[pm64]\n\t" POLL(R) "s_mov_b64 exec, -1\n1:" \
               : "+{v[124:127]}"(of[0]) : [fo] "v"(fo), [srd] "s"(srd(p.ws)), [pm] "s"(pm), [pm64] "s"(pm64) : "scc", "memory")
  bf8 W[WPRE][5];
#pragma unroll
  for (int ks = 0; ks <= WPRE; ++ks) {
    SB();
    if (ks == PK0) POLL1("v124");
    if (ks == PK1) POLL1("v125");
    if (ks == PK2) POLL1("v126");
    SB();
    if (ks < WPRE) {
#pragma unroll
      for (int i = 0; i < 5; ++i) W[ks][i] = ld_tr8(b0 + ks * 5120 + i * 32, 8 * 160);
    }
  }
  SB();
  __builtin_amdgcn_s_setprio(IPRIO);
  asm volatile("s_cmp_eq_u32 %[pm], 0\n\ts_cbranch_scc1 5f\n\t"
               "s_mov_b64 exec, %[pm64]\n" LSPIN("3", "0", "8") "s_movk_i32 %[l], 0x4000\n"
               "1:\n\t" CHK("v124") CHK("v125") CHK("v126") "s_sub_u32 %[l], %[l], 1\n\ts_cbranch_scc0 1b\n"
               "2:\n\ts_mov_b64 exec, 1\n\tds_or_b32 %[pa], %[gb] offset:4\n\ts_mov_b64 exec, -1\n\t.if " STR(PDL) "\n\ts_sleep " STR(PDL) "\n\t.endif\n\ts_branch 4f\n"
               LSPIN("5", "4", "%[sg]") "4:\n\t" LOADS "s_min_u32 %[n], %[n], 0x100000\n" LSPIN("7", "4", "32")
               : OFS("+"), [n] "+s"(n), [l] "=&s"(l), [r] "=&s"(got), [t] "=&v"(t)
               : [fo] "v"(fo), [srd] "s"(srd(p.ws)), [tag] "s"(tag), [gb] "v"(gb), [pm] "s"(pm), [pm64] "s"(pm64),
                 [pa] "v"(pa), [oo] "v"(oo), [sg] "s"(sg), [s4] "s"(4096), [s8] "s"(8192) : "scc", "vcc", "memory");
  GST(0);
  STAMP(7);
  __builtin_amdgcn_s_setprio(MPRIO);
  u32 sfa = Y_S + m * 4, sfv = kh + 1;
  SPIN(sfa); SPIN(sfv);
  asm volatile("s_mov_b32 m0, %0" :: "s"(wave * ST_W));
  auto landed = [&](int ks) {
    if (ks == 0) asm volatile("s_waitcnt vmcnt(9)" : "+{v[124:127]}"(of[0]));
    if (ks == 1) asm volatile("s_waitcnt vmcnt(8)" : "+{v[128:131]}"(of[1]));
    if (ks == 2) asm volatile("s_waitcnt vmcnt(7)" : "+{v[132:135]}"(of[2]));
    if (ks == 3) asm volatile("s_waitcnt vmcnt(6)" : "+{v[136:139]}"(of[3]));
    if (ks == 4) asm volatile("s_waitcnt vmcnt(5)" : "+{v[140:143]}"(of[4]));
    if (ks == 5) asm volatile("s_waitcnt vmcnt(4)" : "+{v[144:147]}"(of[5]));
    if (ks == 6) asm volatile("s_waitcnt vmcnt(3)" : "+{v[148:151]}"(of[6]));
    if (ks == 7) asm volatile("s_waitcnt vmcnt(2)" : "+{v[152:155]}"(of[7]));
    if (ks == 8) asm volatile("s_waitcnt vmcnt(1)" : "+{v[156:159]}"(of[8]));
    if (ks == 9) asm volatile("s_waitcnt vmcnt(0)" : "+{v[160:163]}"(of[9]));
  };
  SB();
  u32 fa = sfa, fv = sfv;
  GST(1);
  f4 acc[5];
#pragma unroll
  for (int n = 0; n < 5; ++n) acc[n] = f4{0.f, 0.f, 0.f, 0.f};
  constexpr int NR = (10 - WPRE) * 10;
  u2 Wh[NR];
  auto kn = [](int i, int& ks, int& n) { if (i < 40) { ks = i / 5; n = i % 5; } else { ks = 8 + (i & 1); n = (i - 40) >> 1; } };
  auto rdh = [&](int i) { int ks, n; kn(WPRE * 5 + (i >> 1), ks, n); if (i >= 0 && i < NR) Wh[i] = ld_tr(b0 + ks * 5120 + n * 32 + (i & 1) * 1280); };
  auto put = [&](int j) { asm volatile("ds_write_addtid_b32 %0 offset:%1" :: "v"(acc[j >> 2][j & 3]), "i"((j >> 2) * ST_N + (j & 3) * 256) : "memory"); };
#pragma unroll
  for (int i = 0; i < 50; ++i) {
    int ks, n; kn(i, ks, n);
    if (n == 0) { landed(ks); SB(); }
    acc[n] = mfma(__builtin_bit_cast(bf8, of[ks]), i < WPRE * 5 ? W[ks][n] : mk8(Wh[2 * (i - WPRE * 5)], Wh[2 * (i - WPRE * 5) + 1]), acc[n]);
    const int j = i - RD0;
    rdh(j + j / RDX); if (j >= 0 && j % RDX == RDX - 1) rdh(j + j / RDX + 1);
    if (i == 41) PIN(fa);
    if (i == 42) { PIN(fv); PIN(Wh[NR - 1]); }
    SB();
    if (i >= 43) { put(2 * (i - 43)); put(2 * (i - 43) + 1); SB(); }
    if (!ARR && i % 5 == 4) GST(2 + i / 5);
  }
  put(14); put(15);
  asm volatile("s_nop 3");
#pragma unroll
  for (int j = 16; j < 20; ++j) put(j);
  lds_set(fa, fv);
  GST(12);
  lds_bar();
  GST(13);
}

DEV void reduce_ffn(const Params& p, rsrc_t rws, u32 tag, int lane, int f, u32 key STM_ARG) {
  const u32 tok = key_tok(key), so = WS_ST2 + ((tok & 1) * 1024 + (tok >> 1)) * 16;
  const u32 ya = WS_YP + f * (56 * 128) + lane * 16, yl = lane < 48 ? ya + 6144 : 0x80000000u, el = lane < 48 ? tag : 0u;
  const u2 xr = __builtin_amdgcn_raw_buffer_load_b64(__builtin_amdgcn_make_buffer_rsrc(p.x.p, 0, S_ * D_ * 2, 0x00020000), tok * 1280 + f * 32 + (lane & 3) * 8, 0, 0);
  const float sg = __builtin_amdgcn_rcpf(1.f + __expf(-key_r(key)));
  __builtin_amdgcn_s_setprio(3);
  if (RDLY) __builtin_amdgcn_s_sleep(RDLY);
  u4 w[8];
#pragma nounroll
  for (u32 spin = 0; spin < SPIN_CAP; ++spin) {
    FENCE();
    u32 b = 0;
#pragma unroll
    for (int j = 0; j < 8; ++j) w[j] = bld128_sc1(rws, j < 6 ? ya + j * 1024 : j == 6 ? yl : so);
#pragma unroll
    for (int j = 0; j < 8; ++j) b |= __builtin_amdgcn_bitop3_b32(w[j].y, w[j].w, j != 6 ? tag : el, 0x7e);
    if (!__any(b != 0u)) break;
    __builtin_amdgcn_s_sleep(2);
  }
  STAMP(6);
#define EACH(V, X) _Pragma("unroll") for (int e = 0; e < 4; ++e) { V[e] = X; PIN(V[e]); }
  auto up = [](u32 a, u32 b, int e) { const u32 v = e & 2 ? b : a; return e & 1 ? bfhi(v) : bflo(v); };
  float y[4], z[4];
  EACH(y, up(w[0].x, w[0].z, e))
#pragma unroll
  for (int j = 1; j < 7; ++j) { EACH(y, y[e] + up(w[j].x, w[j].z, e)) }
  EACH(y, y[e] + dppf<0x128>(y[e]))
  auto s16 = [](float v) { auto r = __builtin_amdgcn_permlane16_swap(__float_as_uint(v), __float_as_uint(v), false, false); return __uint_as_float(r[0]) + __uint_as_float(r[1]); };
  auto s32 = [](float v) { auto r = __builtin_amdgcn_permlane32_swap(__float_as_uint(v), __float_as_uint(v), false, false); return __uint_as_float(r[0]) + __uint_as_float(r[1]); };
  EACH(y, s16(y[e])) EACH(y, s32(y[e]))
  EACH(z, up(xr.x, xr.y, e) - __uint_as_float(w[7].x)) EACH(z, z[e] * __uint_as_float(w[7].z)) EACH(y, fmaf(y[e], sg, z[e]))
  EACH(z, y[e] * -1.4426950408889634f) EACH(z, __builtin_amdgcn_exp2f(z[e])) EACH(z, 1.f + z[e]) EACH(z, __builtin_amdgcn_rcpf(z[e])) EACH(y, y[e] * z[e])
  u4 v; v.x = pk2(y[0], y[1]); v.z = pk2(y[2], y[3]); v.y = v.w = tag;
  bst128_sc1(rws, lane < 8 ? WS_GR + f * 128 + lane * 16 : 0x80000000u, v);
  STAMP(2);
}

DEV void epilogue(const Params& p, int lane, int m, int mg, int ng, bool is_main, int f, Epi& ei STM_ARG) {
  const rsrc_t rws = __builtin_amdgcn_make_buffer_rsrc(p.ws.p, 0, WS_END, 0x00020000);
  float (&gf)[20] = ei.gf;
  f2 xf[10], g2[10];
#pragma unroll
  for (int i = is_main ? 0 : YG; i < (is_main ? 0 : YG2); ++i) ei.gate(i);
  SB();
  ei.wait<0>();
  lds_add(Y_WO, is_main ? 8 : 9);
  STAMP(5);
  const u32 tag = __builtin_amdgcn_readfirstlane(ei.tag);
  if (is_main && GW) lds_wait_ge<4>(Y_PUB, GW);
  if (is_main) ei.check(p.ws.p, tag);
  u4 fin = ei.fin;
  if (__builtin_expect(!(fin.y == tag && fin.w == tag), 0)) {
#pragma nounroll
    for (u32 spin = 0; !(fin.y == tag && fin.w == tag) && spin < SPIN_CAP; ++spin) {
      FENCE();
      __builtin_amdgcn_s_sleep(2);
      fin = bld128_sc1(rws, WS_FIN);
    }
  }
#pragma unroll
  for (int i = is_main ? 0 : YG2; i < 20; ++i) ei.gate(i);
  STAMP(10);
  if (!is_main && __builtin_expect(f < 40 && m == 3, 1)) reduce_ffn(p, rws, tag, lane, f, (lane & 4) ? fin.z : fin.x STM);
  const u32 row = Epi::row(lane), odd = ng & 1;
  u32 sadr[2];
  sadr[1] = m * ST_W + (row & 3) * 256 + (row >> 2) * 64;
  sadr[0] = sadr[1] + (odd + ((lane >> 1) & 1)) * ST_N + (lane & 1) * 32;
  sadr[1] += (odd ? 0 : 4 * ST_N) + (lane & 3) * 16;
  PIN(sadr[0]); PIN(sadr[1]);
  SB();
  {
    const u32 tok = ((ei.soff >> 3) & 2046) | ((ei.soff >> 14) & 1), c = key_tok(fin.z) == tok;
    static_assert(WS_ST2 % 32768 == 0, "");
    if (c || key_tok(fin.x) == tok) {
      const u32 a = WS_GR + ng * 640 + c * 64, a0 = a + (odd + ((lane >> 1) & 1)) * 128 + (lane & 1) * 32, a1 = a + (odd ? 0 : 512) + (lane & 3) * 16;
      u4 w[5];
      lds_wait_ge<2>(Y_WO, 256);
#pragma nounroll
      for (u32 spin = 0; spin < SPIN_CAP; ++spin) {
        FENCE();
        u32 b = 0;
#pragma unroll
        for (int j = 0; j < 5; ++j) w[j] = bld128_sc1(rws, j < 4 ? a0 + (j >> 1) * 256 + (j & 1) * 16 : a1);
#pragma unroll
        for (int j = 0; j < 5; ++j) b |= __builtin_amdgcn_bitop3_b32(w[j].y, w[j].w, tag, 0x7e);
        if (!__any(b != 0u)) break;
        __builtin_amdgcn_s_sleep(2);
      }
#pragma unroll
      for (int j = 0; j < 5; ++j) { gf[4 * j] = bflo(w[j].x); gf[4 * j + 1] = bfhi(w[j].x); gf[4 * j + 2] = bflo(w[j].z); gf[4 * j + 3] = bfhi(w[j].z); }
    }
  }
#pragma unroll
  for (int e = 0; e < 10; ++e) { xf[e] = f2{bflo(ei.xw(e)), bfhi(ei.xw(e))}; g2[e] = f2{gf[2 * e], gf[2 * e + 1]}; PIN(xf[e]); PIN(g2[e]); }
  STAMP(7);
  __builtin_amdgcn_s_setprio(LPRIO);
  u4 so = srd(p.out);
  u32 fa = Y_S + m * 4, first;
  SPIN(so); PIN(fa);
  GST(1);
  {
    u32 n = 1u << 20, t, l;
    asm volatile("1:\n\ts_sleep 2\n\tds_read_b32 %1, %4\n\ts_sub_u32 %0, %0, 1\n\ts_cselect_b32 %2, 0, 1\n\ts_waitcnt lgkmcnt(0)\n\t"
                 "v_readfirstlane_b32 %3, %1\n\ts_cmp_lt_u32 %3, %2\n\ts_cbranch_scc1 1b"
                 : "+s"(n), "=&v"(t), "=&s"(l), "=&s"(first) : "v"(fa) : "scc", "memory");
  }
  GST(2);
  f4 pa[5], pb[5];
  auto rd = [&](f4 (&d)[5], u32 a0, u32 a1) {
#pragma unroll
    for (int j = 0; j < 5; ++j) d[j] = ld_f4(j < 4 ? a0 + (j >> 1) * 2 * ST_N + (j & 1) * 16 : a1);
  };
  u32 o0 = (first - 1) * (4 * ST_W), o1 = 4 * ST_W - o0;
  SPIN(o0); SPIN(o1);
  rd(pa, sadr[0] + o0, sadr[1] + o0);
  SB();
  sadr[0] += o1; sadr[1] += o1;
  PIN(sadr[0]); PIN(sadr[1]);
#pragma unroll
  for (int j = 0; j < 5; ++j) PIN(pa[j]);
  GST(3);
  __builtin_amdgcn_s_setprio(3);
  bar();
  GST(4);
  rd(pb, sadr[0], sadr[1]);
  SB();
  auto fin2 = [&](int e) {
    const f4 s4 = pa[e >> 1] + pb[e >> 1];
    const f2 r = __builtin_elementwise_fma(f2{s4[2 * (e & 1)], s4[2 * (e & 1) + 1]}, g2[e], xf[e]);
    return pk2(r.x, r.y);
  };
  u4 o[2]; u2 oh;
#pragma unroll
  for (int ps = 0; ps < 2; ++ps) {
#pragma unroll
    for (int e = 0; e < 4; ++e) o[ps][e] = fin2(4 * ps + e);
    asm volatile("buffer_store_dwordx4 %0, %1, %2, 0 offen offset:%3 " OUTPOL :: "v"(o[ps]), "v"(ei.goff[0]), "s"(so), "i"(ps * 64) : "memory");
    SB();
  }
  oh.x = fin2(8); oh.y = fin2(9);
  asm volatile("buffer_store_dwordx2 %0, %1, %2, 0 offen " OUTPOL :: "v"(oh), "v"(ei.goff[1]), "s"(so), "v"(o[0]), "v"(o[1]) : "memory");
  GST(5);
  STAMP(8);
}

#define NTW 2
#define NTX 2
#define PUT_STAMPS(m) do {} while (0)
#define ROLE extern "C" __global__ __launch_bounds__(768, 3) void
#define PAGE __attribute__((aligned(4096)))
#define STAMP0 ""
#define STAMP_INIT() do {} while (0)

#define ROWS3(W, X0, X1) \
    "buffer_load_dwordx4 " W ", v3, s[8:11], s15 offen\n\tbuffer_load_dwordx4 " X0 ", v4, s[4:7], s15 offen\n\t" \
    "buffer_load_dwordx4 " X1 ", v5, s[4:7], s15 offen\n\ts_addk_i32 s15, 256\n\ts_cmpk_eq_u32 s15, 1280\n\ts_cselect_b32 s15, 0, s15\n\t"
ROLE PAGE fused_kernel(Params p) {
  asm volatile(
    STAMP0
    "s_load_dword s94, s[0:1], 0\n\t"
    "s_load_dword s95, s[0:1], 64\n\t"
    "v_readfirstlane_b32 s3, v0\n\t"
    "s_cmpk_ge_u32 s2, 200\n\t"
    "s_cbranch_scc1 3f\n\t"
    "s_cmpk_lt_u32 s3, 256\n\t"
    "s_cbranch_scc1 role_c4\n\t"
    "s_cmpk_lt_u32 s3, 512\n\t"
    "s_cbranch_scc1 role_c3\n\t"
    "s_setprio 3\n\t"
    "s_load_dwordx8 s[4:11], s[0:1], 0\n\t"
    "s_bfe_u32 s12, s3, 0x20006\n\t"
    "s_lshr_b32 s13, s2, 3\n\t"
    "s_mul_i32 s14, s13, 13\n\t"
    "s_lshr_b32 s14, s14, 6\n\t"
    "s_mul_i32 s15, s14, 5\n\t"
    "s_sub_u32 s13, s13, s15\n\t"
    "s_add_u32 s15, s13, s14\n\t"
    "s_sub_u32 s16, s15, 5\n\t"
    "s_min_u32 s15, s15, s16\n\t"
    "s_bitcmp1_b32 s15, 1\n\t"
    "s_cselect_b64 vcc, -1, 0\n\t"
    "s_and_b32 s16, s15, 1\n\t"
    "s_lshl_b32 s17, s2, 1\n\t"
    "s_and_b32 s17, s17, 12\n\t"
    "s_lshr_b32 s17, " STR(KOFF) ", s17\n\t"
    "s_and_b32 s17, s17, 6\n\t"
    "s_add_u32 s16, s16, s17\n\t"
    "s_bitcmp1_b32 s2, 1\n\t"
    "s_cselect_b32 s17, 5, 0\n\t"
    "s_add_u32 s14, s14, s17\n\t"
    "s_bitcmp1_b32 s2, 0\n\t"
    "s_cselect_b32 s17, 5, 0\n\t"
    "s_add_u32 s13, s13, s17\n\t"
    "s_mul_i32 s18, s14, 13\n\t"
    "s_add_u32 s18, s18, s12\n\t"
    "s_sub_u32 s18, s18, 1\n\t"
    "s_mulk_i32 s18, 20480\n\t"
    "s_add_u32 s19, s18, 81920\n\t"
    "s_add_u32 s20, s18, 163840\n\t"
    "s_add_u32 s21, s18, 245760\n\t"
    "s_min_u32 s21, s21, 2600960\n\t"
    "s_max_i32 s18, s18, 0\n\t"
    "s_cmp_lt_u32 s12, 2\n\t"
    "s_cselect_b64 s[22:23], -1, 0\n\t"
    "s_mov_b64 s[24:25], -1\n\t"
    "s_cmp_eq_u32 s12, 0\n\t"
    "s_cmov_b32 s24, 0xffffff00\n\t"
    "s_lshl_b32 s26, s12, 10\n\t"
    "s_add_u32 s26, s26, 116736\n\t"
    "s_mov_b32 s27, 122880\n\t"
    "s_bfe_u32 s17, s2, 0x10002\n\t"
    "s_mul_i32 s17, s17, 10\n\t"
    "s_add_u32 s17, s17, s16\n\t"
    "s_lshl3_add_u32 s28, s16, s12\n\t"
    "s_mulk_i32 s28, 240\n\t"
    "s_add_u32 s28, s28, s13\n\t"
    "s_lshl_b32 s29, s12, 2\n\t"
    "v_bfe_u32 v3, v0, 3, 3\n\t"
    "v_xor_b32 v4, s29, v0\n\t"
    "v_lshrrev_b32 v5, 1, v3\n\t"
    "v_bitop3_b32 v5, v4, v5, 7 bitop3:0x28\n\t"
    "v_and_b32 v6, 2, v3\n\t"
    "v_bitop3_b32 v6, v4, v6, 7 bitop3:0x28\n\t"
    "v_mad_u32_u24 v1, v3, 20, s17\n\t"
    "v_mad_u32_u24 v2, v3, 30, s28\n\t"
    "v_lshl_or_b32 v1, v1, 3, v5\n\t"
    "v_lshl_or_b32 v2, v2, 3, v6\n\t"
    "v_lshlrev_b32 v1, 4, v1\n\t"
    "v_lshlrev_b32 v2, 4, v2\n\t"
    "s_waitcnt lgkmcnt(0)\n\t"
    "s_cbranch_vccnz 5f\n"
    "4:\n\t"
    HALF_A("s26", "v1", "v2", "s[4:7]", "s[8:11]", "s18", "s21", "s[24:25]", "s[22:23]")
    "s_cbranch_vccnz role_ml\n"
    "5:\n\t"
    HALF_B("s26", "v1", "v2", "s[4:7]", "s[8:11]", "s19", "s20", "s27")
    "s_cbranch_vccnz 4b\n\t"
    "s_branch role_ml\n"
    "3:\n\t"
    "s_cmpk_ge_u32 s3, 512\n\t"
    "s_cbranch_scc1 role_al\n\t"
    "s_load_dwordx4 s[4:7], s[0:1], 0\n\t"
    "s_load_dwordx4 s[8:11], s[0:1], 64\n\t"
    "s_load_dwordx2 s[16:17], s[0:1], 32\n\t"
    "s_getpc_b64 s[12:13]\n"
    "2:\n\t"
    "s_lshr_b32 s3, s3, 6\n\t"
    "s_and_b32 s14, s2, -8\n\t"
    "s_add_u32 s14, s14, s3\n\t"
    "s_addk_i32 s14, -200\n\t"
    "s_mulk_i32 s14, 1280\n\t"
    "v_and_b32 v1, 63, v0\n\t"
    "v_min_u32 v2, 9, v1\n\t"
    "v_lshl_add_u32 v2, v2, 7, s14\n\t"
    "v_min_u32 v2, code_end - 2b, v2\n\t"
    "global_load_dword v167, v2, s[12:13]\n\t"
    "s_mul_i32 s3, s3, 7\n\t"
    "s_lshr_b32 s14, s2, 3\n\t"
    "s_add_u32 s3, s3, s14\n\t"
    "s_sub_u32 s3, s3, 25\n\t"
    "s_and_b32 s14, s2, 3\n\t"
    "s_lshl_b32 s14, s14, 5\n\t"
    "s_add_u32 s14, s14, s3\n\t"
    "s_bfe_u32 s15, s2, 0x10002\n\t"
    "s_lshl4_add_u32 s14, s14, s15\n\t"
    "s_mulk_i32 s14, 1280\n\t"
    "s_lshl_b32 s15, s2, 1\n\t"
    "s_and_b32 s15, s15, 12\n\t"
    "s_lshr_b32 s15, " STR(KOFF) ", s15\n\t"
    "s_and_b32 s15, s15, 6\n\t"
    "s_lshl_b32 s15, s15, 7\n\t"
    "s_addk_i32 s15, 256\n\t"
    "v_and_b32 v2, 3, v0\n\t"
    "v_and_b32 v3, 48, v0\n\t"
    "v_lshlrev_b32 v3, 2, v3\n\t"
    "v_lshl_or_b32 v3, v2, 4, v3\n\t"
    "v_bfe_u32 v2, v0, 2, 2\n\t"
    "v_mul_u32_u24 v2, 2560, v2\n\t"
    "v_add3_u32 v4, v2, v3, s14\n\t"
    "v_add_u32 v5, 10240, v4\n\t"
    "s_cmp_ge_u32 s3, 32\n\t"
    "s_waitcnt lgkmcnt(0)\n\t"
    "s_load_dword s94, s[16:17], 0\n\t"
    "s_cbranch_scc1 role_ac\n\t"
    ROWS3("v[100:103]", "v[104:107]", "v[108:111]") ROWS3("v[112:115]", "v[116:119]", "v[120:123]") ROWS3("v[124:127]", "v[128:131]", "v[132:135]")
    ROWS3("v[136:139]", "v[140:143]", "v[144:147]") ROWS3("v[148:151]", "v[152:155]", "v[156:159]")
    "s_branch role_ac"
    :: "s"(__builtin_amdgcn_kernarg_segment_ptr()), "s"(__builtin_amdgcn_workgroup_id_x()), "v"(__builtin_amdgcn_workitem_id_x())
    : "v167", "s99", "vcc", "scc", "memory");
}

struct Ids {
  int lane, wave, xcd, r, mg, ng, ra, rb, rt, j, a7, f, koff;
  DEV Ids() {
    lane = threadIdx.x & 63; wave = __builtin_amdgcn_readfirstlane(threadIdx.x >> 6);
    const int b = blockIdx.x;
    xcd = b & 7; r = b >> 3;
    mg = xcd * 4 + (r >> 3); ng = r & 7;
    ra = (r * 13) >> 6; rb = r - 5 * ra;
    rt = (xcd >> 1) * 5 + ra; j = (xcd & 1) * 5 + rb;
    a7 = r - 25;
    f = a7 < 6 ? 2 * (xcd + 8 * (a7 >> 1)) + (a7 & 1) : 48 + ((FPERM >> (4 * xcd)) & 7);
    koff = (KOFF >> ((xcd >> 1) * 4)) & 6;
  }
};

template <int NS>
DEV void main_wave(Params& p) {
  STAMP_INIT();
  const Ids id;
  const int lane = id.lane, wave = id.wave;
  const rsrc_t rws = __builtin_amdgcn_make_buffer_rsrc(p.ws.p, 0, WS_END, 0x00020000);
  u32 tag;
  G2 g2;
  main_compute<NS>(p, rws, tag, lane, wave, ((id.xcd >> 1) & 1) * 5 + id.ra, id.xcd >> 2, id.rb, id.j, id.mg, id.ng, g2 STM);
  __builtin_amdgcn_s_setprio(0);
  gemm2(p, rws, tag, wave, id.ng, g2 STM);
  PUT_STAMPS(true);
}
ROLE role_ml(Params p) {
  LdIn in; in.take(); SB();
  STAMP_INIT();
  const Ids id;
  const int lane = id.lane, wave = id.wave;
  Epi ei;
  main_loader(p, in, lane, id.xcd >> 2, id.mg, id.ng, id.koff, ei STM);
  epilogue(p, lane, in.lw, id.mg, id.ng, true, 0, ei STM);
  PUT_STAMPS(true);
}
ROLE PAGE role_c3(Params p) { main_wave<3>(p); }
ROLE role_c4(Params p) { main_wave<4>(p); }
ROLE PAGE role_al(Params p) {
  STAMP_INIT();
  const Ids id;
  const int lane = id.lane, wave = id.wave, lw = wave - 8;
  Epi ei;
  aux_loader(p, lane, lw, id.a7, id.f, id.f < 54, id.mg, id.ng, id.koff, ei STM);
  epilogue(p, lane, lw, id.mg, id.ng, false, id.f, ei STM);
  PUT_STAMPS(false);
}
ROLE PAGE __attribute__((amdgpu_num_vgpr(160))) role_ac(Params p) {
  RowIn rin; rin.take(); SB();
  STAMP_INIT();
  const Ids id;
  const int lane = id.lane, wave = id.wave;
  const rsrc_t rws = __builtin_amdgcn_make_buffer_rsrc(p.ws.p, 0, WS_END, 0x00020000);
  u32 tag;
  aux_compute(p, rws, tag, lane, wave, id.f, id.f < 54, id.xcd, id.a7, rin STM);
  NEW_PAGE();
  G2 g2;
  g2.init(wave, id.mg, id.ng);
  tag = __builtin_amdgcn_readfirstlane(tag);
  SPIN(tag);
  __builtin_amdgcn_s_waitcnt(0x0f70);
  gemm2(p, rws, tag, wave, id.ng, g2 STM);
  PUT_STAMPS(false);
}
ROLE code_end(Params p) {}

extern "C" void kernel_launch(void* const* d_in, const int* in_sizes, int n_in,
                              void* d_out, int out_size, void* d_ws, size_t ws_size,
                              hipStream_t stream) {
  auto buf = [](void* q, u32 bytes) { Buf r; r.p = (char*)q; r.bytes = bytes; r.flags = 0x00020000u; return r; };
  Params p;
  p.x = buf(d_in[0], S_ * D_ * 2); p.wqkv = buf(d_in[1], D_ * 3 * D_ * 2); p.wo = buf(d_in[2], D_ * D_ * 2); p.wr = buf(d_in[4], D_ * 2);
  p.wg = buf(d_in[5], D_ * F_ * 2); p.wu = buf(d_in[6], D_ * F_ * 2); p.wd = buf(d_in[7], F_ * D_ * 2);
  p.out = buf(d_out, S_ * D_ * 2); p.ws = buf(d_ws, WS_END);
  (void)hipFuncSetAttribute((const void*)fused_kernel, hipFuncAttributeMaxDynamicSharedMemorySize, LDS_BYTES);
  fused_kernel<<<256, 768, LDS_BYTES, stream>>>(p);
}
```

```cpp
#include <hip/hip_runtime.h>
#include <stdint.h>

typedef unsigned short u16;
typedef unsigned int u32;
typedef unsigned long long u64;
typedef __attribute__((ext_vector_type(4))) float f4;
typedef __attribute__((ext_vector_type(2))) float f2;
typedef __attribute__((ext_vector_type(4))) unsigned u4;
typedef __attribute__((ext_vector_type(2))) unsigned u2;
typedef __attribute__((ext_vector_type(8))) __bf16 bf8;
typedef __attribute__((ext_vector_type(2))) __bf16 bf2;
typedef __attribute__((ext_vector_type(4))) short s4;
#define LAS __attribute__((address_space(3)))
#define GAS __attribute__((address_space(1)))
#define DEV __device__ __forceinline__

constexpr int S_ = 2048, D_ = 640, F_ = 1728;

constexpr u32 WS_EPOCH = 0;
constexpr u32 WS_FIN = 256;
constexpr u32 WS_KEY = 24576;
constexpr u32 WS_GR = 40960;
constexpr u32 WS_YP = 8u << 20;
constexpr u32 WS_ST2 = 448u << 10;
constexpr u32 WS_FLAG = 512u << 10;
constexpr u32 WS_BOX = 640u << 10;
constexpr u32 WS_O = 1u << 20;
constexpr u32 WS_XS = 4u << 20;
constexpr u32 WS_END = WS_YP + 40 * 56 * 128;
constexpr u32 WS_STAMP = 16u << 20;

constexpr u32 SLOT = 38912;
constexpr u32 XB = 0, WB = 14336;
constexpr u32 MISC = 4 * SLOT;
constexpr u32 L_CS = MISC;
constexpr u32 L_SYNC = MISC + 768;
constexpr u32 L_MR = MISC + 1024;
constexpr u32 L_DUMP = MISC + 2048;
constexpr u32 L_Y = MISC + 3072;
constexpr u32 L_HV = MISC + 6144;
constexpr u32 LDS_BYTES = 163840;
constexpr u32 Y_Y = L_SYNC;
constexpr u32 Y_PUB = L_SYNC + 8;
constexpr u32 Y_WO = L_SYNC + 12;
constexpr u32 Y_CB = L_SYNC + 16;
constexpr u32 Y_ST = L_SYNC + 32;
constexpr u32 Y_S = L_SYNC + 128;
constexpr u32 Y_F = L_SYNC + 64;
constexpr u32 L_NONE = 0x30000;
constexpr u32 L_X = SLOT;
constexpr u32 L_HK = L_X + 21 * 512;
constexpr u32 L_CQ = L_HK + 2048;
constexpr u32 L_WO = L_CQ + 512;
constexpr u32 ST_N = 1040, ST_W = 5 * ST_N;
static_assert(L_WO + 103680 == MISC + 256 && 8 * ST_W <= L_WO, "lds map");

#define KD 44
constexpr int KEY_DELAY = KD;
constexpr int HELLO_DELAY = 4;
constexpr int POLL_SLEEP = 1;
#define WOA_ 14
#define WO7_ 5
constexpr int WO7 = WO7_, WOA = WOA_;
#define GSP 0
constexpr int GSPLIT = GSP;
#define OPRE_ 10
#define WPRE 6
#define RD0 5
#define RDX 6
#define STL 5
#define EBAR 1
#define WPR1 6
#define PGAP 2
#define LPRIO 2
#define IPRIO 2
#define BOXW 2
#define MPRIO 0
#define RDLY 0
#define KSLP 6
#define AWO 1
#define AWT 63
#define AW0 63
#define YG2 12
#define DLATE 0
#define GS2 0
#define NFD 0
#define PBR2 1
#define WEXP 0
#define WDLY 0
#define GWT 0
#define GW0 4
#define PWM 0x21
#define PR3 2
#define PR2 2
#define Q2I 2
#define Q2O 2
#define Q3I 2
#define Q3O 2
#define PFAIR 0
#define PK0 1
#define PK1 3
#define PK2 5
#define WOB 20
#define PPR 3
#define RPM 3
#define RPM4 2
#define KSC 1
#define QSC 0
#define PFT 0
#define PFL 0
#define LDP 0
#define STP 1
#define S8D 3
#define XST 1
#define EFT 1
#define WOF 22
#define YTCH 1
#define PCI 4
#define STR_(x) #x
#define STR(x) STR_(x)
#define LGAP 7
#define PDL 0
#define PC0 6
#define AWW 4
#define LPOLL 1
#define WOP 27
#define WLD 10
#define GW 3
#define C3E 1
#define CSD 2
#define LKP 2
#define WE4 4
#define WE3 2
#define PM0 WE
#define PM1 5
#define PM2 6
#define EPM 0
#define F3L 0
#define OSLP 0
#define ARR 0
constexpr int OPRE = OPRE_;
#define PADC4 "1"
#define NOTOUCH
#define KOFF 0x6420
#define TX0 10
#define INIT1 1
#define TH0 3
#define TH1 3
#define FFN_DELAY 12
#define WOC 11
#define YG 0
#define SSLP 1
#define FPERM 0x54362107
#define OUTP 0
#define OUTPOL ""
#define PBRK 1
#define WOST 0
#define WOSTA 0
#define WTH 6
#define WTH2 4
#define PACE 0
#define PACE0 1
#define MORD 0
#define REVM 0xc
#define KORD 12
constexpr u64 KT0 = 0x9786453120, KT1 = 0x8975642301, KT2 = KT0, KT3 = KT1, KT4 = 0x9867534210;
#define SCAP 15
constexpr u32 SPIN_CAP = 1u << SCAP;

struct Buf { char* p; u32 bytes, flags; };
struct Params { Buf x, wqkv, ws, wo, wr, out, wg, wu, wd; };

DEV float bflo(u32 v) { return __uint_as_float(v << 16); }
DEV float bfhi(u32 v) { return __uint_as_float(v & 0xffff0000u); }
DEV u32 pk2(float a, float b) {
  bf2 r; r[0] = (__bf16)a; r[1] = (__bf16)b;
  return __builtin_bit_cast(u32, r);
}
DEV float rbf(float a) { return bflo(pk2(a, a) & 0xffffu); }
DEV u4 srd(const Buf& b) { return __builtin_bit_cast(u4, b); }
template <int N> DEV void wait_vm() { asm volatile("s_waitcnt vmcnt(%0)" :: "i"(N) : "memory"); }
template <int N> DEV void wait_bar() { asm volatile("s_waitcnt vmcnt(%0) lgkmcnt(0)\n\ts_barrier" :: "i"(N) : "memory"); }
DEV void lds_bar() { asm volatile("s_waitcnt lgkmcnt(0)\n\ts_barrier" ::: "memory"); }

DEV bf8 ld_b128(u32 a) { return *(const LAS bf8*)(uintptr_t)a; }
DEV u2 ld_u2(u32 a) { return *(const LAS u2*)(uintptr_t)a; }
DEV f4 ld_f4(u32 a) { return *(const LAS f4*)(uintptr_t)a; }
DEV float ld_f(u32 a) { return *(const LAS float*)(uintptr_t)a; }
DEV u32 ld_u(u32 a) { return *(const LAS u32*)(uintptr_t)a; }
DEV void st_u4(u32 a, u4 v) { *(LAS u4*)(uintptr_t)a = v; }
DEV void st_u2(u32 a, u2 v) { *(LAS u2*)(uintptr_t)a = v; }
DEV void st_f4(u32 a, f4 v) { *(LAS f4*)(uintptr_t)a = v; }
DEV void st_f(u32 a, float v) { *(LAS float*)(uintptr_t)a = v; }
DEV void st_u(u32 a, u32 v) { *(LAS u32*)(uintptr_t)a = v; }
DEV u2 ld_tr(u32 a) {
  return __builtin_bit_cast(u2, __builtin_amdgcn_ds_read_tr16_b64_v4i16((LAS s4*)(uintptr_t)a));
}
DEV bf8 mk8(u2 a, u2 b) { u4 r; r.x = a.x; r.y = a.y; r.z = b.x; r.w = b.y; return __builtin_bit_cast(bf8, r); }
DEV bf8 ld_tr8(u32 a, u32 d) { return mk8(ld_tr(a), ld_tr(a + d)); }
DEV f4 mfma(bf8 a, bf8 b, f4 c) { return __builtin_amdgcn_mfma_f32_16x16x32_bf16(a, b, c, 0, 0, 0); }
DEV f4 mfma16(u2 a, u2 b, f4 c) { return __builtin_amdgcn_mfma_f32_16x16x16bf16_1k(__builtin_bit_cast(s4, a), __builtin_bit_cast(s4, b), c, 0, 0, 0); }

#define PIN(x) asm volatile("" : "+v"(x))
#define SB() __builtin_amdgcn_sched_barrier(0)
#define RLX __ATOMIC_RELAXED, __HIP_MEMORY_SCOPE_AGENT
DEV u32 gld32(const char* p) { return __hip_atomic_load((const GAS u32*)p, RLX); }
DEV void gst64(char* p, u64 v) { __hip_atomic_store((GAS u64*)p, v, RLX); }
DEV void gst32(char* p, u32 v) { __hip_atomic_store((GAS u32*)p, v, RLX); }
typedef __amdgpu_buffer_rsrc_t rsrc_t;
extern "C" __device__ u4 raw_load(u4 srd, u32 voff, u32 soff, u32 aux) __asm("llvm.amdgcn.raw.buffer.load.v4i32");
extern "C" __device__ void raw_store(u4 v, u4 srd, u32 voff, u32 soff, u32 aux) __asm("llvm.amdgcn.raw.buffer.store.v4i32");
DEV u4 bld128_sc1(rsrc_t r, u32 off) { return __builtin_amdgcn_raw_buffer_load_b128(r, off, 0, 16); }
DEV void bst128_sc1(rsrc_t r, u32 off, u4 v) { __builtin_amdgcn_raw_buffer_store_b128(v, r, off, 0, 16); }

#define FSTAMPS 0
#define STAMP(i) do {} while (0)
#define CSTAMP(i) do {} while (0)
#define FST(i) do {} while (0)
#define GST(i) do {} while (0)
#define KST(i) do {} while (0)
#define PST(i) do {} while (0)
#define EST(i) do {} while (0)
#define STM_ARG
#define STM

template <int CTRL> DEV float dppf(float v) {
  return __uint_as_float(__builtin_amdgcn_update_dpp(0u, __float_as_uint(v), CTRL, 0xf, 0xf, true));
}
template <int CTRL> DEV u32 dppu(u32 v) { return __builtin_amdgcn_update_dpp(0u, v, CTRL, 0xf, 0xf, true); }
DEV float row_sum16(float v) {
  v += dppf<0xB1>(v);
  v += dppf<0x4E>(v);
  v += dppf<0x141>(v);
  v += dppf<0x140>(v);
  return v;
}
DEV void top2_merge(u32& a1, u32& a2, u32 b1, u32 b2) {
  u32 lo = min(a1, b1);
  a1 = max(a1, b1);
  a2 = max(lo, max(a2, b2));
}
DEV void top2_wave(u32& k1, u32& k2) {
  top2_merge(k1, k2, dppu<0xB1>(k1), dppu<0xB1>(k2));
  top2_merge(k1, k2, dppu<0x4E>(k1), dppu<0x4E>(k2));
  top2_merge(k1, k2, dppu<0x124>(k1), dppu<0x124>(k2));
  top2_merge(k1, k2, dppu<0x128>(k1), dppu<0x128>(k2));
  {
    auto r1 = __builtin_amdgcn_permlane16_swap(k1, k1, false, false);
    auto r2 = __builtin_amdgcn_permlane16_swap(k2, k2, false, false);
    k1 = r1[0]; k2 = r2[0]; top2_merge(k1, k2, r1[1], r2[1]);
  }
  {
    auto r1 = __builtin_amdgcn_permlane32_swap(k1, k1, false, false);
    auto r2 = __builtin_amdgcn_permlane32_swap(k2, k2, false, false);
    k1 = r1[0]; k2 = r2[0]; top2_merge(k1, k2, r1[1], r2[1]);
  }
}
DEV u32 mk_tag(u32 ep) {
  u32 tag = ep * 0x9E3779B1u + 0x7F4A7C15u;
  tag ^= tag >> 15; tag *= 0x85EBCA77u; tag ^= tag >> 13;
  return tag + (tag == ep);
}
DEV u32 epoch_get(const Params& p) { u32 e; asm volatile("s_load_dword %0, %1, 0" : "=s"(e) : "s"(p.ws.p)); return e; }
DEV u32 epoch_tag(u32 e) { asm volatile("s_waitcnt lgkmcnt(0)" : "+s"(e)); return mk_tag(e); }

DEV float silu_f(float v) { return v * __builtin_amdgcn_rcpf(1.f + __expf(-v)); }

DEV u32 key_tok(u32 k) { return ~k & 2047u; }
DEV float key_r(u32 k) {
  u32 o = k >> 16;
  u32 b = (o & 0x8000u) ? (o & 0x7fffu) : (~o & 0xffffu);
  return bflo(b);
}
DEV float vmax(float a, float b) { return __builtin_elementwise_maximum(a, b); }
DEV void bar() { asm volatile("s_barrier" ::: "memory"); }
#define NEW_PAGE() asm volatile("s_branch 1f\n\t.p2align 12\n1:" ::: "memory")
#define FENCE() asm volatile("" ::: "memory")
DEV u32 lds_peek(u32 a) { return __hip_atomic_load((LAS u32*)(uintptr_t)a, __ATOMIC_RELAXED, __HIP_MEMORY_SCOPE_WORKGROUP); }
DEV void lds_add(u32 a, u32 v) {
  asm volatile("s_mov_b64 exec, 1\n\tds_add_u32 %0, %1\n\ts_mov_b64 exec, -1" :: "v"(a), "v"(v) : "memory");
}
DEV void lds_set(u32 a, u32 v) {
  asm volatile("s_mov_b64 exec, 1\n\tds_write_b32 %0, %1\n\ts_mov_b64 exec, -1" :: "v"(a), "v"(v) : "memory");
}
template <int SLP> DEV void lds_wait_ge(u32 a, u32 v) {
  u32 n = 1u << 20, t, l, r;
  asm volatile(".if %6\n\ts_branch 2f\n\t.endif\n1:\n\t.if %6\n\ts_sleep %6\n\t.endif\n2:\n\t"
               "ds_read_b32 %1, %4\n\ts_sub_u32 %0, %0, 1\n\ts_cselect_b32 %2, 0, %5\n\ts_waitcnt lgkmcnt(0)\n\t"
               "v_readfirstlane_b32 %3, %1\n\ts_cmp_lt_u32 %3, %2\n\ts_cbranch_scc1 1b"
               : "+s"(n), "=&v"(t), "=&s"(l), "=&s"(r) : "v"(a), "s"(v), "i"(SLP) : "scc", "memory");
}
template <int SLP> DEV void lds_wait_bit(u32 a, u32 v) {
  u32 n = 1u << 20, t, l, r;
  asm volatile("s_branch 2f\n1:\n\ts_sleep %6\n2:\n\tds_read_b32 %1, %4\n\ts_sub_u32 %0, %0, 1\n\ts_cselect_b32 %2, -1, 0\n\ts_waitcnt lgkmcnt(0)\n\t"
               "v_readfirstlane_b32 %3, %1\n\ts_or_b32 %3, %3, %2\n\ts_and_b32 %3, %3, %5\n\ts_cbranch_scc0 1b"
               : "+s"(n), "=&v"(t), "=&s"(l), "=&s"(r) : "v"(a), "s"(v), "i"(SLP) : "scc", "memory");
}
DEV u32 norm2(u32 x, float m, float rs) {
  const u32 d = pk2(bflo(x) - m, bfhi(x) - m);
  return pk2(bflo(d) * rs, bfhi(d) * rs);
}

DEV float dot2(u32 a, u32 b, float c) { return __builtin_amdgcn_fdot2_f32_bf16(__builtin_bit_cast(bf2, a), __builtin_bit_cast(bf2, b), c, false); }
DEV float fsel(u32 m, float a, float b) {
  return __uint_as_float(__builtin_amdgcn_bitop3_b32(m, __float_as_uint(a), __float_as_uint(b), 0xCA));
}
struct RowIn {
  u4 x[2][5], w[5];
  DEV void take() {
    asm volatile("" : "={v[100:103]}"(w[0]), "={v[104:107]}"(x[0][0]), "={v[108:111]}"(x[1][0]), "={v[112:115]}"(w[1]), "={v[116:119]}"(x[0][1]), "={v[120:123]}"(x[1][1]),
                 "={v[124:127]}"(w[2]), "={v[128:131]}"(x[0][2]), "={v[132:135]}"(x[1][2]), "={v[136:139]}"(w[3]), "={v[140:143]}"(x[0][3]), "={v[144:147]}"(x[1][3]),
                 "={v[148:151]}"(w[4]), "={v[152:155]}"(x[0][4]), "={v[156:159]}"(x[1][4]));
  }
  DEV void wait(int i) {
    if (i == 0) asm volatile("s_waitcnt vmcnt(12)" : "+{v[100:103]}"(w[0]), "+{v[104:107]}"(x[0][0]), "+{v[108:111]}"(x[1][0]));
    if (i == 1) asm volatile("s_waitcnt vmcnt(9)" : "+{v[112:115]}"(w[1]), "+{v[116:119]}"(x[0][1]), "+{v[120:123]}"(x[1][1]));
    if (i == 2) asm volatile("s_waitcnt vmcnt(6)" : "+{v[124:127]}"(w[2]), "+{v[128:131]}"(x[0][2]), "+{v[132:135]}"(x[1][2]));
    if (i == 3) asm volatile("s_waitcnt vmcnt(3)" : "+{v[136:139]}"(w[3]), "+{v[140:143]}"(x[0][3]), "+{v[144:147]}"(x[1][3]));
    if (i == 4) asm volatile("s_waitcnt vmcnt(0)" : "+{v[148:151]}"(w[4]), "+{v[152:155]}"(x[0][4]), "+{v[156:159]}"(x[1][4]));
  }
  DEV void stats(const Params& p, rsrc_t rws, u32& tag, int lane, int s8, u32 slot STM_ARG) {
    const u32 ep = epoch_get(p);
    const u32 dq = ((lane >> 2) ^ (lane >> 4)) & 3;
    const int sidx = s8 + (lane >> 4) + dq * 2, tok = 2 * (sidx & 1023) + (sidx >> 10);
    u32 so = ((dq & 1) | (lane & 3)) ? 0x80000000u : WS_ST2 + sidx * 16, ko = lane ? L_NONE + lane * 16 : slot;
    u32 m1 = __builtin_amdgcn_sbfe(lane, 0, 1), m2 = __builtin_amdgcn_sbfe(lane, 1, 1), mz = __builtin_amdgcn_sbfe(dq, 1, 1), mv = ~__builtin_amdgcn_sbfe(dq, 0, 1);
    u4 on; on.x = on.y = on.z = on.w = 0x3f803f80u;
    PIN(so); PIN(ko); PIN(m1); PIN(m2); PIN(mz); PIN(mv); PIN(on);
    const bf8 ones = __builtin_bit_cast(bf8, on);
    const f4 z4 = {0.f, 0.f, 0.f, 0.f};
    f4 ax[2] = {z4, z4}, aq[2] = {z4, z4}, aw[2] = {z4, z4}, a1 = z4;
    SB();
#pragma unroll
    for (int i = 0; i < 5; ++i) {
      wait(i);
      const bf8 wv = __builtin_bit_cast(bf8, w[i]);
#pragma unroll
      for (int z = 0; z < 2; ++z) {
        const bf8 v = __builtin_bit_cast(bf8, x[z][i]);
        aq[z] = mfma(v, v, aq[z]); ax[z] = mfma(v, ones, ax[z]); aw[z] = mfma(v, wv, aw[z]);
      }
      a1 = mfma(wv, ones, a1);
      SB();
      PST(6 + i);
    }
    tag = epoch_tag(ep);
    PST(11);
    const float sw = (a1[0] + a1[1]) + (a1[2] + a1[3]);
    auto quad = [](float v) { v += dppf<0xB1>(v); v += dppf<0x4E>(v); return v; };
    auto diag = [&](f4 d) { return fsel(m2, fsel(m1, d[3], d[2]), fsel(m1, d[1], d[0])); };
    auto both = [&](float a, float b) { return fsel(mz, dppf<0x128>(b), a); };
    const float mean = fsel(mz, (ax[1][0] + ax[1][1]) + (ax[1][2] + ax[1][3]), (ax[0][0] + ax[0][1]) + (ax[0][2] + ax[0][3])) * (1.f / 640.f);
    const float sxw = quad(both(diag(aw[0]), diag(aw[1])));
    float var = fmaf(-mean, mean, quad(both(diag(aq[0]), diag(aq[1]))) * (1.f / 640.f));
    if (__builtin_expect(__any(mv && !(var * 64.f >= mean * mean)), 0)) {
      float q[2];
#pragma unroll
      for (int z = 0; z < 2; ++z) {
        f4 a = z4;
#pragma unroll
        for (int i = 0; i < 5; ++i) a = mfma(ones, __builtin_bit_cast(bf8, x[z][i]), a);
        const float mu = quad(a[0]) * (1.f / 640.f);
        float t = 0.f;
#pragma unroll
        for (int i = 0; i < 5; ++i)
#pragma unroll
          for (int e = 0; e < 4; ++e) { const float c = bflo(x[z][i][e]) - mu, d = bfhi(x[z][i][e]) - mu; t = fmaf(c, c, fmaf(d, d, t)); }
        t = quad(t);
        t += __shfl_xor(t, 16); t += __shfl_xor(t, 32);
        q[z] = t;
      }
      var = both(q[0], q[1]) * (1.f / 640.f);
    }
    const float mb = rbf(mean);
    const float sd = rbf(__builtin_amdgcn_sqrtf(rbf(rbf(var) + 1e-5f)));
    const float rs = __builtin_amdgcn_rcpf(sd);
    u4 st; st.x = __float_as_uint(mb); st.z = __float_as_uint(rs); st.y = st.w = tag;
    PST(12);
    bst128_sc1(rws, so, st);
    PST(13);
    const float rr = fmaf(-mb, sw, sxw) * rs;
    const u32 b = pk2(rr, rr) & 0xffffu;
    const u32 o = (b & 0x8000u) ? (~b & 0xffffu) : (b | 0x8000u);
    u32 k1 = ((o << 16) | (0xffffu - tok)) & mv, k2 = 0;
    top2_merge(k1, k2, dppu<0x128>(k1), 0);
    k1 = max(k1, dppu<0x124>(k1)); k2 = max(k2, dppu<0x124>(k2));
    {
      auto r1 = __builtin_amdgcn_permlane16_swap(k1, k1, false, false);
      auto r2 = __builtin_amdgcn_permlane16_swap(k2, k2, false, false);
      k1 = r1[0]; k2 = r2[0]; top2_merge(k1, k2, r1[1], r2[1]);
    }
    {
      auto r1 = __builtin_amdgcn_permlane32_swap(k1, k1, false, false);
      auto r2 = __builtin_amdgcn_permlane32_swap(k2, k2, false, false);
      k1 = r1[0]; k2 = r2[0]; top2_merge(k1, k2, r1[1], r2[1]);
    }
    u4 kk; kk.x = k1; kk.z = k2; kk.y = kk.w = tag;
    PST(14);
    st_u4(ko, kk);
  }
};

template <int N, int DM, int DS, int E60 = 0, int TH = 63> DEV void dma_run(u32 m0v, u32 voff, u4 srd, u32 so) {
  asm volatile("s_mov_b32 m0, %2\n\t"
               "s_mov_b32 exec_hi, %7\n\t"
               ".rept %4\n\t"
               ".if %7 == -1 || " STR(WEXP) " != 5\n\tbuffer_load_dwordx4 %1, %3, %0 offen lds\n\t.else\n\tbuffer_load_dwordx4 v[100:103], %1, %3, %0 offen\n\t.endif\n\t"
               "s_add_u32 m0, m0, %5\n\t"
               "s_add_u32 %0, %0, %6\n\t"
               ".if %8 < 63\n\ts_waitcnt vmcnt(%8)\n\t.endif\n\t"
               ".endr\n\t"
               "s_mov_b32 exec_hi, -1"
               : "+s"(so) : "v"(voff), "s"(m0v), "s"(srd), "i"(N), "i"(DM), "i"(DS), "i"(E60 ? 0x0fffffff : -1), "i"(TH) : "scc");
}

struct WoDma {
  u32 vo, vol, m, so, m0;
  DEV void init(int lane, int lw, int ng, int start, int last) {
    const int l60 = min(lane, 59);
    vo = (lw * 6 + l60 / 10) * 1280 + (l60 % 10) * 16 + ng * 160;
    if (WEXP == 1) vo = ng * 960 + lw * 7680 + l60 * 16;
    if (WEXP == 2) vo = 0x80000000u;
    vol = min(last * 6 + l60 / 10, 639) * 1280 + (l60 % 10) * 16 + ng * 160;
    m0 = L_WO + lw * 960; m = m0 + start * 3840; so = start * (24 * 1280);
    PIN(vo); PIN(vol);
    asm volatile("" : "+s"(m), "+s"(so), "+s"(m0));
  }
  template <int I0, int I1, int TH = 63, int SKIP = 0, int NL = 4> DEV void issue(u4 srd, int lw, u32 f = 0) {
    constexpr int E = I1 > 26 ? 26 : I1;
    if (E > I0)
      asm volatile(".if %7\n\ts_cmp_gt_u32 %8, 53\n\ts_cbranch_scc1 1f\n\t.endif\n\t"
                   "s_mov_b32 m0, %0\n\ts_mov_b32 exec_hi, 0x0fffffff\n\t"
                   ".rept %5\n\tbuffer_load_dwordx4 %2, %3, %1 offen lds\n\ts_addk_i32 m0, 960 * %9\n\ts_addk_i32 %1, 7680 * %9\n\t"
                   ".if %6 < 63\n\ts_waitcnt vmcnt(%6)\n\t.endif\n\t.endr\n\t"
                   "s_mov_b32 %0, m0\n\ts_mov_b32 exec_hi, -1\n1:"
                   : "+s"(m), "+s"(so) : "v"(vo), "s"(srd), "s"(m0), "i"(E > I0 ? (E - I0) * 4 / NL : 1), "i"(TH), "i"(SKIP), "s"(f), "i"(NL) : "scc");
    if (I1 > 26) dma_run<1, 0, 0, 1>(L_WO + lw * 960 + 26 * 3840, vol, srd, 0);
  }
};

struct Epi {
  u4 xv[2], sv, fin; u2 xh; u32 goff[2], soff, tag;
  float gf[20], c[4];
  static DEV u32 row(u32 lane) { return ((lane >> 1) & 12) | ((lane >> 2) & 1) | ((lane >> 4) & 2); }
  DEV void init(int lane, int m, int mg, int ng) {
    const int mt = mg * 4 + m, parity = mt >> 6;
    const u32 u = (mt & 63) * 16 + row(lane), o = (2 * u + parity) * 1280 + ng * 160;
    goff[0] = o + (ng & 1) * 32 + (lane & 3) * 16; goff[1] = o + ((ng & 1) ? 0 : 128) + (lane & 3) * 8;
    soff = WS_ST2 + (parity * 1024 + u) * 16;
    PIN(goff[0]); PIN(goff[1]); PIN(soff);
  }
  DEV void issue(u4 srdx, u4 srdws, u32 fo = WS_FIN) {
    asm volatile("buffer_load_dwordx4 %0, %1, %2, 0 offen sc1" : "=v"(sv) : "v"(soff), "s"(srdws));
    asm volatile("buffer_load_dwordx4 %0, %1, %2, 0 offen" : "=v"(xv[0]) : "v"(goff[0]), "s"(srdx));
    asm volatile("buffer_load_dwordx4 %0, %1, %2, 0 offen offset:64" : "=v"(xv[1]) : "v"(goff[0]), "s"(srdx));
    asm volatile("buffer_load_dwordx2 %0, %1, %2, 0 offen" : "=v"(xh) : "v"(goff[1]), "s"(srdx));
    asm volatile("buffer_load_dwordx4 %0, off, %1, %2 sc1" : "=v"(fin) : "s"(srdws), "s"(fo));
  }
  template <int N> DEV void wait() {
    asm volatile("s_waitcnt vmcnt(%5)" : "+v"(xv[0]), "+v"(xv[1]), "+v"(xh), "+v"(sv), "+v"(fin) : "i"(N) : "memory");
  }
  DEV void check(char* ws, u32 tag) {
    if (__builtin_expect(__any(__builtin_amdgcn_bitop3_b32(sv.y, sv.w, tag, 0x7e) != 0u), 0)) {
      const rsrc_t rws = __builtin_amdgcn_make_buffer_rsrc(ws, 0, WS_END, 0x00020000);
#pragma nounroll
      for (u32 spin = 0; !__all(sv.y == tag && sv.w == tag) && spin < SPIN_CAP; ++spin) {
        FENCE();
        sv = bld128_sc1(rws, soff);
      }
      __builtin_amdgcn_s_waitcnt(0x0f70);
    }
  }
  DEV u32 xw(int i) const { return i < 8 ? xv[i >> 2][i & 3] : xh[i & 1]; }
  DEV void gate(int i) {
    if (i == 0) {
      c[0] = __uint_as_float(sv.z); c[1] = -__uint_as_float(sv.x) * c[0];
      c[2] = c[0] * -1.4426950408889634f; c[3] = c[1] * -1.4426950408889634f;
    }
    float x = i & 1 ? bfhi(xw(i >> 1)) : bflo(xw(i >> 1));
    PIN(x);
    float z = fmaf(x, c[0], c[1]), t = fmaf(x, c[2], c[3]);
    PIN(z); PIN(t);
    gf[i] = z * __builtin_amdgcn_rcpf(1.f + __builtin_amdgcn_exp2f(t));
    PIN(gf[i]);
  }
};
#define SPIN(x) asm volatile("" : "+s"(x))
#define HALF_A(SB, VX, VW, SX, SW, SA, SC, EA, EC) \
    "s_add_u32 m0, " SB ", 12288\n\ts_mov_b64 exec, " EC "\n\ts_cbranch_execz 7f\n\tbuffer_load_dwordx4 " VX ", " SX ", " SC " offen lds\n7:\n\t" \
    "s_mov_b32 m0, " SB "\n\ts_mov_b64 exec, " EA "\n\tbuffer_load_dwordx4 " VX ", " SX ", " SA " offen lds\n\t" \
    "s_addk_i32 m0, 14336\n\ts_mov_b64 exec, -1\n\tbuffer_load_dwordx4 " VW ", " SW ", 0 offen lds\n\t" \
    "s_addk_i32 m0, 6912\n\ts_nop 0\n\tbuffer_load_dwordx4 " VW ", " SW ", 0 offen offset:1280 lds\n\t" \
    "s_addk_i32 m0, 6912\n\ts_nop 0\n\tbuffer_load_dwordx4 " VW ", " SW ", 0 offen offset:2560 lds\n\t"
#define HALF_B(SB, VX, VW, SX, SW, SB1, SB2, SH) \
    "s_add_u32 m0, " SB ", 4096\n\ts_nop 0\n\tbuffer_load_dwordx4 " VX ", " SX ", " SB1 " offen lds\n\t" \
    "s_addk_i32 m0, 4096\n\ts_nop 0\n\tbuffer_load_dwordx4 " VX ", " SX ", " SB2 " offen lds\n\t" \
    "s_addk_i32 m0, 10240\n\ts_nop 0\n\tbuffer_load_dwordx4 " VW ", " SW ", " SH " offen lds\n\t" \
    "s_addk_i32 m0, 6912\n\ts_nop 0\n\tbuffer_load_dwordx4 " VW ", " SW ", " SH " offen offset:1280 lds\n\t" \
    "s_addk_i32 m0, 6912\n\ts_nop 0\n\tbuffer_load_dwordx4 " VW ", " SW ", " SH " offen offset:2560 lds\n\t"
struct LdIn {
  u4 srdx, srdw; u32 vx, vw, lw, j, tp, cls, kb, sa, sb1, sb2, sc, sb, sh; u64 ec, ea;
  DEV void take() {
    asm volatile("" : "={s[4:7]}"(srdx), "={s[8:11]}"(srdw), "={v1}"(vx), "={v2}"(vw), "={s12}"(lw), "={s13}"(j), "={s14}"(tp), "={s15}"(cls), "={s16}"(kb),
                 "={s18}"(sa), "={s19}"(sb1), "={s20}"(sb2), "={s21}"(sc), "={s[22:23]}"(ec), "={s[24:25]}"(ea), "={s26}"(sb), "={s27}"(sh));
  }
};
DEV void main_loader(Params& p, LdIn& in, int lane, int parity, int mg, int ng, u32 koff, Epi& ei STM_ARG) {
  const int lw = in.lw, tp = in.tp, j = in.j, rt = parity * 10 + tp, u0 = tp * 104 - 8;
  const u4 srdx = in.srdx;
  u64 ktab = KT0;
  ktab = in.cls == 1 ? KT1 : ktab; SPIN(ktab); ktab = in.cls == 2 ? KT2 : ktab; SPIN(ktab); ktab = in.cls == 3 ? KT3 : ktab; SPIN(ktab); ktab = in.cls == 4 ? KT4 : ktab;
  auto next = [&]() {
    ktab >>= 4;
    u32 kb = ((u32)ktab & 15u) + koff;
    kb = min(kb, kb - 10u);
    const int d = kb - in.kb;
    in.kb = kb;
    in.vx += d * 128; in.vw += d * (64 * 3840);
    in.sb = in.sb >= 3 * SLOT ? in.sb - 3 * SLOT : in.sb + SLOT;
    PIN(in.vx); PIN(in.vw); SPIN(in.sb);
  };
  auto issue = [&]() {
    asm volatile("s_bitcmp1_b32 %12, 1\n\ts_cselect_b64 vcc, -1, 0\n\ts_cbranch_vccnz 5f\n4:\n\t"
                 HALF_A("%0", "%1", "%2", "%3", "%4", "%5", "%6", "%7", "%8") "s_cbranch_vccnz 6f\n5:\n\t"
                 HALF_B("%0", "%1", "%2", "%3", "%4", "%9", "%10", "%11") "s_cbranch_vccnz 4b\n6:"
                 :: "s"(in.sb), "v"(in.vx), "v"(in.vw), "s"(in.srdx), "s"(in.srdw), "s"(in.sa), "s"(in.sc), "s"(in.ea), "s"(in.ec),
                    "s"(in.sb1), "s"(in.sb2), "s"(in.sh), "s"(in.cls) : "scc", "vcc");
    next();
  };
#define STAGE_BAR(E) asm volatile("s_cmp_lt_u32 %0, 2\n\ts_cbranch_scc1 1f\n\ts_waitcnt vmcnt(%1)\n1:\n\ts_waitcnt vmcnt(%2)\n\ts_barrier" :: "s"(lw), "i"(9 + (E)), "i"(10 + (E)) : "scc", "memory")
  next();
  u32 ep = epoch_get(p);
  const u4 srdws = srd(p.ws);
  const int srow = lw * 28 + min(lane, 27);
  u32 go1 = WS_ST2 + (parity * 1024 + min(max(u0 + srow, 0), 1023)) * 16;
  u32 wa = lw < 2 ? (XST ? 0u : 0x80000000u) + WS_XS + ((rt * 10 + j) * 14 + (lw ? 14 : -7)) * 512 + min(lane, 27) * 128 : (BOXW == 0 || (BOXW == 2 && lane) ? 0x80000000u : WS_BOX + (mg * 8 + ng) * 256 + (lw & 1) * 64);
  u32 ho = (lane | (lw ^ 3)) ? 0x80000000u : WS_FLAG + (rt * 10 + j) * 256 + 128;
  PIN(go1); PIN(wa); PIN(ho);
  WoDma wo; wo.init(lane, lw, ng, 0, lw == 2 ? 52 : 106);
  ei.init(lane, lw, mg, ng);
  SB();
  PST(4);
  issue();
  u32 tagv = epoch_tag(ep);
  PIN(tagv);
  STAMP(9);
  PST(5);
  STAGE_BAR(0);
  PST(7);
  STAMP(2);
  asm volatile("buffer_store_dword %0, %1, %2, 0 offen sc1" :: "v"(tagv), "v"(ho), "s"(srdws));
#pragma unroll 1
  for (int s = 2; s < 9; ++s) {
    issue();
    STAGE_BAR(0);
  }
  EST(2);
  u4 g1;
  auto stats = [&]() {
    asm volatile("buffer_load_dwordx4 %0, %1, %2, 0 offen sc1" : "=v"(g1) : "v"(go1), "s"(srdws));
    asm volatile("s_mov_b32 m0, %0\n\ts_mov_b64 exec, 0x0fffffff\n\tbuffer_load_dword %1, %2, 0 offen sc1 lds\n\t"
                 "s_add_u32 m0, m0, 504\n\ts_nop 0\n\tbuffer_load_dword %1, %2, 0 offen offset:8 sc1 lds\n\ts_mov_b64 exec, -1"
                 :: "s"(L_MR + lw * 112), "v"(go1), "s"(srdws) : "scc");
  };
  if (STP >= 2) {
    if (STP == 2) stats();
    asm volatile("s_bitcmp1_b32 %12, 1\n\ts_cselect_b64 vcc, -1, 0\n\ts_cbranch_vccnz 5f\n4:\n\t"
                 HALF_A("%0", "%1", "%2", "%3", "%4", "%5", "%6", "%7", "%8") "s_cbranch_vccnz 6f\n\t"
                 "s_cmp_lt_u32 %13, 2\n\ts_cbranch_scc1 1f\n\ts_waitcnt vmcnt(%14 + 4)\n1:\n\ts_waitcnt vmcnt(%14 + 5)\n\ts_barrier\n5:\n\t"
                 HALF_B("%0", "%1", "%2", "%3", "%4", "%9", "%10", "%11") "s_cbranch_vccz 6f\n\ts_waitcnt vmcnt(%14 + 5)\n\ts_barrier\n\ts_branch 4b\n6:"
                 :: "s"(in.sb), "v"(in.vx), "v"(in.vw), "s"(in.srdx), "s"(in.srdw), "s"(in.sa), "s"(in.sc), "s"(in.ea), "s"(in.ec),
                    "s"(in.sb1), "s"(in.sb2), "s"(in.sh), "s"(in.cls), "s"(lw), "i"(STP == 2 ? 3 : 0) : "scc", "vcc", "memory");
    EST(4);
    if (STP == 3) stats();
  } else {
  issue();
  if (STP == 0) stats();
  EST(3);
  STAGE_BAR(STP == 0 ? 3 : 0);
  EST(4);
  STAMP(1);
  if (STP == 1) stats();
  }
  const u4 srdo = srd(p.wo);
  asm volatile("s_mov_b32 m0, %0\n\ts_cmp_lt_u32 %3, 2\n\ts_cbranch_scc1 1f\n\tbuffer_atomic_add %4, %1, %2, 0 offen\n\ts_branch 2f\n"
               "1:\n\tbuffer_load_dword %1, %2, 0 offen sc1 lds\n2:" :: "s"(L_DUMP + lw * 256), "v"(wa), "s"(srdws), "s"(lw), "v"(0u) : "scc");
  wo.issue<0, WO7>(srdo, lw);
  EST(5);
  asm volatile("s_waitcnt vmcnt(%1)" : "+v"(g1) : "i"(WO7 + 3 + (STP == 2 ? 9 : 0)) : "memory");
  EST(7);
  if (__builtin_expect(__any(__builtin_amdgcn_bitop3_b32(g1.y, g1.w, tagv, 0x7e) != 0u), 0)) {
    const rsrc_t rws = __builtin_amdgcn_make_buffer_rsrc(p.ws.p, 0, WS_END, 0x00020000);
#pragma nounroll
    for (u32 spin = 0; spin < SPIN_CAP; ++spin) {
      FENCE();
      g1 = bld128_sc1(rws, go1);
      if (__all(g1.y == tagv && g1.w == tagv)) break;
      __builtin_amdgcn_s_sleep(2);
    }
    __builtin_amdgcn_s_waitcnt(0x0f70);
    st_u(L_MR + srow * 4, g1.x); st_u(L_MR + 512 + srow * 4, g1.z);
  }
  __builtin_amdgcn_s_setprio(LDP);
  if (FSTAMPS + 0 == 6) { wait_vm<WO7 + 1>(); EST(8); }
  wait_bar<WO7 + 1>();
  EST(6);
  STAMP(3);
  if (WDLY) __builtin_amdgcn_s_sleep(WDLY);
  static_assert((32 - 4 * WO7) % 2 == 0, "");
  ei.tag = tagv;
  if (lw < 2) {
    dma_run<16 - 2 * WO7, 1920, 12 * 1280, 1, WTH>(L_WO + (4 * WO7 + lw) * 960, wo.vo, srdo, 4 * WO7 * 7680);
    dma_run<17, 1920, 12 * 1280, 1, WTH>(L_WO + (53 + lw) * 960, wo.vo, srdo, 53 * 7680);
  }
  ei.issue(srdx, srdws);
  ei.wait<5>();
  lds_add(Y_WO, 1);
  STAMP(6);
  ei.wait<0>();
  if (lw >= 2) {
    __builtin_amdgcn_s_sleep(WLD);
    dma_run<20, 960, 7680, 1, WTH2>(L_WO + (lw == 2 ? 32 : 86) * 960, wo.vo, srdo, (lw == 2 ? 30 : 83) * 7680);
    dma_run<1, 0, 0, 1>(L_WO + (lw == 2 ? 52 : 106) * 960, wo.vol, srdo, 0);
  }
  STAMP(4);
}

DEV int fresh_lane() {
  int l;
  asm volatile("v_mbcnt_lo_u32_b32 %0, -1, 0\n\tv_mbcnt_hi_u32_b32 %0, -1, %0" : "=v"(l));
  __builtin_assume(l >= 0 && l < 64);
  return l;
}
struct G2 {
  u32 sb, so, sf, sg, pm, mb, me, mo;
  u32 b0, oo, fo, gb, one, pa, po, tv;
  DEV void pre(int wave, int mg, int ng) {
    const int m = wave & 3, kh = wave >> 2;
    sb = L_WO + kh * 320 * 160; so = WS_O + (mg * 4 + m) * 20480 + kh * 10240; sf = WS_BOX + (mg * 8 + ng) * 256 + kh * 64; sg = 256u << kh;
    pm = ((0x4b5au >> (mg & 15)) & 1) * 0x3e0u + 0x1fu;
    SPIN(pm);
    pm *= (PWM >> wave) & 1;
    SPIN(sb); SPIN(so); SPIN(sf); SPIN(sg); SPIN(pm);
  }
  DEV void pub(u32 tp, u32 parity, u32 j, u32 rb) {
    const u32 r0 = tp * 104;
    mb = WS_BOX + (parity * 16 + (r0 >> 6)) * 2048 + (j >= 5 ? 64 : 0) + rb * 4; mo = (r0 & 63) ? 20 : 0;
    me = ((0x5au >> tp) & 1) ? 0xffffffu : 0xffffu;
    SPIN(mb); SPIN(mo); SPIN(me);
  }
  template <bool MAIN> DEV void init(u32 tag) {
    const u32 lane = fresh_lane();
    b0 = sb + (((lane >> 1) & 16) | ((lane >> 2) & 7)) * 160 + (lane & 3) * 8;
    pa = Y_PUB;
    PIN(b0); PIN(pa);
    SB();
    oo = so + lane * 16; fo = sf + lane * 4; gb = sg;
    PIN(oo); PIN(fo); PIN(gb);
    if (MAIN) { po = mb + lane * 256 + (mo & (((int)lane - 8) >> 31)); tv = tag; one = 1; PIN(po); PIN(tv); PIN(one); }
  }
};

constexpr u64 sel_mask(int r) {
  u64 m = 0;
  for (int l = 0; l < 64; ++l) {
    const int i = l & 15, c = 4 * ((l >> 4) & 1) + r;
    if (i < 8 ? c <= i : c > i - 8) m |= 1ull << l;
  }
  return m;
}
constexpr u64 SEL0 = sel_mask(0), SEL1 = sel_mask(1), SEL2 = sel_mask(2), SEL3 = sel_mask(3);
#define LM(m) __builtin_amdgcn_inverse_ballot_w64(m)
DEV u2 and2(u2 a, u2 m) { u2 r; r.x = a.x & m.x; r.y = a.y & m.y; return r; }

struct Att {
  u32 aA0[2], aA1[2], aB[2];
  u32 mOwn, mRot, mV, mLo, so[2], xg, aXw, aXr, aCS, aCV, aST, aMR, aH, aF;
  u4 mS;
  DEV void init_k(u32 ln, u32 n) {
    const u32 a0 = XB + ((ln & 15) << 7) + ((((ln >> 1) & 7) ^ (ln >> 4)) << 4);
    const u32 b0 = WB + ((ln & 48) << 6) + ((ln & 12) << 5) + (((2 * n + ((ln >> 1) & 1)) ^ ((ln >> 2) & 6)) << 4) + ((ln & 1) << 3);
#pragma unroll
    for (int s = 0; s < 2; ++s) {
      aA0[s] = a0 + s * 2 * SLOT; aA1[s] = (a0 ^ 64) + s * 2 * SLOT; aB[s] = b0 + s * 2 * SLOT;
      PIN(aA0[s]); PIN(aA1[s]); PIN(aB[s]);
    }
  }
  template <int NS> DEV void init(u32 ln, int n, int tau, bool edge, int tp, int parity, int j) {
    constexpr int s0 = NS == 4 ? 0 : 4;
    constexpr u32 SGN = 0x80000000u;
    const u32 i16 = ln & 15, g16 = ln & 48, h32 = ln >> 5, i4 = i16 * 4;
    mLo = ~(u32)__builtin_amdgcn_sbfe((int)ln, 3, 1);
    const u32 gt = (u32)(((int)g16 - 16 * tau) >> 31);
    mOwn = mLo & gt; mRot = ~mLo & ~gt;
    mV = (u32)(((int)i16 - 4 * tau) >> 31);
    u32 l0 = (u32)SEL0, l1 = (u32)SEL1, l2 = (u32)SEL2, l3 = (u32)SEL3;
    SPIN(l0); SPIN(l1); SPIN(l2); SPIN(l3);
    mS.x = __builtin_amdgcn_sbfe(l0, ln, 1); mS.y = __builtin_amdgcn_sbfe(l1, ln, 1); mS.z = __builtin_amdgcn_sbfe(l2, ln, 1); mS.w = __builtin_amdgcn_sbfe(l3, ln, 1);
    PIN(mOwn); PIN(mRot); PIN(mV); PIN(mLo); PIN(mS);
    const u32 up = tp * 104 + 8 + s0 * 16 + h32 * 16 + i16;
    const u32 hs = ln << 26;
    so[0] = WS_O + (parity * 64 - 1) * 20480 + (2 * j + (n >> 1)) * 1024 + (n & 1) * 512 + (up >> 4) * 20480 + (((ln & 16) | (up & 15)) << 4);
    so[1] = so[0] + 2 * 20480;
    if (NS == 4) so[0] |= ~hs & mLo & SGN;
    else so[1] |= (hs | (tp == 9 ? SGN : 0u)) & SGN;
    PIN(so[0]); PIN(so[1]);
    const int xl = n > 0 ? L_X + ((n - 1) * 7 + s0) * 512 : L_NONE, xh = n < 3 ? L_X + (n * 7 + s0) * 512 : L_NONE;
    aXr = xl + (int)h32 * (xh - xl) + ln * 4;
    aXw = aXr ^ 128;
    aCS = L_CQ + (n >> 1) * 256 + (n & 1) * 32 + g16 * 4;
    aCV = L_CS + 512 + n * 64 + i4;
    if (NS == 4) aCV += min(g16, 1u) * 0x10000;
    aF = Y_F + s0 * 4 + n * 4 + ln * 4 + min(ln, 1u) * 0x10000;
    aST = L_MR + s0 * 64 + i4;
    aMR = L_MR + s0 * 64 + g16;
    aH = n * 512 + ln * 8;
    const u32 xa = WS_XS + (((parity * 10 + tp) * 10 + j) * 14 + (n ? 7 : 0) + s0) * 512 + (edge ? 0u : SGN);
    xg = (xa + (ln & 31) * 16) | ((hs ^ (n ? SGN : 0u)) & SGN);
    PIN(aXw); PIN(aXr); if (NS == 3) PIN(aCS); PIN(aCV); PIN(aST); PIN(aMR); PIN(aH); PIN(aF); PIN(xg);
  }
};

template <int NS>
DEV void main_compute(const Params& p, rsrc_t rws, u32& tag, int lane, int wave, int tp, int parity, int rb, int j, int mg, int ng, G2& g2 STM_ARG) {
  constexpr int s0 = NS == 4 ? 0 : 4;
  const int n = (wave & 3) ^ (NS == 3);
  const int rt = parity * 10 + tp;
  u32 ep = epoch_get(p);
  Att at;
  at.init_k(lane, n);
  if (NS == 4) st_u(lane < 32 ? wave * SLOT + XB + lane * 4 : L_SYNC + wave * 64 + (lane & 15) * 4, lane < 32 ? 0xbf80bf80u : 0u);
  int tau = n - rb; tau += tau < 0 ? 5 : 0;
  const bool edge = ((0x0f00001e >> (n * 8 + tau)) & 1) != 0;
  SB();
  PST(2);
  if (INIT1) at.template init<NS>(lane, n, tau, edge, tp, parity, j);
  SB();
  PST(3);
  tag = epoch_tag(ep);
  const u32 (&aA0)[2] = at.aA0, (&aA1)[2] = at.aA1, (&aB)[2] = at.aB, (&so)[2] = at.so;
  const u32 &mOwn = at.mOwn, &mRot = at.mRot, &mV = at.mV, &mLo = at.mLo, &xg = at.xg, &aXw = at.aXw, &aXr = at.aXr, &aCS = at.aCS, &aCV = at.aCV,
            &aST = at.aST, &aMR = at.aMR, &aH = at.aH, &aF = at.aF;
  const u4& mS = at.mS;
  f4 acc[NS][3];
  u2 m0, mm, mp;
#pragma unroll
  for (int a = 0; a < NS; ++a)
#pragma unroll
    for (int b = 0; b < 3; ++b) acc[a][b] = f4{0.f, 0.f, 0.f, 0.f};
  bf8 A[3][NS], B[3][3];
  auto ldf = [&](int buf, int st, int kk) {
    const int rs = (st + 3) & 3, h = rs >> 1;
    const u32 of = (rs & 1) * SLOT;
    B[buf][0] = ld_tr8(aB[h] + of + kk * 4096, 512);
    A[buf][0] = ld_b128((kk ? aA1[h] : aA0[h]) + of + s0 * 2048);
    B[buf][1] = ld_tr8(aB[h] + of + kk * 4096 + 8192, 512);
    B[buf][2] = ld_tr8(aB[h] + of + kk * 4096 + 16384, 512);
#pragma unroll
    for (int a = 1; a < NS; ++a) A[buf][a] = ld_b128((kk ? aA1[h] : aA0[h]) + of + (s0 + a) * 2048);
  };
  auto mma = [&](int buf, int b0, int b1) {
    if (MORD == 1) {
#pragma unroll
      for (int b = b0; b < b1; ++b)
#pragma unroll
        for (int i = 0; i < NS; ++i) {
          const int a = (b & 1) ? NS - 1 - i : i;
          acc[a][b] = b < 2 ? mfma(B[buf][b], A[buf][a], acc[a][b]) : mfma(A[buf][a], B[buf][b], acc[a][b]);
        }
    } else if (MORD == 2) {
#pragma unroll
      for (int a = 0; a < NS; ++a)
#pragma unroll
        for (int i = 0; i < 2; ++i) { const int b = (a & 1) ? 1 - i : i; acc[a][b] = mfma(B[buf][b], A[buf][a], acc[a][b]); }
      if (b1 == 3) {
#pragma unroll
        for (int a = 0; a < NS; ++a) acc[a][2] = mfma(A[buf][a], B[buf][2], acc[a][2]);
      }
    } else {
#pragma unroll
    for (int a = 0; a < NS; ++a)
#pragma unroll
      for (int b = b0; b < b1; ++b)
        acc[a][b] = b < 2 ? mfma(B[buf][b], A[buf][a], acc[a][b]) : mfma(A[buf][a], B[buf][b], acc[a][b]);
    }
  };
  auto pattern = [&]() {
#pragma unroll
    for (int i = 0; i < NS + 6; ++i) {
      if (i < 3 * NS) __builtin_amdgcn_sched_group_barrier(0x008, 1, 0);
      __builtin_amdgcn_sched_group_barrier(0x100, 1, 0);
    }
    if (3 * NS > NS + 6) __builtin_amdgcn_sched_group_barrier(0x008, 2 * NS - 6, 0);
  };

  u32 inner = (n + 1) & 2, csm0 = L_CQ + (n >> 1) * 256 + (n & 1) * 32;
  const u4 sws = srd(p.ws);
  u32 eg = 0x0f00001eu & (1u << (n * 8 + tau));
  SPIN(eg);
  auto isedge = [&]() { SPIN(eg); return __builtin_expect(eg != 0, 1); };
  u32 fn0 = Y_F + s0 * 4 + ((0x8400u >> (4 * n)) & 15), fn1 = (0x4884u >> (4 * n)) & 15, xd = n ? 7 * 512 : -7 * 512;
  u32 c0s = tp == 0 ? 0xf149f2cau : 0u;
  SPIN(inner); SPIN(csm0); SPIN(fn0); SPIN(fn1); SPIN(xd); SPIN(c0s);
#define PRIO(PI, PO) asm volatile("s_setprio %1\n\t.if %1 != %2\n\ts_cmp_lg_u32 %0, 0\n\ts_cbranch_scc1 1f\n\ts_setprio %2\n\t1:\n\t.endif" :: "s"(inner), "i"(PI), "i"(PO) : "scc")
  u4 on; on.x = on.y = on.z = on.w = 0xbf80bf80u;
  if (NS == 3) PIN(on);
  u32 uLo, uHi, aFn, xr, etag, aCr, nV, nLo, aL, lv[3];
  u2 hs; u4 tv[NS]; f4 c0;
  auto fill = [&](int i, bool late = false) {
    if (i == 0) { m0.x = __builtin_amdgcn_perm(mS.y, mS.x, 0x07060100); m0.y = __builtin_amdgcn_perm(mS.w, mS.z, 0x07060100); PIN(m0); }
    if (i == 1) { mm.x = ~m0.x & mLo; mm.y = ~m0.y & mLo; PIN(mm); }
    if (i == 2) { mp.x = ~(m0.x | mLo); mp.y = ~(m0.y | mLo); PIN(mp); }
    if (i == 3) { nLo = ~mLo; aCr = (aCV & 0xffffu) | (L_CS & ~0xffffu); PIN(nLo); PIN(aCr); }
    if (i == 12) { lv[0] = 1; PIN(lv[0]); if (NS == 4) { lv[1] = 2; PIN(lv[1]); } else { aL = Y_F + n * 4; PIN(aL); } }
    if (NS == 4 && !late) {
      if (i == 4) { const float cf = __uint_as_float(c0s); c0 = f4{cf, cf, cf, cf}; PIN(c0); }
      return;
    }
    if (i == 4) { u32 up = __builtin_amdgcn_sbfe(aH, 8, 1); PIN(up); hs.x = (mV ^ up) & 0x3f803f80u; aFn = up & fn1; PIN(hs.x); PIN(aFn); }
    if (i == 5) { hs.y = hs.x; PIN(hs); aFn += fn0; xr = xg + xd; PIN(aFn); PIN(xr); }
    if (i == 6 && late) { etag = (int)xg >> 31; nV = ~mV; PIN(etag); PIN(nV); etag = tag & ~etag; PIN(etag); }
    if (i == 7 && late) { uLo = 0x3f80u; uHi = 0x3f800000u; PIN(uLo); PIN(uHi); if (NS == 4) { lv[2] = 3; PIN(lv[2]); } }
    if (i >= 8 && i < 8 + NS && late) { u4& v = tv[i - 8]; v.y = v.w = tag; PIN(v); }
  };
  auto fills = [&]() {
#pragma unroll
    for (int i = 0; i < 13; ++i) fill(i);
    SB();
  };
  if (NS == 3) fills();
  auto pre2 = [&]() {
    int w_ = wave, mg_ = mg, ng_ = ng, tp_ = tp;
    SPIN(w_); SPIN(mg_); SPIN(ng_); SPIN(tp_);
    g2.pre(w_, mg_, ng_); g2.pub(tp_, parity, j, rb);
    SB();
  };
  STAMP(1);
  PST(4);
  if (PBRK && NS == 4) NEW_PAGE();
  lds_bar();
  PST(5);
  STAMP(2);
  CSTAMP(0);
  ldf(0, 0, 0);
#pragma unroll
  for (int s = 0; s < 9; ++s) {
    SB();
    if (s == 8 && NS == 3 && C3E) { PRIO(2, 3); SB(); }
    ldf(1, s, 1);
    mma(0, 0, 3);
    if (s == 8 && NS == 3) {
#pragma unroll
      for (int i = 0; i < 9 - S8D; ++i) {
        __builtin_amdgcn_sched_group_barrier(0x008, 1, 0);
        if (i < S8D) __builtin_amdgcn_sched_group_barrier(0x100, 2, 0); else __builtin_amdgcn_sched_group_barrier(0x100, 1, 0);
      }
    } else pattern();
    SB();
    if (s == 0 && !INIT1) { at.template init<NS>(lane, n, tau, edge, tp, parity, j); SB(); }
    if (s == 8) break;
    if (s >= 1) KST(2 * s - 1);
    if (s >= 6) EST(2 * s - 11);
    if (s < 3) PST(6 + 2 * s);
    if (NS == 3 && s >= PACE0 && PACE) asm volatile(".rept %0\n\ts_nop 7\n\t.endr" :: "i"(PACE));
    bar();
    if (PBRK && NS == 3 && s == 7) NEW_PAGE();
    if (s < 3) PST(7 + 2 * s);
    KST(2 * s);
    if (s >= 5) EST(2 * s - 10);
    SB();
    ldf(0, s + 1, 0);
    mma(1, 0, 3);
    pattern();
    SB();
  }
  if (NS == 4 || !C3E) PRIO(2, 3);
  KST(15);
  EST(5);
  if (PBRK && NS == 4 && CSD) { pre2(); NEW_PAGE(); fills(); if (CSD > 2) __builtin_amdgcn_s_sleep(CSD - 2); }
  if (NS == 4)
    asm volatile("s_mov_b32 m0, %8\n\ts_mov_b32 exec_lo, 0x10001\n\ts_mov_b32 exec_hi, 0x10001\n\t"
                 "ds_write_addtid_b32 %0\n\tds_write_addtid_b32 %1 offset:4\n\tds_write_addtid_b32 %2 offset:8\n\tds_write_addtid_b32 %3 offset:12\n\t"
                 "ds_write_addtid_b32 %4 offset:16\n\tds_write_addtid_b32 %5 offset:20\n\tds_write_addtid_b32 %6 offset:24\n\tds_write_addtid_b32 %7 offset:28\n\t"
                 "s_mov_b64 exec, -1"
                 :: "v"(acc[0][0][0]), "v"(acc[0][0][1]), "v"(acc[0][0][2]), "v"(acc[0][0][3]), "v"(acc[0][1][0]), "v"(acc[0][1][1]), "v"(acc[0][1][2]), "v"(acc[0][1][3]),
                    "s"(csm0) : "memory");
  SB();
  if (PBRK && NS == 4 && !CSD) NEW_PAGE();
  const bf8 ones = __builtin_bit_cast(bf8, on);
  SB();
  __builtin_amdgcn_s_waitcnt(0xc07f);
  EST(7);
  bar();
  EST(6);
  FST(0);
  SB();
  f2 stt[NS];
  f4 cs[2];
  auto ldqk = [&](int buf, int kk) {
    B[buf][0] = ld_tr8(aB[0] + kk * 4096, 512);
    A[buf][0] = ld_b128((kk ? aA1[0] : aA0[0]) + s0 * 2048);
    B[buf][1] = ld_tr8(aB[0] + kk * 4096 + 8192, 512);
#pragma unroll
    for (int a = 1; a < NS; ++a) A[buf][a] = ld_b128((kk ? aA1[0] : aA0[0]) + (s0 + a) * 2048);
    FENCE();
  };
  auto mmcs = [&](int buf) { if (NS == 3) { cs[1] = mfma(B[buf][1], ones, cs[1]); cs[0] = mfma(B[buf][0], ones, cs[0]); } };
  if (NS == 3) { cs[1] = ld_f4(aCS + 16); cs[0] = ld_f4(aCS); FENCE(); }
  ldqk(0, 0);
  ldqk(2, 1);
#pragma unroll
  for (int a = 0; a < NS; ++a) { stt[a].x = ld_f(aST + a * 64); stt[a].y = ld_f(aST + a * 64 + 512); FENCE(); }
  B[0][2] = ld_tr8(aB[0] + 16384, 512); B[2][2] = ld_tr8(aB[0] + 4096 + 16384, 512);
  mma(1, 0, 3);
#pragma unroll
  for (int i = 0; i < 3 * NS; ++i) {
    __builtin_amdgcn_sched_group_barrier(0x008, 1, 0);
    __builtin_amdgcn_sched_group_barrier(0x100, NS == 4 ? RPM4 : RPM, 0);
  }
  SB();
  mmcs(1);
  if (NS == 3) { PIN(cs[0]); PIN(cs[1]); }
  SB();
  FST(1);
  mma(0, 0, 2);
  mmcs(0);
  if (NS == 3) { PIN(cs[0]); PIN(cs[1]); }
  SB();
  auto step = [&](int l) { asm volatile("ds_write_b32 %0, %1" :: "v"(aF), "v"(lv[l - 1]) : "memory"); };
  u32 lvl;
  auto reached = [&](u32 v) { return __builtin_expect((u32)__builtin_amdgcn_readfirstlane(lvl) >= v, 1); };
  mmcs(2);
  if (NS == 3) { PIN(cs[0]); PIN(cs[1]); }
  SB();
  acc[0][1] = mfma(B[2][1], A[2][0], acc[0][1]); SB();
  acc[0][0] = mfma(B[2][0], A[2][0], acc[0][0]); SB();
#pragma unroll
  for (int a = 1; a < NS; ++a) { acc[a][1] = mfma(B[2][1], A[2][a], acc[a][1]); SB(); }
#pragma unroll
  for (int i = 0; i < 5; ++i) {
    if (i + 1 < NS) acc[i + 1][0] = mfma(B[2][0], A[2][i + 1], acc[i + 1][0]);
    if (NS == 4) {
      if (i >= 3) acc[0][2] = mfma(A[2 * (i - 3)][0], B[2 * (i - 3)][2], acc[0][2]);
      SB();
      if (i < 4) {
#pragma unroll
        for (int r = 2 * (i & 1); r < 2 * (i & 1) + 2; ++r) { float v = dppf<0x150>(acc[0][1 - (i >> 1)][r]); PIN(v); cs[1 - (i >> 1)][r] = v; }
      }
    }
    SB();
  }
  PIN(cs[0]); PIN(cs[1]);
#pragma unroll
  for (int a = 0; a < NS; ++a) PIN(stt[a]);
#pragma unroll
  for (int a = 0; a < NS; ++a) { PIN(acc[a][0]); PIN(acc[a][1]); PIN(acc[a][2]); }
  SB();
  CSTAMP(1);
  STAMP(3);
  FST(2);
  auto bc4 = [](float v) { return f4{v, v, v, v}; };
  auto pk4 = [](f4 v) { u2 r; r.x = pk2(v[0], v[1]); r.y = pk2(v[2], v[3]); return r; };
  u2 q[NS], kLo[NS], kHi[NS + 1];
  auto lnq = [&]() {
#pragma unroll
    for (int a = NS - 1; a >= 0; --a) {
      if (QSC) {
#pragma unroll
        for (int r = 0; r < 4; ++r) { float t = fmaf(stt[a].x, cs[0][r], acc[a][0][r]); PIN(t); acc[a][0][r] = t; }
      } else acc[a][0] = __builtin_elementwise_fma(bc4(stt[a].x), cs[0], acc[a][0]);
    }
    SB();
  };
  {
    u2 k[NS];
    if (KSC) {
#pragma unroll
      for (int a = 0; a < NS; ++a)
#pragma unroll
        for (int r = 0; r < 4; ++r) { float t = fmaf(stt[a].x, cs[1][r], acc[a][1][r]); PIN(t); acc[a][1][r] = t; }
      SB();
#pragma unroll
      for (int a = 0; a < NS; ++a)
#pragma unroll
        for (int r = 0; r < 4; ++r) { float t = acc[a][1][r] * stt[a].y; PIN(t); acc[a][1][r] = t; }
      SB();
    } else {
#pragma unroll
      for (int a = 0; a < NS; ++a) { acc[a][1] = __builtin_elementwise_fma(bc4(stt[a].x), cs[1], acc[a][1]); PIN(acc[a][1]); }
      SB();
#pragma unroll
      for (int a = 0; a < NS; ++a) { acc[a][1] *= stt[a].y; PIN(acc[a][1]); }
      SB();
    }
#pragma unroll
    for (int a = NS - 1; a >= 0; --a) { k[a] = pk4(acc[a][1]); PIN(k[a]); }
    SB();
#pragma unroll
    for (int i = 0; i < NS; ++i) {
      const int a = NS - 1 - i;
      asm volatile("v_and_b32 %0, %4, %7\n\tv_and_b32 %1, %5, %7\n\t"
                   "v_and_b32_dpp %0, %4, %6 row_ror:8 row_mask:0xf bank_mask:0x3\n\tv_and_b32_dpp %1, %5, %6 row_ror:8 row_mask:0xf bank_mask:0x3\n\t"
                   "v_and_b32 %2, %4, %6\n\tv_and_b32 %3, %5, %6\n\t"
                   "v_and_b32_dpp %2, %4, %7 row_ror:8 row_mask:0xf bank_mask:0xc\n\tv_and_b32_dpp %3, %5, %7 row_ror:8 row_mask:0xf bank_mask:0xc"
                   : "=&v"(kHi[a + 1].x), "=&v"(kHi[a + 1].y), "=&v"(kLo[a].x), "=&v"(kLo[a].y) : "v"(k[a].x), "v"(k[a].y), "v"(mOwn), "v"(mRot));
      if (NS == 4 && i == 0) { st_u2(L_HK + aH, kHi[NS]); st_f(aCV, acc[0][2][0]); step(1); }
    }
  }
  SB();
  lnq();
  if (NS == 3) { FENCE(); lvl = lds_peek(aL); FENCE(); kHi[0] = ld_u2(L_HK + aH); FENCE(); }
  SB();
  FST(3);
  const f4 z = {0.f, 0.f, 0.f, 0.f};
  f4 t[NS];
  {
    constexpr int L = NS - 1;
    f4 d0[NS], d1[NS];
    u4 qq[NS];
    auto cq_ = [&](int a) { if (a >= 0) q[a] = pk4(acc[a][0]); SB(); };
    auto lo_ = [&](int a) { qq[a].x = q[a].x & mLo; qq[a].y = q[a].y & mLo; SB(); };
    auto hi_ = [&](int a) { qq[a].z = q[a].x & nLo; qq[a].w = q[a].y & nLo; SB(); };
    auto d0_ = [&](int a) { d0[a] = mfma16(kLo[a], q[a], (NS == 4 && a == 0) ? c0 : z); SB(); };
    auto sel = [&](int a, int r0, int r1) { for (int r = r0; r < r1; ++r) t[a][r] = fsel(mS[r], d0[a][r], d1[a][r]); SB(); };
    auto pkt = [&](int a) { tv[a].x = pk2(t[a][0], t[a][1]); tv[a].z = pk2(t[a][2], t[a][3]); SB(); };
    cq_(L); cq_(L - 1);
    d0_(L); lo_(L); hi_(L);
    d0_(L - 1); lo_(L - 1); cq_(L - 2);
    d1[L] = mfma(mk8(kHi[L], kHi[L + 1]), __builtin_bit_cast(bf8, qq[L]), z); SB(); hi_(L - 1); cq_(L - 3);
    if (NS == 3) { fill(10, true); SB(); }
    d1[L - 1] = mfma16(kHi[L - 1], qq[L - 1].xy, z); SB(); lo_(L - 2); hi_(L - 2);
    d1[L - 1] = mfma16(kHi[L], qq[L - 1].zw, d1[L - 1]); SB();
    if (NS == 4) { fill(11, true); SB(); }
    else {
      fill(9, true); fill(8, true);
      PIN(d0[L]); PIN(d0[L - 1]); PIN(d1[L]); PIN(d1[L - 1]); PIN(qq[0]); PIN(q[0]);
      if (!reached(1)) { lds_wait_ge<0>(Y_F + n * 4, 1); kHi[0] = ld_u2(L_HK + aH); }
      SB();
    }
    d0_(L - 2); sel(L, 0, 3);
    d1[L - 2] = mfma(mk8(kHi[L - 2], kHi[L - 1]), __builtin_bit_cast(bf8, qq[L - 2]), z); SB(); sel(L, 3, 4); pkt(L);
    if (NS == 4) {
      d0_(0); fill(10, true); fill(9, true); SB();
      d1[0] = mfma16(kHi[1], q[0], z);
      SB(); fill(8, true); SB();
    }
#pragma unroll
    for (int a = L - 1; a >= 0; --a) { sel(a, 0, 4); pkt(a); }
  }
#pragma unroll
  for (int a = 0; a < NS; ++a) { PIN(t[a]); PIN(tv[a]); }
  FST(4);
  if (isedge()) {
#pragma unroll
    for (int a = NS - 1; a >= 0; --a) raw_store(tv[a], sws, xg + a * 512, 0, 16);
  }
  SB();
  f4 vm[NS], vr[NS];
  float csv, cq[NS];
  u32 aHV;
  auto vmm = [&](int i) { const int a = NS - 1 - i % 3, buf = 2 * (i / 3); acc[a][2] = mfma(A[buf][a], B[buf][2], acc[a][2]); SB(); };
#pragma unroll
  for (int a = 0; a < NS; ++a) {
    asm volatile("ds_write2st64_b32 %0, %1, %2 offset0:%3 offset1:%4" :: "v"(aXw), "v"(tv[a].x), "v"(tv[a].z), "i"(2 * a), "i"(2 * a + 1) : "memory");
    SB();
    if (a == 0) vmm(0);
    if (a == 2) vmm(1);
  }
  step(NS == 4 ? 2 : 1);
  STAMP(10);
  FST(5);
  if (NS == 3) PRIO(Q2I, Q2O); else PRIO(2, PR2);
  SB();
  vmm(2);
  csv = ld_f(aCr); vm[NS - 1] = ld_f4(aMR + (NS - 1) * 64); SB();
  vmm(3);
  vm[NS - 2] = ld_f4(aMR + (NS - 2) * 64); vm[NS - 3] = ld_f4(aMR + (NS - 3) * 64); SB();
  vmm(4);
  if (NS == 4) vm[0] = ld_f4(aMR);
  vr[NS - 1] = ld_f4(aMR + 512 + (NS - 1) * 64); SB();
  vmm(5);
#pragma unroll
  for (int a = NS - 2; a >= 0; --a) vr[a] = ld_f4(aMR + 512 + a * 64);
  SB();
  aHV = L_HV + aH;
  PIN(aHV);
  u4 p0, vp;
  p0.x = p0.y = 0; PIN(p0);
  if (NS == 3) { vp.x = vp.y = 0; PIN(vp); }
  if (NS == 4) { fill(4, true); fill(5, true); }
  fill(6, true); fill(7, true);
#pragma unroll
  for (int a = 0; a < NS; ++a) { cq[a] = stt[a].y * (0.22360679774997896f * 1.4426950408889634f); PIN(cq[a]); }
  SB();
  FST(6);
  u4 e[NS];
#pragma unroll
  for (int a = 0; a < NS; ++a) asm volatile("" : "=v"(e[a]));
  auto look = [&]() {
#pragma unroll
    for (int a = 0; a < NS; ++a) e[a] = raw_load(sws, xr + a * 512, 0, 16);
  };
  u2 vLo[NS];
#pragma unroll
  for (int a = NS - 1; a >= 0; --a) acc[a][2] = __builtin_elementwise_fma(vm[a], bc4(csv), acc[a][2]);
  SB();
#pragma unroll
  for (int a = NS - 1; a >= 0; --a) acc[a][2] *= vr[a];
  SB();
#pragma unroll
  for (int a = NS - 1; a >= 0; --a) { vLo[a] = pk4(acc[a][2]); PIN(vLo[a]); }
  SB();
  if (LKP == 0 || (LKP == 2 && NS == 3)) { if (isedge()) look(); }
  SB();
  constexpr u32 XT = NS == 4 ? 2 : 1;
  u2 y[NS];
  u32 cx;
  auto ldy = [&]() {
    FENCE(); cx = lds_peek(aFn); FENCE();
#pragma unroll
    for (int a = 0; a < NS; ++a) { y[a].x = ld_u(aXr + a * 512); y[a].y = ld_u(aXr + a * 512 + 256); }
    FENCE();
  };
  ldy();
  SB();
  u4 vv[NS];
  {
    u32 w[NS][4];
#pragma unroll
    for (int a = NS - 1; a >= 0; --a) { w[a][2] = vLo[a].x & nV; w[a][3] = vLo[a].y & nV; w[a][0] = vLo[a].x & mV; w[a][1] = vLo[a].y & mV; SB(); }
#pragma unroll
    for (int a = NS - 1; a >= 0; --a) {
      asm volatile("v_permlane32_swap_b32 %0, %2\n\tv_permlane32_swap_b32 %1, %3" : "+v"(w[a][0]), "+v"(w[a][1]), "+v"(w[a][2]), "+v"(w[a][3]));
      vv[a] = u4{w[a][0], w[a][1], w[a][2], w[a][3]};
      if (NS == 4 && a == NS - 2) { st_u2(aHV, vv[NS - 1].zw); step(3); }
      SB();
    }
#pragma unroll
    for (int a = 0; a < NS; ++a) PIN(vv[a]);
  }
  if (NS == 3) { FENCE(); lvl = lds_peek(aL); FENCE(); vp.zw = ld_u2(aHV); FENCE(); }
  SB();
  if (LKP == 1 || (LKP == 2 && NS == 4)) { if (isedge()) look(); }
  SB();
  FST(7);
  if (__any(cx < XT)) {
#pragma nounroll
    for (u32 spin = 0; spin < (1u << 20); ++spin) {
      FENCE();
      if (__all(lds_peek(aFn) >= XT)) break;
    }
    ldy();
  }
  auto addp = [](f4& t, u32 a, u32 b, u32 lo, u32 hi) {
    t[0] = __builtin_amdgcn_fdot2_f32_bf16(__builtin_bit_cast(bf2, a), __builtin_bit_cast(bf2, lo), t[0], false);
    t[1] = __builtin_amdgcn_fdot2_f32_bf16(__builtin_bit_cast(bf2, a), __builtin_bit_cast(bf2, hi), t[1], false);
    t[2] = __builtin_amdgcn_fdot2_f32_bf16(__builtin_bit_cast(bf2, b), __builtin_bit_cast(bf2, lo), t[2], false);
    t[3] = __builtin_amdgcn_fdot2_f32_bf16(__builtin_bit_cast(bf2, b), __builtin_bit_cast(bf2, hi), t[3], false);
  };
#pragma unroll
  for (int a = 0; a < NS; ++a) { addp(t[a], y[a].x, y[a].y, uLo, uHi); PIN(t[a]); }
  if (NS == 3) {
    if (!reached(3)) { lds_wait_ge<0>(Y_F + n * 4, 3); vp.zw = ld_u2(aHV); }
    PIN(vp);
  }
  if (NS == 3) { if (Q3I != Q2I || Q3O != Q2O) PRIO(Q3I, Q3O); } else if (PR3 != PR2) PRIO(2, PR3);
  STAMP(8);
  FST(8);
  if (isedge()) {
    auto bad = [&]() {
      u32 b = __builtin_amdgcn_bitop3_b32(e[0].y, e[0].w, etag, 0x7e);
#pragma unroll
      for (int a = 1; a < NS; ++a) b |= __builtin_amdgcn_bitop3_b32(e[a].y, e[a].w, etag, 0x7e);
      PIN(b);
      return __any(b != 0u);
    };
    if (bad()) {
#pragma nounroll
      for (u32 spin = 0; spin < SPIN_CAP; ++spin) {
        FENCE();
        look();
        if (!bad()) break;
      }
    }
#pragma unroll
    for (int a = 0; a < NS; ++a) { addp(t[a], e[a].x, e[a].z, uLo, uHi); PIN(t[a]); }
  }
  STAMP(6);
  FST(9);
  SB();
  float mx[4];
#pragma unroll
  for (int a = 0; a < NS; ++a) mx[a] = vmax(vmax(t[a][0], t[a][1]), t[a][2]);
  SB();
#pragma unroll
  for (int a = 0; a < NS; ++a) mx[a] = vmax(mx[a], t[a][3]);
  if (NS == 3) mx[3] = mx[2];
  SB();
  {
    u32 c[2];
#pragma unroll
    for (int h = 0; h < 2; ++h) {
      auto w = __builtin_amdgcn_permlane16_swap(__float_as_uint(mx[2 * h]), __float_as_uint(mx[2 * h + 1]), false, false);
      mx[2 * h] = __uint_as_float(w[0]); mx[2 * h + 1] = __uint_as_float(w[1]);
    }
    SB();
#pragma unroll
    for (int h = 0; h < 2; ++h) c[h] = __float_as_uint(vmax(mx[2 * h], mx[2 * h + 1]));
    SB();
#pragma unroll
    for (int h = 0; h < 2; ++h) {
      if (NS == 3 && h == 1) { mx[2] = __uint_as_float(c[1]); continue; }
      auto w = __builtin_amdgcn_permlane16_swap(c[h], c[h], false, false);
      mx[2 * h] = __uint_as_float(w[0]); mx[2 * h + 1] = __uint_as_float(w[1]);
    }
    SB();
  }
#pragma unroll
  for (int a = 0; a < NS; ++a) { mx[a] *= -cq[a]; SB(); }
#pragma unroll
  for (int a = NS - 1; a >= 0; --a) { t[a] = __builtin_elementwise_fma(t[a], bc4(cq[a]), bc4(mx[a])); SB(); }
  f4 o[NS], sm[NS];
  u2 pp[NS], ob[4];
  bf8 p1[NS];
  auto ex = [&](int a, int r) { if (a >= 0) { t[a][r] = __builtin_amdgcn_exp2f(t[a][r]); t[a][r + 1] = __builtin_amdgcn_exp2f(t[a][r + 1]); } SB(); };
  auto rc = [&](int a) { asm volatile("v_rcp_f32 %0, %1" : "=v"(mx[a]) : "v"(sm[a][0]), "v"(sm[a])); };
  ex(NS - 1, 0); ex(NS - 1, 2); ex(NS - 2, 0);
  FST(10);
#pragma unroll
  for (int a = NS - 1; a >= 0; --a) {
    const bool prev = NS == 3 || a > 0;
    pp[a] = pk4(t[a]); SB();
    p1[a] = mk8(and2(pp[a], m0), and2(pp[a], mp)); PIN(p1[a]); SB();
    sm[a] = mfma16(hs, pp[a], z); SB();
    if (prev) { p0.z = pp[a].x & mm.x; p0.w = pp[a].y & mm.y; PIN(p0); }
    if (a == 0) rc(NS - 1);
    SB();
    o[a] = mfma(__builtin_bit_cast(bf8, vv[a]), p1[a], z); SB();
    ex(a - 1, 2);
    if (a == 0) { rc(NS - 2); SB(); }
    if (prev) { o[a] = mfma(__builtin_bit_cast(bf8, a ? vv[a - 1] : vp), __builtin_bit_cast(bf8, p0), o[a]); SB(); }
    ex(a - 2, 0);
  }
  auto put = [&](int h) {
    auto wx = __builtin_amdgcn_permlane32_swap(ob[2 * h].x, ob[2 * h + 1].x, false, false);
    auto wy = __builtin_amdgcn_permlane32_swap(ob[2 * h].y, ob[2 * h + 1].y, false, false);
    u4 v; v.x = wx[0]; v.y = wy[0]; v.z = wx[1]; v.w = wy[1];
    raw_store(v, sws, so[h], 0, 16);
    SB();
  };
  if (NS == 4) { rc(1); SB(); }
#pragma unroll
  for (int a = NS - 1; a > 0; --a) { o[a] *= mx[a]; SB(); }
  PIN(sm[0]);
  mx[0] = __builtin_amdgcn_rcpf(sm[0][0]); SB();
#pragma unroll
  for (int a = NS - 1; a > 1; --a) { ob[a] = pk4(o[a]); SB(); }
  if (NS == 3) asm volatile("" : "=v"(ob[3]));
  o[0] *= mx[0]; SB();
  ob[1] = pk4(o[1]); SB();
  ob[0] = pk4(o[0]); SB();
  put(1); put(0);
  CSTAMP(2);
  STAMP(4);
  FST(11);
  __builtin_amdgcn_s_setprio(0);
  SB();
  if (PBR2) NEW_PAGE();
  SB();
  if (NS == 3) pre2();
}

constexpr u32 A_D = 0, A_G = L_WO, A_U = L_WO + 40960;
constexpr u32 A_XN = 40960;
constexpr u32 A_RED = A_XN + 2560;
constexpr u32 A_H = A_RED + 2048;
constexpr u32 A_TOP = A_H + 1024;

DEV void aux_loader(Params& p, int lane, int lw, int a7, int f, bool ffn, int mg, int ng, int koff, Epi& ei STM_ARG) {
  const u32 ep = epoch_get(p);
  const u4 srdo = srd(p.wo), srdg = srd(p.wg), srdu = srd(p.wu), srdd = srd(p.wd);
  const int rr = lane >> 2;
  u32 vg = (lw * 16 + rr) * (F_ * 2) + f * 64 + (((lane & 3) ^ (((rr >> 3) & 1) << 1)) << 4);
  u32 vd = (f * 32 + lw * 8 + (lane >> 3)) * 1280 + (lane & 7) * 16;
  u32 vt = min((f * 4 + lw) * 30 + min(lane, 29), 6399) * 128;
  u32 ho = (lane | lw) ? 0x80000000u : WS_FLAG + (200 + f) * 256 + 128;
  PIN(vg); PIN(vd); PIN(vt); PIN(ho);
  ei.init(lane, lw, mg, ng);
  WoDma wo; wo.init(lane, lw, ng, 0, 104 + lw);
  u32 tag = epoch_tag(ep);
  PIN(tag);
  if (FFN_DELAY) __builtin_amdgcn_s_sleep(FFN_DELAY);
  STAMP(9);
  if (ffn) {
    const u4 s0 = (f & 1) ? srdu : srdg, s1 = (f & 1) ? srdg : srdu;
    const u32 a0 = ((f & 1) ? A_U : A_G) + lw * 1024, a1 = ((f & 1) ? A_G : A_U) + lw * 1024;
    dma_run<10, 4096, 64 * F_ * 2, 0, TH0>(a0, vg, s0, 0);
    asm volatile("s_mov_b32 m0, %0\n\ts_nop 0\n\tbuffer_load_dword %1, %2, 0 offen lds" :: "s"(L_DUMP + lw * 256), "v"(vt), "s"(srdo));
    STAMP(10);
    dma_run<10, 4096, 64 * F_ * 2, 0, TH1>(a1, vg, s1, 0);
    if (!DLATE) dma_run<10, 4096, 128>(A_D + lw * 1024, vd, srdd, 0);
    STAMP(1);
  } else {
    wo.issue<0, 27, TH0>(srdo, lw);
  }
  wait_bar<DLATE ? 0 : 10>();
  if (DLATE && ffn) dma_run<10, 4096, 128>(A_D + lw * 1024, vd, srdd, 0);
  asm volatile("buffer_store_dword %0, %1, %2, 0 offen sc1" :: "v"(tag), "v"(ho), "s"(srd(p.ws)));
  if (BOXW == 2) asm volatile("buffer_atomic_add %0, %1, %2, 0 offen" :: "v"(0u), "v"((lane | (lw & 2)) ? 0x80000000u : WS_BOX + (mg * 8 + ng) * 256 + lw * 64), "s"(srd(p.ws)));
  bar();
  STAMP(2);
  ei.issue(srd(p.x), srd(p.ws), WS_ST2);
  wo.issue<0, WOC, AW0, 1>(srdo, lw, f);
  asm volatile("s_cmp_gt_u32 %0, 53\n\ts_cbranch_scc1 1f\n\ts_waitcnt vmcnt(%1)\n\ts_barrier\n\ts_branch 2f\n1:\n\ts_waitcnt vmcnt(0)\n2:" :: "s"(f), "i"(WOC + 5) : "scc", "memory");
  STAMP(3);
  ei.wait<WOC>();
  ei.check(p.ws.p, tag);
  ei.tag = tag;
  SB();
#pragma unroll
  for (int i = 0; i < YG; ++i) ei.gate(i);
  SB();
  asm volatile("buffer_load_dwordx4 %0, off, %1, 0 offset:%2 sc1" : "=v"(ei.fin) : "s"(srd(p.ws)), "i"(WS_FIN));
  static_assert((26 - WOC) % 3 == 0, "");
  const u32 skip = lw == 3 ? 99 : f;
  wo.issue<WOC, 27, AWT, 1, 3>(srdo, lw, skip);
  asm volatile("s_cmp_gt_u32 %1, 53\n\ts_cbranch_scc0 1f\n\ts_waitcnt vmcnt(1)\n1:\n\ts_waitcnt vmcnt(%2)" : "+v"(ei.fin) : "s"(skip), "i"((26 - WOC) * 4 / 3 + 1) : "scc");
  STAMP(4);
}

DEV void aux_compute(const Params& p, rsrc_t rws, u32& tag, int lane, int wave, int f, bool ffn, int xcd, int a7, RowIn& rin STM_ARG) {
  const int task = a7 + 7 * wave;
  if (task < 32) rin.stats(p, rws, tag, lane, xcd * 256 + task * 8, A_TOP + wave * 16 STM);
  else tag = epoch_tag(epoch_get(p));
  PST(4);
  const int g = lane >> 4, i16 = lane & 15;
  const int q = i16 >> 2, pp = lane & 3;
  const int t = wave & 1, kq = wave >> 1;
  u32 ab = (kq * 160 + 8 * g + q) * 64 + (((2 * t + (pp >> 1)) ^ ((g & 1) << 1)) << 4) + (pp & 1) * 8;
  u32 xb = A_XN + (i16 & 1) * 1280 + (kq * 160 + 8 * g) * 2;
  u32 ar = i16 < 2 ? A_RED + (wave * 16 + g * 2 + i16) * 16 : L_NONE + lane * 16;
  u32 ad[5], ah[5];
#pragma unroll
  for (int i = 0; i < 5; ++i) {
    const int nt = wave * 5 + i;
    ad[i] = A_D + (8 * g + q) * 128 + pp * 8 + (nt >> 2) * 4096 + (nt & 3) * 32;
    ah[i] = (i16 & 7) == i ? A_H + wave * 128 + (i16 >> 3) * 64 + g * 16 : L_NONE + lane * 16;
    PIN(ad[i]); PIN(ah[i]);
  }
  u32 as = A_RED + (lane >> 5) * 256 + (lane & 31) * 4, aw = A_H + wave * 128 + ((lane >> 2) & 1) * 64 + (4 * (lane >> 3) + (lane & 3)) * 2;
  u32 yo = (i16 & 7) < 5 ? WS_YP + ((wave * 5 + (i16 & 7)) * 56 + f) * 128 + (i16 >> 3) * 64 + g * 16 : 0x80000000u;
  PIN(ab); PIN(xb); PIN(ar); PIN(as); PIN(aw); PIN(yo);
  STAMP(1);
  u32 so = 0x80000000u, xo = 0x80000000u;
  if (wave >= 5) {
    if (wave == 5) st_u(L_SYNC + lane * 4, 0);
    const u32 nt = (a7 < 4 && wave == 5) ? 5 : 4, a = A_TOP + min((u32)lane & 7, nt - 1) * 16;
    u4 k;
#pragma nounroll
    for (u32 spin = 0; spin < (1u << 20); ++spin) {
      FENCE();
      k = __builtin_bit_cast(u4, ld_f4(a));
      if (__all(k.y == tag && k.w == tag)) break;
      __builtin_amdgcn_s_sleep(1);
    }
    if (wave == 5) {
      if ((lane & 7) >= nt) k.x = k.z = 0;
      u32 k1 = k.x, k2 = k.z;
      top2_merge(k1, k2, dppu<0xB1>(k1), dppu<0xB1>(k2));
      top2_merge(k1, k2, dppu<0x4E>(k1), dppu<0x4E>(k2));
      top2_merge(k1, k2, dppu<0x141>(k1), dppu<0x141>(k2));
      k.x = k1; k.z = k2;
      bst128_sc1(rws, lane < 8 ? WS_KEY + f * 128 + lane * 16 : 0x80000000u, k);
    } else {
      if (YTCH && wave == 7) { u32 d = __builtin_amdgcn_raw_buffer_load_b32(rws, f >= 40 ? 0x80000000u : lane < 54 ? WS_YP + (f * 56 + lane) * 128 : WS_GR + f * 128, 0, 16); PIN(d); }
      __builtin_amdgcn_s_sleep(KEY_DELAY - (YTCH ? 16 : 0));
      if (YTCH && wave == 6) __builtin_amdgcn_s_sleep(16);
#pragma nounroll
      for (u32 spin = 0;; ++spin) {
        FENCE();
        k = bld128_sc1(rws, WS_KEY + min(lane, 55) * 128);
        if (__all(k.y == tag && k.w == tag) || spin > SPIN_CAP) break;
        __builtin_amdgcn_s_sleep(8);
      }
      u32 k1 = lane < 56 ? k.x : 0, k2 = lane < 56 ? k.z : 0;
      top2_wave(k1, k2);
      k.x = k1; k.z = k2;
      if (f == 55) bst128_sc1(rws, (lane | t * 8) < 8 ? WS_FIN + lane * 16 : 0x80000000u, k);
      const int tk = key_tok(__builtin_amdgcn_readfirstlane(t ? k2 : k1));
      xo = tk * 1280; so = WS_ST2 + ((tk & 1) * 1024 + (tk >> 1)) * 16;
    }
  }
  const rsrc_t rx = __builtin_amdgcn_make_buffer_rsrc(p.x.p, 0, S_ * D_ * 2, 0x00020000);
  const u4 xa = __builtin_amdgcn_raw_buffer_load_b128(rx, xo + lane * 16, 0, 0);
  const u32 xc = __builtin_amdgcn_raw_buffer_load_b32(rx, xo + 1024 + lane * 4, 0, 0);
  u4 sv = bld128_sc1(rws, so);
  FENCE();
  STAMP(2);
  NEW_PAGE();
  lds_bar();
  STAMP(3);
  bf8 Wg[5], Wu[5];
#pragma unroll
  for (int ks = 0; ks < 5; ++ks) { Wg[ks] = ld_tr8(A_G + ab + ks * 2048, 256); Wu[ks] = ld_tr8(A_U + ab + ks * 2048, 256); }
#pragma unroll
  for (int ks = 0; ks < 5; ++ks) { PIN(Wg[ks]); PIN(Wu[ks]); }
  bar();
  if (__builtin_expect(wave >= 6, 1)) {
    if (__builtin_expect(__any(__builtin_amdgcn_bitop3_b32(sv.y, sv.w, tag, 0x7e) != 0u), 0)) {
#pragma nounroll
      for (u32 spin = 0; !__all(sv.y == tag && sv.w == tag) && spin < SPIN_CAP; ++spin) {
        FENCE();
        __builtin_amdgcn_s_sleep(1);
        sv = bld128_sc1(rws, so);
      }
    }
    const float m = __uint_as_float(sv.x), r_ = __uint_as_float(sv.z);
    u4 o; o.x = norm2(xa.x, m, r_); o.y = norm2(xa.y, m, r_); o.z = norm2(xa.z, m, r_); o.w = norm2(xa.w, m, r_);
    st_u4(A_XN + t * 1280 + lane * 16, o); st_u(A_XN + t * 1280 + 1024 + lane * 4, norm2(xc, m, r_));
    lds_add(Y_CB, 1);
  }
  if (__builtin_expect(!ffn, 0)) {
    lds_add(Y_PUB, 1);
    return;
  }
  lds_wait_ge<1>(Y_CB, 2);
  STAMP(8);
  {
    bf8 xb8[5];
#pragma unroll
    for (int ks = 0; ks < 5; ++ks) xb8[ks] = ld_b128(xb + ks * 64);
    SB();
    f4 ag = {0.f, 0.f, 0.f, 0.f}, au = {0.f, 0.f, 0.f, 0.f};
#pragma unroll
    for (int ks = 0; ks < 5; ++ks) { ag = mfma(Wg[ks], xb8[ks], ag); au = mfma(Wu[ks], xb8[ks], au); }
    SB();
    st_f4(ar, ag); st_f4(ar + 128, au);
  }
  STAMP(5);
  lds_bar();
  STAMP(6);
  {
    float a[4], b[4];
#pragma unroll
    for (int k = 0; k < 4; ++k) a[k] = ld_f(as + k * 512);
#pragma unroll
    for (int k = 0; k < 4; ++k) b[k] = ld_f(as + k * 512 + 128);
    bf8 Wd[5], hb[5];
#pragma unroll
    for (int i = 0; i < 5; ++i) Wd[i] = ld_tr8(ad[i], 512);
    SB();
    const float hh = rbf(silu_f(rbf((a[0] + a[1]) + (a[2] + a[3])))) * rbf((b[0] + b[1]) + (b[2] + b[3]));
    *(LAS u16*)(uintptr_t)aw = (u16)(pk2(hh, hh) & 0xffffu);
    FENCE();
#pragma unroll
    for (int i = 0; i < 5; ++i) hb[i] = ld_b128(ah[i]);
    SB();
    f4 y = {0.f, 0.f, 0.f, 0.f};
#pragma unroll
    for (int i = 0; i < 5; ++i) y = mfma(Wd[i], hb[i], y);
    u4 v; v.x = pk2(y[0], y[1]); v.z = pk2(y[2], y[3]); v.y = v.w = tag;
    bst128_sc1(rws, yo, v);
  }
  STAMP(4);
  lds_add(Y_PUB, 1);
}

template <bool MAIN, int WE>
DEV void gemm2(const Params& p, u32 tag, int wave, G2& c STM_ARG) {
  const int m = wave & 3, kh = wave >> 2;
  c.template init<MAIN>(tag);
  const u32 b0 = c.b0, oo = c.oo, fo = c.fo, gb = c.gb, pa = c.pa;
  u32 pm = c.pm;
  u64 pm64 = pm;
  SPIN(pm64);
  u4 of[10];
#define LG "s_nop " STR(LGAP) "\n\t"
#define LOADS "buffer_load_dwordx4 v[124:127], %[oo], %[srd], 0 offen offset:0 sc1\n\t" LG \
              "buffer_load_dwordx4 v[128:131], %[oo], %[srd], 0 offen offset:1024 sc1\n\t" LG \
              "buffer_load_dwordx4 v[132:135], %[oo], %[srd], 0 offen offset:2048 sc1\n\t" LG \
              "buffer_load_dwordx4 v[136:139], %[oo], %[srd], 0 offen offset:3072 sc1\n\t" LG \
              "buffer_load_dwordx4 v[140:143], %[oo], %[srd], %[s4] offen offset:0 sc1\n\t" LG \
              "buffer_load_dwordx4 v[144:147], %[oo], %[srd], %[s4] offen offset:1024 sc1\n\t" LG \
              "buffer_load_dwordx4 v[148:151], %[oo], %[srd], %[s4] offen offset:2048 sc1\n\t" LG \
              "buffer_load_dwordx4 v[152:155], %[oo], %[srd], %[s4] offen offset:3072 sc1\n\t" LG \
              "buffer_load_dwordx4 v[156:159], %[oo], %[srd], %[s8] offen offset:0 sc1\n\t" LG \
              "buffer_load_dwordx4 v[160:163], %[oo], %[srd], %[s8] offen offset:1024 sc1\n\t" LG \
              ""
#define OFS(C) C "{v[124:127]}"(of[0]), C "{v[128:131]}"(of[1]), C "{v[132:135]}"(of[2]), C "{v[136:139]}"(of[3]), C "{v[140:143]}"(of[4]), C "{v[144:147]}"(of[5]), C "{v[148:151]}"(of[6]), C "{v[152:155]}"(of[7]), C "{v[156:159]}"(of[8]), C "{v[160:163]}"(of[9])
#define POLL(R) "v_mov_b32 " R ", 0\n\tbuffer_atomic_add " R ", %[fo], %[srd], 0 offen sc0\n\t"
#define CHK(R) "s_waitcnt vmcnt(2)\n\tv_cmp_ne_u32 vcc, %[tag], " R "\n\ts_cbranch_vccz 2f\n\t" POLL(R)
#define LSPIN(L, O, V) L ":\n\tds_read_b32 %[t], %[pa] offset:" O "\n\ts_sub_u32 %[n], %[n], 1\n\ts_cselect_b32 %[l], -1, 0\n\ts_waitcnt lgkmcnt(0)\n\t" \
                 "v_readfirstlane_b32 %[r], %[t]\n\ts_or_b32 %[r], %[r], %[l]\n\ts_and_b32 %[l], %[r], " V "\n\ts_cbranch_scc0 " L "b\n\t"
  static_assert(Y_WO == Y_PUB + 4, "");
  u32 n = SPIN_CAP << 5, got, t, l, sg = c.sg;
  asm volatile("" : OFS("="));
  bf8 W[WPRE][5];
  auto ldw = [&](int ks) {
#pragma unroll
    for (int i = 0; i < 5; ++i) W[ks][i] = ld_tr8(b0 + ks * 5120 + i * 32, 8 * 160);
  };
  bool early = false;
  if (MAIN) {
    if (WE) {
      early = __builtin_amdgcn_readfirstlane(lds_peek(pa + 4)) & 4;
#pragma unroll
      for (int ks = 0; ks < WE; ++ks)
#pragma unroll
        for (int i = 0; i < 5; ++i) asm volatile("" : "=v"(W[ks][i]));
      if (__builtin_expect(early, 1)) {
#pragma unroll
        for (int ks = 0; ks < WE; ++ks) ldw(ks);
      }
    }
    SB();
    __builtin_amdgcn_s_setprio(3);
    FST(13);
    if (FSTAMPS + 0 == 1) { wait_vm<0>(); FST(14); }
    asm volatile("s_mov_b64 exec, 1\n\ts_waitcnt vmcnt(0)\n\tds_add_rtn_u32 %0, %1, %2\n\ts_mov_b32 exec_lo, %6\n\ts_waitcnt lgkmcnt(0)\n\t"
                 "v_readfirstlane_b32 vcc_lo, %0\n\ts_cmp_lg_u32 vcc_lo, 7\n\ts_cbranch_scc1 1f\n\t"
                 "buffer_atomic_swap %3, %4, %5, 0 offen\n1:\n\ts_mov_b64 exec, -1"
                 : "=&v"(t) : "v"(pa), "v"(c.one), "v"(c.tv), "v"(c.po), "s"(srd(p.ws)), "s"(c.me) : "scc", "vcc", "memory");
    __builtin_amdgcn_s_setprio(0);
    STAMP(5);
    FST(12);
  }
  GST(14);
  if (!__builtin_expect(early, 1)) {
    lds_wait_bit<1>(Y_WO, 4);
    if (MAIN) {
#pragma unroll
      for (int ks = 0; ks < WE; ++ks) ldw(ks);
    }
  }
  GST(15);
#define POLL1(R) asm volatile("s_cmp_eq_u32 %[pm], 0\n\ts_cbranch_scc1 1f\n\ts_mov_b64 exec, %[pm64]\n\t" POLL(R) "s_mov_b64 exec, -1\n1:" \
               : "+{v[124:127]}"(of[0]) : [fo] "v"(fo), [srd] "s"(srd(p.ws)), [pm] "s"(pm), [pm64] "s"(pm64) : "scc", "memory")
#pragma unroll
  for (int ks = MAIN ? WE : 0; ks <= WPRE; ++ks) {
    SB();
    if (ks == (MAIN ? PM0 : PK0)) POLL1("v124");
    if (ks == (MAIN ? PM1 : PK1)) POLL1("v125");
    if (ks == (MAIN ? PM2 : PK2)) POLL1("v126");
    SB();
    if (ks < WPRE) ldw(ks);
  }
  SB();
  __builtin_amdgcn_s_setprio(IPRIO);
  asm volatile("s_cmp_eq_u32 %[pm], 0\n\ts_cbranch_scc1 5f\n\t"
               "s_mov_b64 exec, %[pm64]\n" LSPIN("3", "0", "8") "s_movk_i32 %[l], 0x4000\n"
               "1:\n\t" CHK("v124") CHK("v125") CHK("v126") "s_sub_u32 %[l], %[l], 1\n\ts_cbranch_scc0 1b\n"
               "2:\n\ts_mov_b64 exec, 1\n\tds_or_b32 %[pa], %[gb] offset:4\n\ts_mov_b64 exec, -1\n\t.if " STR(PDL) "\n\ts_sleep " STR(PDL) "\n\t.endif\n\ts_branch 4f\n"
               LSPIN("5", "4", "%[sg]") "4:\n\t" LOADS "s_min_u32 %[n], %[n], 0x100000\n" LSPIN("7", "4", "32")
               : OFS("+"), [n] "+s"(n), [l] "=&s"(l), [r] "=&s"(got), [t] "=&v"(t)
               : [fo] "v"(fo), [srd] "s"(srd(p.ws)), [tag] "s"(tag), [gb] "v"(gb), [pm] "s"(pm), [pm64] "s"(pm64),
                 [pa] "v"(pa), [oo] "v"(oo), [sg] "s"(sg), [s4] "s"(4096), [s8] "s"(8192) : "scc", "vcc", "memory");
  GST(0);
  STAMP(7);
  __builtin_amdgcn_s_setprio(MPRIO);
  u32 sfa = Y_S + m * 4, sfv = kh + 1;
  SPIN(sfa); SPIN(sfv);
  asm volatile("s_mov_b32 m0, %0" :: "s"(wave * ST_W));
  auto landed = [&](int ks) {
    if (ks == 0) asm volatile("s_waitcnt vmcnt(9)" : "+{v[124:127]}"(of[0]));
    if (ks == 1) asm volatile("s_waitcnt vmcnt(8)" : "+{v[128:131]}"(of[1]));
    if (ks == 2) asm volatile("s_waitcnt vmcnt(7)" : "+{v[132:135]}"(of[2]));
    if (ks == 3) asm volatile("s_waitcnt vmcnt(6)" : "+{v[136:139]}"(of[3]));
    if (ks == 4) asm volatile("s_waitcnt vmcnt(5)" : "+{v[140:143]}"(of[4]));
    if (ks == 5) asm volatile("s_waitcnt vmcnt(4)" : "+{v[144:147]}"(of[5]));
    if (ks == 6) asm volatile("s_waitcnt vmcnt(3)" : "+{v[148:151]}"(of[6]));
    if (ks == 7) asm volatile("s_waitcnt vmcnt(2)" : "+{v[152:155]}"(of[7]));
    if (ks == 8) asm volatile("s_waitcnt vmcnt(1)" : "+{v[156:159]}"(of[8]));
    if (ks == 9) asm volatile("s_waitcnt vmcnt(0)" : "+{v[160:163]}"(of[9]));
  };
  SB();
  u32 fa = sfa, fv = sfv;
  GST(1);
  f4 acc[5];
#pragma unroll
  for (int n = 0; n < 5; ++n) acc[n] = f4{0.f, 0.f, 0.f, 0.f};
  constexpr int NR = (10 - WPRE) * 10;
  u2 Wh[NR];
  auto kn = [](int i, int& ks, int& n) { if (i < 40) { ks = i / 5; n = i % 5; } else { ks = 8 + (i & 1); n = (i - 40) >> 1; } };
  auto rdh = [&](int i) { int ks, n; kn(WPRE * 5 + (i >> 1), ks, n); if (i >= 0 && i < NR) Wh[i] = ld_tr(b0 + ks * 5120 + n * 32 + (i & 1) * 1280); };
  auto put = [&](int j) { asm volatile("ds_write_addtid_b32 %0 offset:%1" :: "v"(acc[j >> 2][j & 3]), "i"((j >> 2) * ST_N + (j & 3) * 256) : "memory"); };
#pragma unroll
  for (int i = 0; i < 50; ++i) {
    int ks, n; kn(i, ks, n);
    if (n == 0) { landed(ks); SB(); }
    acc[n] = mfma(__builtin_bit_cast(bf8, of[ks]), i < WPRE * 5 ? W[ks][n] : mk8(Wh[2 * (i - WPRE * 5)], Wh[2 * (i - WPRE * 5) + 1]), acc[n]);
    const int j = i - RD0;
    rdh(j + j / RDX); if (j >= 0 && j % RDX == RDX - 1) rdh(j + j / RDX + 1);
    if (i == 41) PIN(fa);
    if (i == 42) { PIN(fv); PIN(Wh[NR - 1]); }
    SB();
    if (i >= 43) { put(2 * (i - 43)); put(2 * (i - 43) + 1); SB(); }
    if (!ARR && i % 5 == 4) GST(2 + i / 5);
  }
  put(14); put(15);
  asm volatile("s_nop 3");
#pragma unroll
  for (int j = 16; j < 20; ++j) put(j);
  lds_set(fa, fv);
  GST(12);
  lds_bar();
  GST(13);
}

DEV void reduce_ffn(const Params& p, rsrc_t rws, u32 tag, int lane, int f, u32 key STM_ARG) {
  const u32 tok = key_tok(key), so = WS_ST2 + ((tok & 1) * 1024 + (tok >> 1)) * 16;
  const u32 ya = WS_YP + f * (56 * 128) + lane * 16, yl = lane < 48 ? ya + 6144 : 0x80000000u, el = lane < 48 ? tag : 0u;
  const u2 xr = __builtin_amdgcn_raw_buffer_load_b64(__builtin_amdgcn_make_buffer_rsrc(p.x.p, 0, S_ * D_ * 2, 0x00020000), tok * 1280 + f * 32 + (lane & 3) * 8, 0, 0);
  const float sg = __builtin_amdgcn_rcpf(1.f + __expf(-key_r(key)));
  __builtin_amdgcn_s_setprio(3);
  if (RDLY) __builtin_amdgcn_s_sleep(RDLY);
  u4 w[8];
#pragma nounroll
  for (u32 spin = 0; spin < SPIN_CAP; ++spin) {
    FENCE();
    u32 b = 0;
#pragma unroll
    for (int j = 0; j < 8; ++j) w[j] = bld128_sc1(rws, j < 6 ? ya + j * 1024 : j == 6 ? yl : so);
#pragma unroll
    for (int j = 0; j < 8; ++j) b |= __builtin_amdgcn_bitop3_b32(w[j].y, w[j].w, j != 6 ? tag : el, 0x7e);
    if (!__any(b != 0u)) break;
    __builtin_amdgcn_s_sleep(2);
  }
  STAMP(6);
#define EACH(V, X) _Pragma("unroll") for (int e = 0; e < 4; ++e) { V[e] = X; PIN(V[e]); }
  auto up = [](u32 a, u32 b, int e) { const u32 v = e & 2 ? b : a; return e & 1 ? bfhi(v) : bflo(v); };
  float y[4], z[4];
  EACH(y, up(w[0].x, w[0].z, e))
#pragma unroll
  for (int j = 1; j < 7; ++j) { EACH(y, y[e] + up(w[j].x, w[j].z, e)) }
  EACH(y, y[e] + dppf<0x128>(y[e]))
  auto s16 = [](float v) { auto r = __builtin_amdgcn_permlane16_swap(__float_as_uint(v), __float_as_uint(v), false, false); return __uint_as_float(r[0]) + __uint_as_float(r[1]); };
  auto s32 = [](float v) { auto r = __builtin_amdgcn_permlane32_swap(__float_as_uint(v), __float_as_uint(v), false, false); return __uint_as_float(r[0]) + __uint_as_float(r[1]); };
  EACH(y, s16(y[e])) EACH(y, s32(y[e]))
  EACH(z, up(xr.x, xr.y, e) - __uint_as_float(w[7].x)) EACH(z, z[e] * __uint_as_float(w[7].z)) EACH(y, fmaf(y[e], sg, z[e]))
  EACH(z, y[e] * -1.4426950408889634f) EACH(z, __builtin_amdgcn_exp2f(z[e])) EACH(z, 1.f + z[e]) EACH(z, __builtin_amdgcn_rcpf(z[e])) EACH(y, y[e] * z[e])
  u4 v; v.x = pk2(y[0], y[1]); v.z = pk2(y[2], y[3]); v.y = v.w = tag;
  bst128_sc1(rws, lane < 8 ? WS_GR + f * 128 + lane * 16 : 0x80000000u, v);
  STAMP(2);
}

DEV void epilogue(const Params& p, int lane, int m, int mg, int ng, bool is_main, int f, Epi& ei STM_ARG) {
  const rsrc_t rws = __builtin_amdgcn_make_buffer_rsrc(p.ws.p, 0, WS_END, 0x00020000);
  float (&gf)[20] = ei.gf;
  f2 xf[10], g2[10];
#pragma unroll
  for (int i = is_main ? 0 : YG; i < (is_main ? 0 : YG2); ++i) ei.gate(i);
  SB();
  ei.wait<0>();
  lds_add(Y_WO, is_main ? 8 : 9);
  STAMP(5);
  const u32 tag = __builtin_amdgcn_readfirstlane(ei.tag);
  if (is_main && GW) lds_wait_ge<4>(Y_PUB, GW);
  if (is_main) ei.check(p.ws.p, tag);
  u4 fin = ei.fin;
  if (__builtin_expect(!(fin.y == tag && fin.w == tag), 0)) {
#pragma nounroll
    for (u32 spin = 0; !(fin.y == tag && fin.w == tag) && spin < SPIN_CAP; ++spin) {
      FENCE();
      __builtin_amdgcn_s_sleep(2);
      fin = bld128_sc1(rws, WS_FIN);
    }
  }
#pragma unroll
  for (int i = is_main ? 0 : YG2; i < 20; ++i) ei.gate(i);
  STAMP(10);
  if (!is_main && __builtin_expect(f < 40 && m == 3, 1)) reduce_ffn(p, rws, tag, lane, f, (lane & 4) ? fin.z : fin.x STM);
    if (EPM != 2 && !is_main && __builtin_expect(f == 55 && m == 3, 0)) {
      if (EPM == 3) lds_wait_ge<4>(Y_WO, 768);
#pragma nounroll
      for (u32 spin = 0;; ++spin) {
        FENCE();
        u32 bad = 0;
#pragma unroll
        for (int i = 0; i < 4; ++i) bad |= __builtin_amdgcn_raw_buffer_load_b32(rws, WS_FLAG + 128 + (i * 64 + lane) * 256, 0, 16) ^ tag;
        if (__all(bad == 0u) || spin > SPIN_CAP) break;
        __builtin_amdgcn_s_sleep(16);
      }
      if (EPM != 1 && lane == 0) gst32(p.ws.p + WS_EPOCH, tag);
    }
  const u32 row = Epi::row(lane), odd = ng & 1;
  u32 sadr[2];
  sadr[1] = m * ST_W + (row & 3) * 256 + (row >> 2) * 64;
  sadr[0] = sadr[1] + (odd + ((lane >> 1) & 1)) * ST_N + (lane & 1) * 32;
  sadr[1] += (odd ? 0 : 4 * ST_N) + (lane & 3) * 16;
  PIN(sadr[0]); PIN(sadr[1]);
  SB();
  {
    const u32 tok = ((ei.soff >> 3) & 2046) | ((ei.soff >> 14) & 1), c = key_tok(fin.z) == tok;
    static_assert(WS_ST2 % 32768 == 0, "");
    if (c || key_tok(fin.x) == tok) {
      const u32 a = WS_GR + ng * 640 + c * 64, a0 = a + (odd + ((lane >> 1) & 1)) * 128 + (lane & 1) * 32, a1 = a + (odd ? 0 : 512) + (lane & 3) * 16;
      u4 w[5];
      lds_wait_ge<2>(Y_WO, 256);
#pragma nounroll
      for (u32 spin = 0; spin < SPIN_CAP; ++spin) {
        FENCE();
        u32 b = 0;
#pragma unroll
        for (int j = 0; j < 5; ++j) w[j] = bld128_sc1(rws, j < 4 ? a0 + (j >> 1) * 256 + (j & 1) * 16 : a1);
#pragma unroll
        for (int j = 0; j < 5; ++j) b |= __builtin_amdgcn_bitop3_b32(w[j].y, w[j].w, tag, 0x7e);
        if (!__any(b != 0u)) break;
        __builtin_amdgcn_s_sleep(2);
      }
#pragma unroll
      for (int j = 0; j < 5; ++j) { gf[4 * j] = bflo(w[j].x); gf[4 * j + 1] = bfhi(w[j].x); gf[4 * j + 2] = bflo(w[j].z); gf[4 * j + 3] = bfhi(w[j].z); }
    }
  }
#pragma unroll
  for (int e = 0; e < 10; ++e) { xf[e] = f2{bflo(ei.xw(e)), bfhi(ei.xw(e))}; g2[e] = f2{gf[2 * e], gf[2 * e + 1]}; PIN(xf[e]); PIN(g2[e]); }
  STAMP(7);
  __builtin_amdgcn_s_setprio(LPRIO);
  u4 so = srd(p.out);
  u32 fa = Y_S + m * 4, first;
  SPIN(so); PIN(fa);
  GST(1);
  {
    u32 n = 1u << 20, t, l;
    asm volatile("1:\n\ts_sleep 2\n\tds_read_b32 %1, %4\n\ts_sub_u32 %0, %0, 1\n\ts_cselect_b32 %2, 0, 1\n\ts_waitcnt lgkmcnt(0)\n\t"
                 "v_readfirstlane_b32 %3, %1\n\ts_cmp_lt_u32 %3, %2\n\ts_cbranch_scc1 1b"
                 : "+s"(n), "=&v"(t), "=&s"(l), "=&s"(first) : "v"(fa) : "scc", "memory");
  }
  GST(2);
  f4 pa[5], pb[5];
  auto rd = [&](f4 (&d)[5], u32 a0, u32 a1) {
#pragma unroll
    for (int j = 0; j < 5; ++j) d[j] = ld_f4(j < 4 ? a0 + (j >> 1) * 2 * ST_N + (j & 1) * 16 : a1);
  };
  u32 o0 = (first - 1) * (4 * ST_W), o1 = 4 * ST_W - o0;
  SPIN(o0); SPIN(o1);
  rd(pa, sadr[0] + o0, sadr[1] + o0);
  SB();
  sadr[0] += o1; sadr[1] += o1;
  PIN(sadr[0]); PIN(sadr[1]);
#pragma unroll
  for (int j = 0; j < 5; ++j) PIN(pa[j]);
  GST(3);
  __builtin_amdgcn_s_setprio(3);
  bar();
  GST(4);
  rd(pb, sadr[0], sadr[1]);
  SB();
  auto fin2 = [&](int e) {
    const f4 s4 = pa[e >> 1] + pb[e >> 1];
    const f2 r = __builtin_elementwise_fma(f2{s4[2 * (e & 1)], s4[2 * (e & 1) + 1]}, g2[e], xf[e]);
    return pk2(r.x, r.y);
  };
  u4 o[2]; u2 oh;
#pragma unroll
  for (int ps = 0; ps < 2; ++ps) {
#pragma unroll
    for (int e = 0; e < 4; ++e) o[ps][e] = fin2(4 * ps + e);
    asm volatile("buffer_store_dwordx4 %0, %1, %2, 0 offen offset:%3 " OUTPOL :: "v"(o[ps]), "v"(ei.goff[0]), "s"(so), "i"(ps * 64) : "memory");
    SB();
  }
  oh.x = fin2(8); oh.y = fin2(9);
  asm volatile("buffer_store_dwordx2 %0, %1, %2, 0 offen " OUTPOL :: "v"(oh), "v"(ei.goff[1]), "s"(so), "v"(o[0]), "v"(o[1]) : "memory");
  if (EPM == 1 && !is_main && f == 55 && m == 3 && lane == 0) gst32(p.ws.p + WS_EPOCH, tag);
  GST(5);
  STAMP(8);
}

#define NTW 2
#define NTX 2
#define PUT_STAMPS(m) do {} while (0)
#define ROLE extern "C" __global__ __launch_bounds__(768, 3) void
#define PAGE __attribute__((aligned(4096)))
#define STAMP0 ""
#define STAMP_INIT() do {} while (0)

#define ROWS3(W, X0, X1) \
    "buffer_load_dwordx4 " W ", v3, s[8:11], s15 offen\n\tbuffer_load_dwordx4 " X0 ", v4, s[4:7], s15 offen\n\t" \
    "buffer_load_dwordx4 " X1 ", v5, s[4:7], s15 offen\n\ts_addk_i32 s15, 256\n\ts_cmpk_eq_u32 s15, 1280\n\ts_cselect_b32 s15, 0, s15\n\t"
ROLE PAGE fused_kernel(Params p) {
  asm volatile(
    STAMP0
    "s_load_dword s94, s[0:1], 0\n\t"
    "s_load_dword s95, s[0:1], 64\n\t"
    "v_readfirstlane_b32 s3, v0\n\t"
    "s_cmpk_ge_u32 s2, 200\n\t"
    "s_cbranch_scc1 3f\n\t"
    "s_cmpk_lt_u32 s3, 256\n\t"
    "s_cbranch_scc1 role_c4\n\t"
    "s_cmpk_lt_u32 s3, 512\n\t"
    "s_cbranch_scc1 role_c3\n\t"
    "s_setprio 3\n\t"
    "s_load_dwordx8 s[4:11], s[0:1], 0\n\t"
    "s_bfe_u32 s12, s3, 0x20006\n\t"
    "s_lshr_b32 s13, s2, 3\n\t"
    "s_mul_i32 s14, s13, 13\n\t"
    "s_lshr_b32 s14, s14, 6\n\t"
    "s_mul_i32 s15, s14, 5\n\t"
    "s_sub_u32 s13, s13, s15\n\t"
    "s_add_u32 s15, s13, s14\n\t"
    "s_sub_u32 s16, s15, 5\n\t"
    "s_min_u32 s15, s15, s16\n\t"
    "s_bitcmp1_b32 s15, 1\n\t"
    "s_cselect_b64 vcc, -1, 0\n\t"
    "s_and_b32 s16, s15, 1\n\t"
    "s_lshl_b32 s17, s2, 1\n\t"
    "s_and_b32 s17, s17, 12\n\t"
    "s_lshr_b32 s17, " STR(KOFF) ", s17\n\t"
    "s_and_b32 s17, s17, 6\n\t"
    "s_add_u32 s16, s16, s17\n\t"
    "s_bitcmp1_b32 s2, 1\n\t"
    "s_cselect_b32 s17, 5, 0\n\t"
    "s_add_u32 s14, s14, s17\n\t"
    "s_bitcmp1_b32 s2, 0\n\t"
    "s_cselect_b32 s17, 5, 0\n\t"
    "s_add_u32 s13, s13, s17\n\t"
    "s_mul_i32 s18, s14, 13\n\t"
    "s_add_u32 s18, s18, s12\n\t"
    "s_sub_u32 s18, s18, 1\n\t"
    "s_mulk_i32 s18, 20480\n\t"
    "s_add_u32 s19, s18, 81920\n\t"
    "s_add_u32 s20, s18, 163840\n\t"
    "s_add_u32 s21, s18, 245760\n\t"
    "s_min_u32 s21, s21, 2600960\n\t"
    "s_max_i32 s18, s18, 0\n\t"
    "s_cmp_lt_u32 s12, 2\n\t"
    "s_cselect_b64 s[22:23], -1, 0\n\t"
    "s_mov_b64 s[24:25], -1\n\t"
    "s_cmp_eq_u32 s12, 0\n\t"
    "s_cmov_b32 s24, 0xffffff00\n\t"
    "s_lshl_b32 s26, s12, 10\n\t"
    "s_add_u32 s26, s26, 116736\n\t"
    "s_mov_b32 s27, 122880\n\t"
    "s_bfe_u32 s17, s2, 0x10002\n\t"
    "s_mul_i32 s17, s17, 10\n\t"
    "s_add_u32 s17, s17, s16\n\t"
    "s_lshl3_add_u32 s28, s16, s12\n\t"
    "s_mulk_i32 s28, 240\n\t"
    "s_add_u32 s28, s28, s13\n\t"
    "s_lshl_b32 s29, s12, 2\n\t"
    "v_bfe_u32 v3, v0, 3, 3\n\t"
    "v_xor_b32 v4, s29, v0\n\t"
    "v_lshrrev_b32 v5, 1, v3\n\t"
    "v_bitop3_b32 v5, v4, v5, 7 bitop3:0x28\n\t"
    "v_and_b32 v6, 2, v3\n\t"
    "v_bitop3_b32 v6, v4, v6, 7 bitop3:0x28\n\t"
    "v_mad_u32_u24 v1, v3, 20, s17\n\t"
    "v_mad_u32_u24 v2, v3, 30, s28\n\t"
    "v_lshl_or_b32 v1, v1, 3, v5\n\t"
    "v_lshl_or_b32 v2, v2, 3, v6\n\t"
    "v_lshlrev_b32 v1, 4, v1\n\t"
    "v_lshlrev_b32 v2, 4, v2\n\t"
    "s_waitcnt lgkmcnt(0)\n\t"
    "s_cbranch_vccnz 5f\n"
    "4:\n\t"
    HALF_A("s26", "v1", "v2", "s[4:7]", "s[8:11]", "s18", "s21", "s[24:25]", "s[22:23]")
    "s_cbranch_vccnz role_ml\n"
    "5:\n\t"
    HALF_B("s26", "v1", "v2", "s[4:7]", "s[8:11]", "s19", "s20", "s27")
    "s_cbranch_vccnz 4b\n\t"
    "s_branch role_ml\n"
    "3:\n\t"
    "s_cmpk_ge_u32 s3, 512\n\t"
    "s_cbranch_scc1 role_al\n\t"
    "s_load_dwordx4 s[4:7], s[0:1], 0\n\t"
    "s_load_dwordx4 s[8:11], s[0:1], 64\n\t"
    "s_load_dwordx2 s[16:17], s[0:1], 32\n\t"
    "s_getpc_b64 s[12:13]\n"
    "2:\n\t"
    "s_lshr_b32 s3, s3, 6\n\t"
    "s_and_b32 s14, s2, -8\n\t"
    "s_add_u32 s14, s14, s3\n\t"
    "s_addk_i32 s14, -200\n\t"
    "s_mulk_i32 s14, 1280\n\t"
    "v_and_b32 v1, 63, v0\n\t"
    "v_min_u32 v2, 9, v1\n\t"
    "v_lshl_add_u32 v2, v2, 7, s14\n\t"
    "v_min_u32 v2, code_end - 2b, v2\n\t"
    "global_load_dword v167, v2, s[12:13]\n\t"
    "s_mul_i32 s3, s3, 7\n\t"
    "s_lshr_b32 s14, s2, 3\n\t"
    "s_add_u32 s3, s3, s14\n\t"
    "s_sub_u32 s3, s3, 25\n\t"
    "s_and_b32 s14, s2, 3\n\t"
    "s_lshl_b32 s14, s14, 5\n\t"
    "s_add_u32 s14, s14, s3\n\t"
    "s_bfe_u32 s15, s2, 0x10002\n\t"
    "s_lshl4_add_u32 s14, s14, s15\n\t"
    "s_mulk_i32 s14, 1280\n\t"
    "s_lshl_b32 s15, s2, 1\n\t"
    "s_and_b32 s15, s15, 12\n\t"
    "s_lshr_b32 s15, " STR(KOFF) ", s15\n\t"
    "s_and_b32 s15, s15, 6\n\t"
    "s_lshl_b32 s15, s15, 7\n\t"
    "s_addk_i32 s15, 256\n\t"
    "v_and_b32 v2, 3, v0\n\t"
    "v_and_b32 v3, 48, v0\n\t"
    "v_lshlrev_b32 v3, 2, v3\n\t"
    "v_lshl_or_b32 v3, v2, 4, v3\n\t"
    "v_bfe_u32 v2, v0, 2, 2\n\t"
    "v_mul_u32_u24 v2, 2560, v2\n\t"
    "v_add3_u32 v4, v2, v3, s14\n\t"
    "v_add_u32 v5, 10240, v4\n\t"
    "s_cmp_ge_u32 s3, 32\n\t"
    "s_waitcnt lgkmcnt(0)\n\t"
    "s_load_dword s94, s[16:17], 0\n\t"
    "s_cbranch_scc1 role_ac\n\t"
    ROWS3("v[100:103]", "v[104:107]", "v[108:111]") ROWS3("v[112:115]", "v[116:119]", "v[120:123]") ROWS3("v[124:127]", "v[128:131]", "v[132:135]")
    ROWS3("v[136:139]", "v[140:143]", "v[144:147]") ROWS3("v[148:151]", "v[152:155]", "v[156:159]")
    "s_branch role_ac"
    :: "s"(__builtin_amdgcn_kernarg_segment_ptr()), "s"(__builtin_amdgcn_workgroup_id_x()), "v"(__builtin_amdgcn_workitem_id_x())
    : "v167", "s99", "vcc", "scc", "memory");
}

struct Ids {
  int lane, wave, xcd, r, mg, ng, ra, rb, rt, j, a7, f, koff;
  DEV Ids() {
    lane = threadIdx.x & 63; wave = __builtin_amdgcn_readfirstlane(threadIdx.x >> 6);
    const int b = blockIdx.x;
    xcd = b & 7; r = b >> 3;
    mg = xcd * 4 + (r >> 3); ng = r & 7;
    ra = (r * 13) >> 6; rb = r - 5 * ra;
    rt = (xcd >> 1) * 5 + ra; j = (xcd & 1) * 5 + rb;
    a7 = r - 25;
    f = a7 < 6 ? 2 * (xcd + 8 * (a7 >> 1)) + (a7 & 1) : 48 + ((FPERM >> (4 * xcd)) & 7);
    koff = (KOFF >> ((xcd >> 1) * 4)) & 6;
  }
};

template <int NS>
DEV void main_wave(Params& p) {
  STAMP_INIT();
  const Ids id;
  const int lane = id.lane, wave = id.wave;
  const rsrc_t rws = __builtin_amdgcn_make_buffer_rsrc(p.ws.p, 0, WS_END, 0x00020000);
  u32 tag;
  G2 g2;
  main_compute<NS>(p, rws, tag, lane, wave, ((id.xcd >> 1) & 1) * 5 + id.ra, id.xcd >> 2, id.rb, id.j, id.mg, id.ng, g2 STM);
  gemm2<true, NS == 4 ? WE4 : WE3>(p, tag, wave, g2 STM);
  PUT_STAMPS(true);
}
ROLE role_ml(Params p) {
  LdIn in; in.take(); SB();
  STAMP_INIT();
  const Ids id;
  const int lane = id.lane, wave = id.wave;
  Epi ei;
  main_loader(p, in, lane, id.xcd >> 2, id.mg, id.ng, id.koff, ei STM);
  epilogue(p, lane, in.lw, id.mg, id.ng, true, 0, ei STM);
  PUT_STAMPS(true);
}
ROLE PAGE role_c3(Params p) { main_wave<3>(p); }
ROLE role_c4(Params p) { main_wave<4>(p); }
ROLE PAGE role_al(Params p) {
  STAMP_INIT();
  const Ids id;
  const int lane = id.lane, wave = id.wave, lw = wave - 8;
  Epi ei;
  aux_loader(p, lane, lw, id.a7, id.f, id.f < 54, id.mg, id.ng, id.koff, ei STM);
  epilogue(p, lane, lw, id.mg, id.ng, false, id.f, ei STM);
  PUT_STAMPS(false);
}
ROLE PAGE __attribute__((amdgpu_num_vgpr(160))) role_ac(Params p) {
  RowIn rin; rin.take(); SB();
  STAMP_INIT();
  const Ids id;
  const int lane = id.lane, wave = id.wave;
  const rsrc_t rws = __builtin_amdgcn_make_buffer_rsrc(p.ws.p, 0, WS_END, 0x00020000);
  u32 tag;
  aux_compute(p, rws, tag, lane, wave, id.f, id.f < 54, id.xcd, id.a7, rin STM);
  NEW_PAGE();
  G2 g2;
  g2.pre(wave, id.mg, id.ng);
  tag = __builtin_amdgcn_readfirstlane(tag);
  SPIN(tag);
  __builtin_amdgcn_s_waitcnt(0x0f70);
  gemm2<false, 0>(p, tag, wave, g2 STM);
  PUT_STAMPS(false);
}
ROLE code_end(Params p) {}

extern "C" void kernel_launch(void* const* d_in, const int* in_sizes, int n_in,
                              void* d_out, int out_size, void* d_ws, size_t ws_size,
                              hipStream_t stream) {
  auto buf = [](void* q, u32 bytes) { Buf r; r.p = (char*)q; r.bytes = bytes; r.flags = 0x00020000u; return r; };
  Params p;
  p.x = buf(d_in[0], S_ * D_ * 2); p.wqkv = buf(d_in[1], D_ * 3 * D_ * 2); p.wo = buf(d_in[2], D_ * D_ * 2); p.wr = buf(d_in[4], D_ * 2);
  p.wg = buf(d_in[5], D_ * F_ * 2); p.wu = buf(d_in[6], D_ * F_ * 2); p.wd = buf(d_in[7], F_ * D_ * 2);
  p.out = buf(d_out, S_ * D_ * 2); p.ws = buf(d_ws, WS_END);
  (void)hipFuncSetAttribute((const void*)fused_kernel, hipFuncAttributeMaxDynamicSharedMemorySize, LDS_BYTES);
  fused_kernel<<<256, 768, LDS_BYTES, stream>>>(p);
}
```

```cpp
#include <hip/hip_runtime.h>
#include <stdint.h>

typedef unsigned short u16;
typedef unsigned int u32;
typedef unsigned long long u64;
typedef __attribute__((ext_vector_type(4))) float f4;
typedef __attribute__((ext_vector_type(2))) float f2;
typedef __attribute__((ext_vector_type(4))) unsigned u4;
typedef __attribute__((ext_vector_type(2))) unsigned u2;
typedef __attribute__((ext_vector_type(8))) __bf16 bf8;
typedef __attribute__((ext_vector_type(2))) __bf16 bf2;
typedef __attribute__((ext_vector_type(4))) short s4;
#define LAS __attribute__((address_space(3)))
#define GAS __attribute__((address_space(1)))
#define DEV __device__ __forceinline__

constexpr int S_ = 2048, D_ = 640, F_ = 1728;

constexpr u32 WS_EPOCH = 0;
constexpr u32 WS_FIN = 256;
constexpr u32 WS_KEY = 24576;
constexpr u32 WS_GR = 40960;
constexpr u32 WS_YP = 8u << 20;
constexpr u32 WS_ST2 = 448u << 10;
constexpr u32 WS_FLAG = 512u << 10;
constexpr u32 WS_BOX = 640u << 10;
constexpr u32 WS_O = 1u << 20;
constexpr u32 WS_XS = 4u << 20;
constexpr u32 WS_END = WS_YP + 40 * 56 * 128;
constexpr u32 WS_STAMP = 16u << 20;

constexpr u32 SLOT = 38912;
constexpr u32 XB = 0, WB = 14336;
constexpr u32 MISC = 4 * SLOT;
constexpr u32 L_CS = MISC;
constexpr u32 L_SYNC = MISC + 768;
constexpr u32 L_MR = MISC + 1024;
constexpr u32 L_DUMP = MISC + 2048;
constexpr u32 L_Y = MISC + 3072;
constexpr u32 L_HV = MISC + 6144;
constexpr u32 LDS_BYTES = 163840;
constexpr u32 Y_Y = L_SYNC;
constexpr u32 Y_PUB = L_SYNC + 8;
constexpr u32 Y_WO = L_SYNC + 12;
constexpr u32 Y_CB = L_SYNC + 16;
constexpr u32 Y_ST = L_SYNC + 32;
constexpr u32 Y_S = L_SYNC + 128;
constexpr u32 Y_F = L_SYNC + 64;
constexpr u32 L_NONE = 0x30000;
constexpr u32 L_X = SLOT;
constexpr u32 L_HK = L_X + 21 * 512;
constexpr u32 L_CQ = L_HK + 2048;
constexpr u32 L_WO = L_CQ + 512;
constexpr u32 ST_N = 1040, ST_W = 5 * ST_N;
static_assert(L_WO + 103680 == MISC + 256 && 8 * ST_W <= L_WO, "lds map");

#define KD 41
constexpr int KEY_DELAY = KD;
constexpr int HELLO_DELAY = 4;
constexpr int POLL_SLEEP = 1;
#define WOA_ 14
#define WO7_ 5
constexpr int WO7 = WO7_, WOA = WOA_;
#define GSP 0
constexpr int GSPLIT = GSP;
#define OPRE_ 10
#define WPRE 6
#define RD0 5
#define RDX 6
#define STL 5
#define EBAR 1
#define WPR1 6
#define PGAP 2
#define LPRIO 2
#define IPRIO 2
#define BOXW 2
#define MPRIO 0
#define RDLY 0
#define KSLP 6
#define AWO 1
#define AWT 63
#define AW0 63
#define YG2 12
#define DLATE 0
#define GS2 0
#define NFD 0
#define PBR2 1
#define WEXP 0
#define WDLY 0
#define GWT 0
#define GW0 4
#define PWM 0x21
#define PR3 2
#define PR2 2
#define Q2I 2
#define Q2O 2
#define Q3I 2
#define Q3O 2
#define PFAIR 0
#define PK0 1
#define PK1 3
#define PK2 5
#define WOB 20
#define PPR 3
#define RPM 3
#define RPM4 2
#define KSC 1
#define QSC 0
#define PFT 0
#define PFL 0
#define LDP 0
#define STP 1
#define S8D 3
#define XST 1
#define EFT 1
#define WOF 22
#define YTCH 1
#define PCI 4
#define STR_(x) #x
#define STR(x) STR_(x)
#define LGAP 7
#define PDL 0
#define PC0 6
#define AWW 4
#define LPOLL 1
#define WOP 27
#define WLD 10
#define GW 6
#define C3E 1
#define CSD 2
#define LKP 2
#define WE4 4
#define WE3 2
#define PM0 WE
#define PM1 5
#define PM2 6
#define EPM 0
#define F3L 0
#define PTAB 0x14141400ull
#define WARM 0
#define WARM2 0
#define WARM3 0
#define WGAP 0
#define EW 0
#define EWM 14
#define EWG 0
#define OSLP 0
#define ARR 0
constexpr int OPRE = OPRE_;
#define PADC4 "1"
#define NOTOUCH
#define KOFF 0x6420
#define TX0 10
#define INIT1 1
#define TH0 2
#define TH1 3
#define FFN_DELAY 12
#define WOC 11
#define YG 0
#define SSLP 1
#define FPERM 0x54362107
#define OUTP 0
#define OUTPOL ""
#define PBRK 1
#define WOST 0
#define WOSTA 0
#define WTH 6
#define WTH2 4
#define PACE 0
#define PACE0 1
#define MORD 0
#define REVM 0xc
#define KORD 12
constexpr u64 KT0 = 0x9786453120, KT1 = 0x8975642301, KT2 = KT0, KT3 = KT1, KT4 = 0x9867534210;
#define SCAP 15
constexpr u32 SPIN_CAP = 1u << SCAP;

struct Buf { char* p; u32 bytes, flags; };
struct Params { Buf x, wqkv, ws, wo, wr, out, wg, wu, wd; };

DEV float bflo(u32 v) { return __uint_as_float(v << 16); }
DEV float bfhi(u32 v) { return __uint_as_float(v & 0xffff0000u); }
DEV u32 pk2(float a, float b) {
  bf2 r; r[0] = (__bf16)a; r[1] = (__bf16)b;
  return __builtin_bit_cast(u32, r);
}
DEV float rbf(float a) { return bflo(pk2(a, a) & 0xffffu); }
DEV u4 srd(const Buf& b) { return __builtin_bit_cast(u4, b); }
template <int N> DEV void wait_vm() { asm volatile("s_waitcnt vmcnt(%0)" :: "i"(N) : "memory"); }
template <int N> DEV void wait_bar() { asm volatile("s_waitcnt vmcnt(%0) lgkmcnt(0)\n\ts_barrier" :: "i"(N) : "memory"); }
DEV void lds_bar() { asm volatile("s_waitcnt lgkmcnt(0)\n\ts_barrier" ::: "memory"); }

DEV bf8 ld_b128(u32 a) { return *(const LAS bf8*)(uintptr_t)a; }
DEV u2 ld_u2(u32 a) { return *(const LAS u2*)(uintptr_t)a; }
DEV f4 ld_f4(u32 a) { return *(const LAS f4*)(uintptr_t)a; }
DEV float ld_f(u32 a) { return *(const LAS float*)(uintptr_t)a; }
DEV u32 ld_u(u32 a) { return *(const LAS u32*)(uintptr_t)a; }
DEV void st_u4(u32 a, u4 v) { *(LAS u4*)(uintptr_t)a = v; }
DEV void st_u2(u32 a, u2 v) { *(LAS u2*)(uintptr_t)a = v; }
DEV void st_f4(u32 a, f4 v) { *(LAS f4*)(uintptr_t)a = v; }
DEV void st_f(u32 a, float v) { *(LAS float*)(uintptr_t)a = v; }
DEV void st_u(u32 a, u32 v) { *(LAS u32*)(uintptr_t)a = v; }
DEV u2 ld_tr(u32 a) {
  return __builtin_bit_cast(u2, __builtin_amdgcn_ds_read_tr16_b64_v4i16((LAS s4*)(uintptr_t)a));
}
DEV bf8 mk8(u2 a, u2 b) { u4 r; r.x = a.x; r.y = a.y; r.z = b.x; r.w = b.y; return __builtin_bit_cast(bf8, r); }
DEV bf8 ld_tr8(u32 a, u32 d) { return mk8(ld_tr(a), ld_tr(a + d)); }
DEV f4 mfma(bf8 a, bf8 b, f4 c) { return __builtin_amdgcn_mfma_f32_16x16x32_bf16(a, b, c, 0, 0, 0); }
DEV f4 mfma16(u2 a, u2 b, f4 c) { return __builtin_amdgcn_mfma_f32_16x16x16bf16_1k(__builtin_bit_cast(s4, a), __builtin_bit_cast(s4, b), c, 0, 0, 0); }

#define PIN(x) asm volatile("" : "+v"(x))
#define SB() __builtin_amdgcn_sched_barrier(0)
#define RLX __ATOMIC_RELAXED, __HIP_MEMORY_SCOPE_AGENT
DEV u32 gld32(const char* p) { return __hip_atomic_load((const GAS u32*)p, RLX); }
DEV void gst64(char* p, u64 v) { __hip_atomic_store((GAS u64*)p, v, RLX); }
DEV void gst32(char* p, u32 v) { __hip_atomic_store((GAS u32*)p, v, RLX); }
typedef __amdgpu_buffer_rsrc_t rsrc_t;
extern "C" __device__ u4 raw_load(u4 srd, u32 voff, u32 soff, u32 aux) __asm("llvm.amdgcn.raw.buffer.load.v4i32");
extern "C" __device__ void raw_store(u4 v, u4 srd, u32 voff, u32 soff, u32 aux) __asm("llvm.amdgcn.raw.buffer.store.v4i32");
DEV u4 bld128_sc1(rsrc_t r, u32 off) { return __builtin_amdgcn_raw_buffer_load_b128(r, off, 0, 16); }
DEV void bst128_sc1(rsrc_t r, u32 off, u4 v) { __builtin_amdgcn_raw_buffer_store_b128(v, r, off, 0, 16); }

#define FSTAMPS 0
#define STAMP(i) do {} while (0)
#define CSTAMP(i) do {} while (0)
#define FST(i) do {} while (0)
#define GST(i) do {} while (0)
#define KST(i) do {} while (0)
#define PST(i) do {} while (0)
#define EST(i) do {} while (0)
#define STM_ARG
#define STM

template <int CTRL> DEV float dppf(float v) {
  return __uint_as_float(__builtin_amdgcn_update_dpp(0u, __float_as_uint(v), CTRL, 0xf, 0xf, true));
}
template <int CTRL> DEV u32 dppu(u32 v) { return __builtin_amdgcn_update_dpp(0u, v, CTRL, 0xf, 0xf, true); }
DEV float row_sum16(float v) {
  v += dppf<0xB1>(v);
  v += dppf<0x4E>(v);
  v += dppf<0x141>(v);
  v += dppf<0x140>(v);
  return v;
}
DEV void top2_merge(u32& a1, u32& a2, u32 b1, u32 b2) {
  u32 lo = min(a1, b1);
  a1 = max(a1, b1);
  a2 = max(lo, max(a2, b2));
}
DEV void top2_wave(u32& k1, u32& k2) {
  top2_merge(k1, k2, dppu<0xB1>(k1), dppu<0xB1>(k2));
  top2_merge(k1, k2, dppu<0x4E>(k1), dppu<0x4E>(k2));
  top2_merge(k1, k2, dppu<0x124>(k1), dppu<0x124>(k2));
  top2_merge(k1, k2, dppu<0x128>(k1), dppu<0x128>(k2));
  {
    auto r1 = __builtin_amdgcn_permlane16_swap(k1, k1, false, false);
    auto r2 = __builtin_amdgcn_permlane16_swap(k2, k2, false, false);
    k1 = r1[0]; k2 = r2[0]; top2_merge(k1, k2, r1[1], r2[1]);
  }
  {
    auto r1 = __builtin_amdgcn_permlane32_swap(k1, k1, false, false);
    auto r2 = __builtin_amdgcn_permlane32_swap(k2, k2, false, false);
    k1 = r1[0]; k2 = r2[0]; top2_merge(k1, k2, r1[1], r2[1]);
  }
}
DEV u32 mk_tag(u32 ep) {
  u32 tag = ep * 0x9E3779B1u + 0x7F4A7C15u;
  tag ^= tag >> 15; tag *= 0x85EBCA77u; tag ^= tag >> 13;
  return tag + (tag == ep);
}
DEV u32 epoch_get(const Params& p) { u32 e; asm volatile("s_load_dword %0, %1, 0" : "=s"(e) : "s"(p.ws.p)); return e; }
DEV u32 epoch_tag(u32 e) { asm volatile("s_waitcnt lgkmcnt(0)" : "+s"(e)); return mk_tag(e); }

DEV float silu_f(float v) { return v * __builtin_amdgcn_rcpf(1.f + __expf(-v)); }

DEV u32 key_tok(u32 k) { return ~k & 2047u; }
DEV float key_r(u32 k) {
  u32 o = k >> 16;
  u32 b = (o & 0x8000u) ? (o & 0x7fffu) : (~o & 0xffffu);
  return bflo(b);
}
DEV float vmax(float a, float b) { return __builtin_elementwise_maximum(a, b); }
DEV void bar() { asm volatile("s_barrier" ::: "memory"); }
#define NEW_PAGE() asm volatile("s_branch 1f\n\t.p2align 12\n1:" ::: "memory")
#define FENCE() asm volatile("" ::: "memory")
DEV u32 lds_peek(u32 a) { return __hip_atomic_load((LAS u32*)(uintptr_t)a, __ATOMIC_RELAXED, __HIP_MEMORY_SCOPE_WORKGROUP); }
DEV void lds_add(u32 a, u32 v) {
  asm volatile("s_mov_b64 exec, 1\n\tds_add_u32 %0, %1\n\ts_mov_b64 exec, -1" :: "v"(a), "v"(v) : "memory");
}
DEV void lds_set(u32 a, u32 v) {
  asm volatile("s_mov_b64 exec, 1\n\tds_write_b32 %0, %1\n\ts_mov_b64 exec, -1" :: "v"(a), "v"(v) : "memory");
}
template <int SLP> DEV void lds_wait_ge(u32 a, u32 v) {
  u32 n = 1u << 20, t, l, r;
  asm volatile(".if %6\n\ts_branch 2f\n\t.endif\n1:\n\t.if %6\n\ts_sleep %6\n\t.endif\n2:\n\t"
               "ds_read_b32 %1, %4\n\ts_sub_u32 %0, %0, 1\n\ts_cselect_b32 %2, 0, %5\n\ts_waitcnt lgkmcnt(0)\n\t"
               "v_readfirstlane_b32 %3, %1\n\ts_cmp_lt_u32 %3, %2\n\ts_cbranch_scc1 1b"
               : "+s"(n), "=&v"(t), "=&s"(l), "=&s"(r) : "v"(a), "s"(v), "i"(SLP) : "scc", "memory");
}
template <int SLP> DEV void lds_wait_bit(u32 a, u32 v) {
  u32 n = 1u << 20, t, l, r;
  asm volatile("s_branch 2f\n1:\n\ts_sleep %6\n2:\n\tds_read_b32 %1, %4\n\ts_sub_u32 %0, %0, 1\n\ts_cselect_b32 %2, -1, 0\n\ts_waitcnt lgkmcnt(0)\n\t"
               "v_readfirstlane_b32 %3, %1\n\ts_or_b32 %3, %3, %2\n\ts_and_b32 %3, %3, %5\n\ts_cbranch_scc0 1b"
               : "+s"(n), "=&v"(t), "=&s"(l), "=&s"(r) : "v"(a), "s"(v), "i"(SLP) : "scc", "memory");
}
DEV u32 norm2(u32 x, float m, float rs) {
  const u32 d = pk2(bflo(x) - m, bfhi(x) - m);
  return pk2(bflo(d) * rs, bfhi(d) * rs);
}

DEV float dot2(u32 a, u32 b, float c) { return __builtin_amdgcn_fdot2_f32_bf16(__builtin_bit_cast(bf2, a), __builtin_bit_cast(bf2, b), c, false); }
DEV float fsel(u32 m, float a, float b) {
  return __uint_as_float(__builtin_amdgcn_bitop3_b32(m, __float_as_uint(a), __float_as_uint(b), 0xCA));
}
struct RowIn {
  u4 x[2][5], w[5];
  DEV void take() {
    asm volatile("" : "={v[100:103]}"(w[0]), "={v[104:107]}"(x[0][0]), "={v[108:111]}"(x[1][0]), "={v[112:115]}"(w[1]), "={v[116:119]}"(x[0][1]), "={v[120:123]}"(x[1][1]),
                 "={v[124:127]}"(w[2]), "={v[128:131]}"(x[0][2]), "={v[132:135]}"(x[1][2]), "={v[136:139]}"(w[3]), "={v[140:143]}"(x[0][3]), "={v[144:147]}"(x[1][3]),
                 "={v[148:151]}"(w[4]), "={v[152:155]}"(x[0][4]), "={v[156:159]}"(x[1][4]));
  }
  DEV void wait(int i) {
    if (i == 0) asm volatile("s_waitcnt vmcnt(12)" : "+{v[100:103]}"(w[0]), "+{v[104:107]}"(x[0][0]), "+{v[108:111]}"(x[1][0]));
    if (i == 1) asm volatile("s_waitcnt vmcnt(9)" : "+{v[112:115]}"(w[1]), "+{v[116:119]}"(x[0][1]), "+{v[120:123]}"(x[1][1]));
    if (i == 2) asm volatile("s_waitcnt vmcnt(6)" : "+{v[124:127]}"(w[2]), "+{v[128:131]}"(x[0][2]), "+{v[132:135]}"(x[1][2]));
    if (i == 3) asm volatile("s_waitcnt vmcnt(3)" : "+{v[136:139]}"(w[3]), "+{v[140:143]}"(x[0][3]), "+{v[144:147]}"(x[1][3]));
    if (i == 4) asm volatile("s_waitcnt vmcnt(0)" : "+{v[148:151]}"(w[4]), "+{v[152:155]}"(x[0][4]), "+{v[156:159]}"(x[1][4]));
  }
  DEV void stats(const Params& p, rsrc_t rws, u32& tag, int lane, int s8, u32 slot STM_ARG) {
    const u32 ep = epoch_get(p);
    const u32 dq = ((lane >> 2) ^ (lane >> 4)) & 3;
    const int sidx = s8 + (lane >> 4) + dq * 2, tok = 2 * (sidx & 1023) + (sidx >> 10);
    u32 so = ((dq & 1) | (lane & 3)) ? 0x80000000u : WS_ST2 + sidx * 16, ko = lane ? L_NONE + lane * 16 : slot;
    u32 m1 = __builtin_amdgcn_sbfe(lane, 0, 1), m2 = __builtin_amdgcn_sbfe(lane, 1, 1), mz = __builtin_amdgcn_sbfe(dq, 1, 1), mv = ~__builtin_amdgcn_sbfe(dq, 0, 1);
    u4 on; on.x = on.y = on.z = on.w = 0x3f803f80u;
    PIN(so); PIN(ko); PIN(m1); PIN(m2); PIN(mz); PIN(mv); PIN(on);
    const bf8 ones = __builtin_bit_cast(bf8, on);
    const f4 z4 = {0.f, 0.f, 0.f, 0.f};
    f4 ax[2] = {z4, z4}, aq[2] = {z4, z4}, aw[2] = {z4, z4}, a1 = z4;
    SB();
#pragma unroll
    for (int i = 0; i < 5; ++i) {
      wait(i);
      const bf8 wv = __builtin_bit_cast(bf8, w[i]);
#pragma unroll
      for (int z = 0; z < 2; ++z) {
        const bf8 v = __builtin_bit_cast(bf8, x[z][i]);
        aq[z] = mfma(v, v, aq[z]); ax[z] = mfma(v, ones, ax[z]); aw[z] = mfma(v, wv, aw[z]);
      }
      a1 = mfma(wv, ones, a1);
      SB();
      PST(6 + i);
    }
    tag = epoch_tag(ep);
    PST(11);
    const float sw = (a1[0] + a1[1]) + (a1[2] + a1[3]);
    auto quad = [](float v) { v += dppf<0xB1>(v); v += dppf<0x4E>(v); return v; };
    auto diag = [&](f4 d) { return fsel(m2, fsel(m1, d[3], d[2]), fsel(m1, d[1], d[0])); };
    auto both = [&](float a, float b) { return fsel(mz, dppf<0x128>(b), a); };
    const float mean = fsel(mz, (ax[1][0] + ax[1][1]) + (ax[1][2] + ax[1][3]), (ax[0][0] + ax[0][1]) + (ax[0][2] + ax[0][3])) * (1.f / 640.f);
    const float sxw = quad(both(diag(aw[0]), diag(aw[1])));
    float var = fmaf(-mean, mean, quad(both(diag(aq[0]), diag(aq[1]))) * (1.f / 640.f));
    if (__builtin_expect(__any(mv && !(var * 64.f >= mean * mean)), 0)) {
      float q[2];
#pragma unroll
      for (int z = 0; z < 2; ++z) {
        f4 a = z4;
#pragma unroll
        for (int i = 0; i < 5; ++i) a = mfma(ones, __builtin_bit_cast(bf8, x[z][i]), a);
        const float mu = quad(a[0]) * (1.f / 640.f);
        float t = 0.f;
#pragma unroll
        for (int i = 0; i < 5; ++i)
#pragma unroll
          for (int e = 0; e < 4; ++e) { const float c = bflo(x[z][i][e]) - mu, d = bfhi(x[z][i][e]) - mu; t = fmaf(c, c, fmaf(d, d, t)); }
        t = quad(t);
        t += __shfl_xor(t, 16); t += __shfl_xor(t, 32);
        q[z] = t;
      }
      var = both(q[0], q[1]) * (1.f / 640.f);
    }
    const float mb = rbf(mean);
    const float sd = rbf(__builtin_amdgcn_sqrtf(rbf(rbf(var) + 1e-5f)));
    const float rs = __builtin_amdgcn_rcpf(sd);
    u4 st; st.x = __float_as_uint(mb); st.z = __float_as_uint(rs); st.y = st.w = tag;
    PST(12);
    bst128_sc1(rws, so, st);
    PST(13);
    const float rr = fmaf(-mb, sw, sxw) * rs;
    const u32 b = pk2(rr, rr) & 0xffffu;
    const u32 o = (b & 0x8000u) ? (~b & 0xffffu) : (b | 0x8000u);
    u32 k1 = ((o << 16) | (0xffffu - tok)) & mv, k2 = 0;
    top2_merge(k1, k2, dppu<0x128>(k1), 0);
    k1 = max(k1, dppu<0x124>(k1)); k2 = max(k2, dppu<0x124>(k2));
    {
      auto r1 = __builtin_amdgcn_permlane16_swap(k1, k1, false, false);
      auto r2 = __builtin_amdgcn_permlane16_swap(k2, k2, false, false);
      k1 = r1[0]; k2 = r2[0]; top2_merge(k1, k2, r1[1], r2[1]);
    }
    {
      auto r1 = __builtin_amdgcn_permlane32_swap(k1, k1, false, false);
      auto r2 = __builtin_amdgcn_permlane32_swap(k2, k2, false, false);
      k1 = r1[0]; k2 = r2[0]; top2_merge(k1, k2, r1[1], r2[1]);
    }
    u4 kk; kk.x = k1; kk.z = k2; kk.y = kk.w = tag;
    PST(14);
    st_u4(ko, kk);
  }
};

template <int N, int DM, int DS, int E60 = 0, int TH = 63> DEV void dma_run(u32 m0v, u32 voff, u4 srd, u32 so) {
  asm volatile("s_mov_b32 m0, %2\n\t"
               "s_mov_b32 exec_hi, %7\n\t"
               ".rept %4\n\t"
               ".if %7 == -1 || " STR(WEXP) " != 5\n\tbuffer_load_dwordx4 %1, %3, %0 offen lds\n\t.else\n\tbuffer_load_dwordx4 v[100:103], %1, %3, %0 offen\n\t.endif\n\t"
               "s_add_u32 m0, m0, %5\n\t"
               "s_add_u32 %0, %0, %6\n\t"
               ".if %8 < 63\n\ts_waitcnt vmcnt(%8)\n\t.endif\n\t"
               ".endr\n\t"
               "s_mov_b32 exec_hi, -1"
               : "+s"(so) : "v"(voff), "s"(m0v), "s"(srd), "i"(N), "i"(DM), "i"(DS), "i"(E60 ? 0x0fffffff : -1), "i"(TH) : "scc");
}

struct WoDma {
  u32 vo, vol, m, so, m0;
  DEV void init(int lane, int lw, int ng, int start, int last) {
    const int l60 = min(lane, 59);
    vo = (lw * 6 + l60 / 10) * 1280 + (l60 % 10) * 16 + ng * 160;
    if (WEXP == 1) vo = ng * 960 + lw * 7680 + l60 * 16;
    if (WEXP == 2) vo = 0x80000000u;
    vol = min(last * 6 + l60 / 10, 639) * 1280 + (l60 % 10) * 16 + ng * 160;
    m0 = L_WO + lw * 960; m = m0 + start * 3840; so = start * (24 * 1280);
    PIN(vo); PIN(vol);
    asm volatile("" : "+s"(m), "+s"(so), "+s"(m0));
  }
  template <int I0, int I1, int TH = 63, int SKIP = 0, int NL = 4> DEV void issue(u4 srd, int lw, u32 f = 0) {
    constexpr int E = I1 > 26 ? 26 : I1;
    if (E > I0)
      asm volatile(".if %7\n\ts_cmp_gt_u32 %8, 53\n\ts_cbranch_scc1 1f\n\t.endif\n\t"
                   "s_mov_b32 m0, %0\n\ts_mov_b32 exec_hi, 0x0fffffff\n\t"
                   ".rept %5\n\tbuffer_load_dwordx4 %2, %3, %1 offen lds\n\ts_addk_i32 m0, 960 * %9\n\ts_addk_i32 %1, 7680 * %9\n\t"
                   ".if %6 < 63\n\ts_waitcnt vmcnt(%6)\n\t.endif\n\t.endr\n\t"
                   "s_mov_b32 %0, m0\n\ts_mov_b32 exec_hi, -1\n1:"
                   : "+s"(m), "+s"(so) : "v"(vo), "s"(srd), "s"(m0), "i"(E > I0 ? (E - I0) * 4 / NL : 1), "i"(TH), "i"(SKIP), "s"(f), "i"(NL) : "scc");
    if (I1 > 26) dma_run<1, 0, 0, 1>(L_WO + lw * 960 + 26 * 3840, vol, srd, 0);
  }
};

struct Epi {
  u4 xv[2], sv, fin; u2 xh; u32 goff[2], soff, tag;
  float gf[20], c[4];
  static DEV u32 row(u32 lane) { return ((lane >> 1) & 12) | ((lane >> 2) & 1) | ((lane >> 4) & 2); }
  DEV void init(int lane, int m, int mg, int ng) {
    const int mt = mg * 4 + m, parity = mt >> 6;
    const u32 u = (mt & 63) * 16 + row(lane), o = (2 * u + parity) * 1280 + ng * 160;
    goff[0] = o + (ng & 1) * 32 + (lane & 3) * 16; goff[1] = o + ((ng & 1) ? 0 : 128) + (lane & 3) * 8;
    soff = WS_ST2 + (parity * 1024 + u) * 16;
    PIN(goff[0]); PIN(goff[1]); PIN(soff);
  }
  DEV void issue(u4 srdx, u4 srdws, u32 fo = WS_FIN) {
    asm volatile("buffer_load_dwordx4 %0, %1, %2, 0 offen sc1" : "=v"(sv) : "v"(soff), "s"(srdws));
    asm volatile("buffer_load_dwordx4 %0, %1, %2, 0 offen" : "=v"(xv[0]) : "v"(goff[0]), "s"(srdx));
    asm volatile("buffer_load_dwordx4 %0, %1, %2, 0 offen offset:64" : "=v"(xv[1]) : "v"(goff[0]), "s"(srdx));
    asm volatile("buffer_load_dwordx2 %0, %1, %2, 0 offen" : "=v"(xh) : "v"(goff[1]), "s"(srdx));
    asm volatile("buffer_load_dwordx4 %0, off, %1, %2 sc1" : "=v"(fin) : "s"(srdws), "s"(fo));
  }
  template <int N> DEV void wait() {
    asm volatile("s_waitcnt vmcnt(%5)" : "+v"(xv[0]), "+v"(xv[1]), "+v"(xh), "+v"(sv), "+v"(fin) : "i"(N) : "memory");
  }
  DEV void check(char* ws, u32 tag) {
    if (__builtin_expect(__any(__builtin_amdgcn_bitop3_b32(sv.y, sv.w, tag, 0x7e) != 0u), 0)) {
      const rsrc_t rws = __builtin_amdgcn_make_buffer_rsrc(ws, 0, WS_END, 0x00020000);
#pragma nounroll
      for (u32 spin = 0; !__all(sv.y == tag && sv.w == tag) && spin < SPIN_CAP; ++spin) {
        FENCE();
        sv = bld128_sc1(rws, soff);
      }
      __builtin_amdgcn_s_waitcnt(0x0f70);
    }
  }
  DEV u32 xw(int i) const { return i < 8 ? xv[i >> 2][i & 3] : xh[i & 1]; }
  DEV void gate(int i) {
    if (i == 0) {
      c[0] = __uint_as_float(sv.z); c[1] = -__uint_as_float(sv.x) * c[0];
      c[2] = c[0] * -1.4426950408889634f; c[3] = c[1] * -1.4426950408889634f;
    }
    float x = i & 1 ? bfhi(xw(i >> 1)) : bflo(xw(i >> 1));
    PIN(x);
    float z = fmaf(x, c[0], c[1]), t = fmaf(x, c[2], c[3]);
    PIN(z); PIN(t);
    gf[i] = z * __builtin_amdgcn_rcpf(1.f + __builtin_amdgcn_exp2f(t));
    PIN(gf[i]);
  }
};
#define SPIN(x) asm volatile("" : "+s"(x))
#define HALF_A(SB, VX, VW, SX, SW, SA, SC, EA, EC) \
    "s_add_u32 m0, " SB ", 12288\n\ts_mov_b64 exec, " EC "\n\ts_cbranch_execz 7f\n\tbuffer_load_dwordx4 " VX ", " SX ", " SC " offen lds\n7:\n\t" \
    "s_mov_b32 m0, " SB "\n\ts_mov_b64 exec, " EA "\n\tbuffer_load_dwordx4 " VX ", " SX ", " SA " offen lds\n\t" \
    "s_addk_i32 m0, 14336\n\ts_mov_b64 exec, -1\n\tbuffer_load_dwordx4 " VW ", " SW ", 0 offen lds\n\t" \
    "s_addk_i32 m0, 6912\n\ts_nop 0\n\tbuffer_load_dwordx4 " VW ", " SW ", 0 offen offset:1280 lds\n\t" \
    "s_addk_i32 m0, 6912\n\ts_nop 0\n\tbuffer_load_dwordx4 " VW ", " SW ", 0 offen offset:2560 lds\n\t"
#define HALF_B(SB, VX, VW, SX, SW, SB1, SB2, SH) \
    "s_add_u32 m0, " SB ", 4096\n\ts_nop 0\n\tbuffer_load_dwordx4 " VX ", " SX ", " SB1 " offen lds\n\t" \
    "s_addk_i32 m0, 4096\n\ts_nop 0\n\tbuffer_load_dwordx4 " VX ", " SX ", " SB2 " offen lds\n\t" \
    "s_addk_i32 m0, 10240\n\ts_nop 0\n\tbuffer_load_dwordx4 " VW ", " SW ", " SH " offen lds\n\t" \
    "s_addk_i32 m0, 6912\n\ts_nop 0\n\tbuffer_load_dwordx4 " VW ", " SW ", " SH " offen offset:1280 lds\n\t" \
    "s_addk_i32 m0, 6912\n\ts_nop 0\n\tbuffer_load_dwordx4 " VW ", " SW ", " SH " offen offset:2560 lds\n\t"
struct LdIn {
  u4 srdx, srdw; u32 vx, vw, lw, j, tp, cls, kb, sa, sb1, sb2, sc, sb, sh; u64 ec, ea;
  DEV void take() {
    asm volatile("" : "={s[4:7]}"(srdx), "={s[8:11]}"(srdw), "={v1}"(vx), "={v2}"(vw), "={s12}"(lw), "={s13}"(j), "={s14}"(tp), "={s15}"(cls), "={s16}"(kb),
                 "={s18}"(sa), "={s19}"(sb1), "={s20}"(sb2), "={s21}"(sc), "={s[22:23]}"(ec), "={s[24:25]}"(ea), "={s26}"(sb), "={s27}"(sh));
  }
};
DEV void main_loader(Params& p, LdIn& in, int lane, int parity, int mg, int ng, u32 koff, Epi& ei STM_ARG) {
  const int lw = in.lw, tp = in.tp, j = in.j, rt = parity * 10 + tp, u0 = tp * 104 - 8;
  const u4 srdx = in.srdx;
  u64 ktab = KT0;
  ktab = in.cls == 1 ? KT1 : ktab; SPIN(ktab); ktab = in.cls == 2 ? KT2 : ktab; SPIN(ktab); ktab = in.cls == 3 ? KT3 : ktab; SPIN(ktab); ktab = in.cls == 4 ? KT4 : ktab;
  auto next = [&]() {
    ktab >>= 4;
    u32 kb = ((u32)ktab & 15u) + koff;
    kb = min(kb, kb - 10u);
    const int d = kb - in.kb;
    in.kb = kb;
    in.vx += d * 128; in.vw += d * (64 * 3840);
    in.sb = in.sb >= 3 * SLOT ? in.sb - 3 * SLOT : in.sb + SLOT;
    PIN(in.vx); PIN(in.vw); SPIN(in.sb);
  };
  auto issue = [&]() {
    asm volatile("s_bitcmp1_b32 %12, 1\n\ts_cselect_b64 vcc, -1, 0\n\ts_cbranch_vccnz 5f\n4:\n\t"
                 HALF_A("%0", "%1", "%2", "%3", "%4", "%5", "%6", "%7", "%8") "s_cbranch_vccnz 6f\n5:\n\t"
                 HALF_B("%0", "%1", "%2", "%3", "%4", "%9", "%10", "%11") "s_cbranch_vccnz 4b\n6:"
                 :: "s"(in.sb), "v"(in.vx), "v"(in.vw), "s"(in.srdx), "s"(in.srdw), "s"(in.sa), "s"(in.sc), "s"(in.ea), "s"(in.ec),
                    "s"(in.sb1), "s"(in.sb2), "s"(in.sh), "s"(in.cls) : "scc", "vcc");
    next();
  };
#define STAGE_BAR(E) asm volatile("s_cmp_lt_u32 %0, 2\n\ts_cbranch_scc1 1f\n\ts_waitcnt vmcnt(%1)\n1:\n\ts_waitcnt vmcnt(%2)\n\ts_barrier" :: "s"(lw), "i"(9 + (E)), "i"(10 + (E)) : "scc", "memory")
  next();
  u32 ep = epoch_get(p);
  const u4 srdws = srd(p.ws);
  const int srow = lw * 28 + min(lane, 27);
  u32 go1 = WS_ST2 + (parity * 1024 + min(max(u0 + srow, 0), 1023)) * 16;
  u32 wa = lw < 2 ? (XST ? 0u : 0x80000000u) + WS_XS + ((rt * 10 + j) * 14 + (lw ? 14 : -7)) * 512 + min(lane, 27) * 128 : (BOXW == 0 || (BOXW == 2 && lane) ? 0x80000000u : WS_BOX + (mg * 8 + ng) * 256 + (lw & 1) * 64);
  u32 ho = (lane | (lw ^ 3)) ? 0x80000000u : WS_FLAG + (rt * 10 + j) * 256 + 128;
  PIN(go1); PIN(wa); PIN(ho);
  WoDma wo; wo.init(lane, lw, ng, 0, lw == 2 ? 52 : 106);
  ei.init(lane, lw, mg, ng);
  SB();
  PST(4);
  issue();
  u32 tagv = epoch_tag(ep);
  PIN(tagv);
  STAMP(9);
  PST(5);
  STAGE_BAR(0);
  PST(7);
  STAMP(2);
  asm volatile("buffer_store_dword %0, %1, %2, 0 offen sc1" :: "v"(tagv), "v"(ho), "s"(srdws));
#pragma unroll 1
  for (int s = 2; s < 9; ++s) {
    issue();
    STAGE_BAR(0);
  }
  EST(2);
  u4 g1;
  auto stats = [&]() {
    asm volatile("buffer_load_dwordx4 %0, %1, %2, 0 offen sc1" : "=v"(g1) : "v"(go1), "s"(srdws));
    asm volatile("s_mov_b32 m0, %0\n\ts_mov_b64 exec, 0x0fffffff\n\tbuffer_load_dword %1, %2, 0 offen sc1 lds\n\t"
                 "s_add_u32 m0, m0, 504\n\ts_nop 0\n\tbuffer_load_dword %1, %2, 0 offen offset:8 sc1 lds\n\ts_mov_b64 exec, -1"
                 :: "s"(L_MR + lw * 112), "v"(go1), "s"(srdws) : "scc");
  };
  if (STP >= 2) {
    if (STP == 2) stats();
    asm volatile("s_bitcmp1_b32 %12, 1\n\ts_cselect_b64 vcc, -1, 0\n\ts_cbranch_vccnz 5f\n4:\n\t"
                 HALF_A("%0", "%1", "%2", "%3", "%4", "%5", "%6", "%7", "%8") "s_cbranch_vccnz 6f\n\t"
                 "s_cmp_lt_u32 %13, 2\n\ts_cbranch_scc1 1f\n\ts_waitcnt vmcnt(%14 + 4)\n1:\n\ts_waitcnt vmcnt(%14 + 5)\n\ts_barrier\n5:\n\t"
                 HALF_B("%0", "%1", "%2", "%3", "%4", "%9", "%10", "%11") "s_cbranch_vccz 6f\n\ts_waitcnt vmcnt(%14 + 5)\n\ts_barrier\n\ts_branch 4b\n6:"
                 :: "s"(in.sb), "v"(in.vx), "v"(in.vw), "s"(in.srdx), "s"(in.srdw), "s"(in.sa), "s"(in.sc), "s"(in.ea), "s"(in.ec),
                    "s"(in.sb1), "s"(in.sb2), "s"(in.sh), "s"(in.cls), "s"(lw), "i"(STP == 2 ? 3 : 0) : "scc", "vcc", "memory");
    EST(4);
    if (STP == 3) stats();
  } else {
  issue();
  if (STP == 0) stats();
  EST(3);
  STAGE_BAR(STP == 0 ? 3 : 0);
  EST(4);
  STAMP(1);
  if (STP == 1) stats();
  }
  const u4 srdo = srd(p.wo);
  asm volatile("s_mov_b32 m0, %0\n\ts_cmp_lt_u32 %3, 2\n\ts_cbranch_scc1 1f\n\tbuffer_atomic_add %4, %1, %2, 0 offen\n\ts_branch 2f\n"
               "1:\n\tbuffer_load_dword %1, %2, 0 offen sc1 lds\n2:" :: "s"(L_DUMP + lw * 256), "v"(wa), "s"(srdws), "s"(lw), "v"(0u) : "scc");
  wo.issue<0, WO7>(srdo, lw);
  EST(5);
  asm volatile("s_waitcnt vmcnt(%1)" : "+v"(g1) : "i"(WO7 + 3 + (STP == 2 ? 9 : 0)) : "memory");
  EST(7);
  if (__builtin_expect(__any(__builtin_amdgcn_bitop3_b32(g1.y, g1.w, tagv, 0x7e) != 0u), 0)) {
    const rsrc_t rws = __builtin_amdgcn_make_buffer_rsrc(p.ws.p, 0, WS_END, 0x00020000);
#pragma nounroll
    for (u32 spin = 0; spin < SPIN_CAP; ++spin) {
      FENCE();
      g1 = bld128_sc1(rws, go1);
      if (__all(g1.y == tagv && g1.w == tagv)) break;
      __builtin_amdgcn_s_sleep(2);
    }
    __builtin_amdgcn_s_waitcnt(0x0f70);
    st_u(L_MR + srow * 4, g1.x); st_u(L_MR + 512 + srow * 4, g1.z);
  }
  __builtin_amdgcn_s_setprio(LDP);
  if (FSTAMPS + 0 == 6) { wait_vm<WO7 + 1>(); EST(8); }
  wait_bar<WO7 + 1>();
  EST(6);
  STAMP(3);
  if (WDLY) __builtin_amdgcn_s_sleep(WDLY);
  static_assert((32 - 4 * WO7) % 2 == 0, "");
  ei.tag = tagv;
  if (lw < 2) {
    dma_run<16 - 2 * WO7, 1920, 12 * 1280, 1, WTH>(L_WO + (4 * WO7 + lw) * 960, wo.vo, srdo, 4 * WO7 * 7680);
    dma_run<17, 1920, 12 * 1280, 1, WTH>(L_WO + (53 + lw) * 960, wo.vo, srdo, 53 * 7680);
  }
  ei.issue(srdx, srdws);
  ei.wait<5>();
  lds_add(Y_WO, 1);
  STAMP(6);
  ei.wait<0>();
  if (lw >= 2) {
    __builtin_amdgcn_s_sleep(WLD);
    dma_run<20, 960, 7680, 1, WTH2>(L_WO + (lw == 2 ? 32 : 86) * 960, wo.vo, srdo, (lw == 2 ? 30 : 83) * 7680);
    dma_run<1, 0, 0, 1>(L_WO + (lw == 2 ? 52 : 106) * 960, wo.vol, srdo, 0);
  }
  STAMP(4);
}

DEV int fresh_lane() {
  int l;
  asm volatile("v_mbcnt_lo_u32_b32 %0, -1, 0\n\tv_mbcnt_hi_u32_b32 %0, -1, %0" : "=v"(l));
  __builtin_assume(l >= 0 && l < 64);
  return l;
}
struct G2 {
  u32 sb, so, sf, sg, pm, mb, me, mo;
  u32 b0, oo, fo, gb, one, pa, po, tv;
  DEV void pre(int wave, int mg, int ng) {
    const int m = wave & 3, kh = wave >> 2;
    sb = L_WO + kh * 320 * 160; so = WS_O + (mg * 4 + m) * 20480 + kh * 10240; sf = WS_BOX + (mg * 8 + ng) * 256 + kh * 64; sg = 256u << kh;
    pm = ((0x4b5au >> (mg & 15)) & 1) * 0x3e0u + 0x1fu;
    SPIN(pm);
    pm *= (PWM >> wave) & 1;
    SPIN(sb); SPIN(so); SPIN(sf); SPIN(sg); SPIN(pm);
  }
  DEV void pub(u32 tp, u32 parity, u32 j, u32 rb) {
    const u32 r0 = tp * 104;
    mb = WS_BOX + (parity * 16 + (r0 >> 6)) * 2048 + (j >= 5 ? 64 : 0) + rb * 4; mo = (r0 & 63) ? 20 : 0;
    me = ((0x5au >> tp) & 1) ? 0xffffffu : 0xffffu;
    SPIN(mb); SPIN(mo); SPIN(me);
  }
  template <bool MAIN> DEV void init(u32 tag) {
    const u32 lane = fresh_lane();
    b0 = sb + (((lane >> 1) & 16) | ((lane >> 2) & 7)) * 160 + (lane & 3) * 8;
    pa = Y_PUB;
    PIN(b0); PIN(pa);
    SB();
    oo = so + lane * 16; fo = sf + lane * 4; gb = sg;
    PIN(oo); PIN(fo); PIN(gb);
    if (MAIN) { po = mb + lane * 256 + (mo & (((int)lane - 8) >> 31)); tv = tag; one = 1; PIN(po); PIN(tv); PIN(one); }
  }
};

constexpr u64 sel_mask(int r) {
  u64 m = 0;
  for (int l = 0; l < 64; ++l) {
    const int i = l & 15, c = 4 * ((l >> 4) & 1) + r;
    if (i < 8 ? c <= i : c > i - 8) m |= 1ull << l;
  }
  return m;
}
constexpr u64 SEL0 = sel_mask(0), SEL1 = sel_mask(1), SEL2 = sel_mask(2), SEL3 = sel_mask(3);
#define LM(m) __builtin_amdgcn_inverse_ballot_w64(m)
DEV u2 and2(u2 a, u2 m) { u2 r; r.x = a.x & m.x; r.y = a.y & m.y; return r; }

struct Att {
  u32 aA0[2], aA1[2], aB[2];
  u32 mOwn, mRot, mV, mLo, so[2], xg, aXw, aXr, aCS, aCV, aST, aMR, aH, aF;
  u4 mS;
  DEV void init_k(u32 ln, u32 n) {
    const u32 a0 = XB + ((ln & 15) << 7) + ((((ln >> 1) & 7) ^ (ln >> 4)) << 4);
    const u32 b0 = WB + ((ln & 48) << 6) + ((ln & 12) << 5) + (((2 * n + ((ln >> 1) & 1)) ^ ((ln >> 2) & 6)) << 4) + ((ln & 1) << 3);
#pragma unroll
    for (int s = 0; s < 2; ++s) {
      aA0[s] = a0 + s * 2 * SLOT; aA1[s] = (a0 ^ 64) + s * 2 * SLOT; aB[s] = b0 + s * 2 * SLOT;
      PIN(aA0[s]); PIN(aA1[s]); PIN(aB[s]);
    }
  }
  template <int NS> DEV void init(u32 ln, int n, int tau, bool edge, int tp, int parity, int j) {
    constexpr int s0 = NS == 4 ? 0 : 4;
    constexpr u32 SGN = 0x80000000u;
    const u32 i16 = ln & 15, g16 = ln & 48, h32 = ln >> 5, i4 = i16 * 4;
    mLo = ~(u32)__builtin_amdgcn_sbfe((int)ln, 3, 1);
    const u32 gt = (u32)(((int)g16 - 16 * tau) >> 31);
    mOwn = mLo & gt; mRot = ~mLo & ~gt;
    mV = (u32)(((int)i16 - 4 * tau) >> 31);
    u32 l0 = (u32)SEL0, l1 = (u32)SEL1, l2 = (u32)SEL2, l3 = (u32)SEL3;
    SPIN(l0); SPIN(l1); SPIN(l2); SPIN(l3);
    mS.x = __builtin_amdgcn_sbfe(l0, ln, 1); mS.y = __builtin_amdgcn_sbfe(l1, ln, 1); mS.z = __builtin_amdgcn_sbfe(l2, ln, 1); mS.w = __builtin_amdgcn_sbfe(l3, ln, 1);
    PIN(mOwn); PIN(mRot); PIN(mV); PIN(mLo); PIN(mS);
    const u32 up = tp * 104 + 8 + s0 * 16 + h32 * 16 + i16;
    const u32 hs = ln << 26;
    so[0] = WS_O + (parity * 64 - 1) * 20480 + (2 * j + (n >> 1)) * 1024 + (n & 1) * 512 + (up >> 4) * 20480 + (((ln & 16) | (up & 15)) << 4);
    so[1] = so[0] + 2 * 20480;
    if (NS == 4) so[0] |= ~hs & mLo & SGN;
    else so[1] |= (hs | (tp == 9 ? SGN : 0u)) & SGN;
    PIN(so[0]); PIN(so[1]);
    const int xl = n > 0 ? L_X + ((n - 1) * 7 + s0) * 512 : L_NONE, xh = n < 3 ? L_X + (n * 7 + s0) * 512 : L_NONE;
    aXr = xl + (int)h32 * (xh - xl) + ln * 4;
    aXw = aXr ^ 128;
    aCS = L_CQ + (n >> 1) * 256 + (n & 1) * 32 + g16 * 4;
    aCV = L_CS + 512 + n * 64 + i4;
    if (NS == 4) aCV += min(g16, 1u) * 0x10000;
    aF = Y_F + s0 * 4 + n * 4 + ln * 4 + min(ln, 1u) * 0x10000;
    aST = L_MR + s0 * 64 + i4;
    aMR = L_MR + s0 * 64 + g16;
    aH = n * 512 + ln * 8;
    const u32 xa = WS_XS + (((parity * 10 + tp) * 10 + j) * 14 + (n ? 7 : 0) + s0) * 512 + (edge ? 0u : SGN);
    xg = (xa + (ln & 31) * 16) | ((hs ^ (n ? SGN : 0u)) & SGN);
    PIN(aXw); PIN(aXr); if (NS == 3) PIN(aCS); PIN(aCV); PIN(aST); PIN(aMR); PIN(aH); PIN(aF); PIN(xg);
  }
};

template <int NS>
DEV void main_compute(const Params& p, rsrc_t rws, u32& tag, int lane, int wave, int tp, int parity, int rb, int j, int mg, int ng, G2& g2 STM_ARG) {
  constexpr int s0 = NS == 4 ? 0 : 4;
  const int n = (wave & 3) ^ (NS == 3);
  const int rt = parity * 10 + tp;
  u32 ep = epoch_get(p);
  Att at;
  at.init_k(lane, n);
  if (NS == 4) st_u(lane < 32 ? wave * SLOT + XB + lane * 4 : L_SYNC + wave * 64 + (lane & 15) * 4, lane < 32 ? 0xbf80bf80u : 0u);
  int tau = n - rb; tau += tau < 0 ? 5 : 0;
  const bool edge = ((0x0f00001e >> (n * 8 + tau)) & 1) != 0;
  SB();
  PST(2);
  if (INIT1) at.template init<NS>(lane, n, tau, edge, tp, parity, j);
  SB();
  PST(3);
  tag = epoch_tag(ep);
  const u32 (&aA0)[2] = at.aA0, (&aA1)[2] = at.aA1, (&aB)[2] = at.aB, (&so)[2] = at.so;
  const u32 &mOwn = at.mOwn, &mRot = at.mRot, &mV = at.mV, &mLo = at.mLo, &xg = at.xg, &aXw = at.aXw, &aXr = at.aXr, &aCS = at.aCS, &aCV = at.aCV,
            &aST = at.aST, &aMR = at.aMR, &aH = at.aH, &aF = at.aF;
  const u4& mS = at.mS;
  f4 acc[NS][3];
  u2 m0, mm, mp;
#pragma unroll
  for (int a = 0; a < NS; ++a)
#pragma unroll
    for (int b = 0; b < 3; ++b) acc[a][b] = f4{0.f, 0.f, 0.f, 0.f};
  bf8 A[3][NS], B[3][3];
  auto ldf = [&](int buf, int st, int kk) {
    const int rs = (st + 3) & 3, h = rs >> 1;
    const u32 of = (rs & 1) * SLOT;
    B[buf][0] = ld_tr8(aB[h] + of + kk * 4096, 512);
    A[buf][0] = ld_b128((kk ? aA1[h] : aA0[h]) + of + s0 * 2048);
    B[buf][1] = ld_tr8(aB[h] + of + kk * 4096 + 8192, 512);
    B[buf][2] = ld_tr8(aB[h] + of + kk * 4096 + 16384, 512);
#pragma unroll
    for (int a = 1; a < NS; ++a) A[buf][a] = ld_b128((kk ? aA1[h] : aA0[h]) + of + (s0 + a) * 2048);
  };
  auto mma = [&](int buf, int b0, int b1) {
    if (MORD == 1) {
#pragma unroll
      for (int b = b0; b < b1; ++b)
#pragma unroll
        for (int i = 0; i < NS; ++i) {
          const int a = (b & 1) ? NS - 1 - i : i;
          acc[a][b] = b < 2 ? mfma(B[buf][b], A[buf][a], acc[a][b]) : mfma(A[buf][a], B[buf][b], acc[a][b]);
        }
    } else if (MORD == 2) {
#pragma unroll
      for (int a = 0; a < NS; ++a)
#pragma unroll
        for (int i = 0; i < 2; ++i) { const int b = (a & 1) ? 1 - i : i; acc[a][b] = mfma(B[buf][b], A[buf][a], acc[a][b]); }
      if (b1 == 3) {
#pragma unroll
        for (int a = 0; a < NS; ++a) acc[a][2] = mfma(A[buf][a], B[buf][2], acc[a][2]);
      }
    } else {
#pragma unroll
    for (int a = 0; a < NS; ++a)
#pragma unroll
      for (int b = b0; b < b1; ++b)
        acc[a][b] = b < 2 ? mfma(B[buf][b], A[buf][a], acc[a][b]) : mfma(A[buf][a], B[buf][b], acc[a][b]);
    }
  };
  auto pattern = [&]() {
#pragma unroll
    for (int i = 0; i < NS + 6; ++i) {
      if (i < 3 * NS) __builtin_amdgcn_sched_group_barrier(0x008, 1, 0);
      __builtin_amdgcn_sched_group_barrier(0x100, 1, 0);
    }
    if (3 * NS > NS + 6) __builtin_amdgcn_sched_group_barrier(0x008, 2 * NS - 6, 0);
  };

  u32 inner = (n + 1) & 2, csm0 = L_CQ + (n >> 1) * 256 + (n & 1) * 32;
  const u4 sws = srd(p.ws);
  u32 eg = 0x0f00001eu & (1u << (n * 8 + tau));
  SPIN(eg);
  auto isedge = [&]() { SPIN(eg); return __builtin_expect(eg != 0, 1); };
  u32 fn0 = Y_F + s0 * 4 + ((0x8400u >> (4 * n)) & 15), fn1 = (0x4884u >> (4 * n)) & 15, xd = n ? 7 * 512 : -7 * 512;
  u32 c0s = tp == 0 ? 0xf149f2cau : 0u;
  SPIN(inner); SPIN(csm0); SPIN(fn0); SPIN(fn1); SPIN(xd); SPIN(c0s);
#define PRIO(PI, PO) asm volatile("s_setprio %1\n\t.if %1 != %2\n\ts_cmp_lg_u32 %0, 0\n\ts_cbranch_scc1 1f\n\ts_setprio %2\n\t1:\n\t.endif" :: "s"(inner), "i"(PI), "i"(PO) : "scc")
  u4 on; on.x = on.y = on.z = on.w = 0xbf80bf80u;
  if (NS == 3) PIN(on);
  u32 uLo, uHi, aFn, xr, etag, aCr, nV, nLo, aL, lv[3];
  u2 hs; u4 tv[NS]; f4 c0;
  auto fill = [&](int i, bool late = false) {
    if (i == 0) { m0.x = __builtin_amdgcn_perm(mS.y, mS.x, 0x07060100); m0.y = __builtin_amdgcn_perm(mS.w, mS.z, 0x07060100); PIN(m0); }
    if (i == 1) { mm.x = ~m0.x & mLo; mm.y = ~m0.y & mLo; PIN(mm); }
    if (i == 2) { mp.x = ~(m0.x | mLo); mp.y = ~(m0.y | mLo); PIN(mp); }
    if (i == 3) { nLo = ~mLo; aCr = (aCV & 0xffffu) | (L_CS & ~0xffffu); PIN(nLo); PIN(aCr); }
    if (i == 12) { lv[0] = 1; PIN(lv[0]); if (NS == 4) { lv[1] = 2; PIN(lv[1]); } else { aL = Y_F + n * 4; PIN(aL); } }
    if (NS == 4 && !late) {
      if (i == 4) { const float cf = __uint_as_float(c0s); c0 = f4{cf, cf, cf, cf}; PIN(c0); }
      return;
    }
    if (i == 4) { u32 up = __builtin_amdgcn_sbfe(aH, 8, 1); PIN(up); hs.x = (mV ^ up) & 0x3f803f80u; aFn = up & fn1; PIN(hs.x); PIN(aFn); }
    if (i == 5) { hs.y = hs.x; PIN(hs); aFn += fn0; xr = xg + xd; PIN(aFn); PIN(xr); }
    if (i == 6 && late) { etag = (int)xg >> 31; nV = ~mV; PIN(etag); PIN(nV); etag = tag & ~etag; PIN(etag); }
    if (i == 7 && late) { uLo = 0x3f80u; uHi = 0x3f800000u; PIN(uLo); PIN(uHi); if (NS == 4) { lv[2] = 3; PIN(lv[2]); } }
    if (i >= 8 && i < 8 + NS && late) { u4& v = tv[i - 8]; v.y = v.w = tag; PIN(v); }
  };
  auto fills = [&]() {
#pragma unroll
    for (int i = 0; i < 13; ++i) fill(i);
    SB();
  };
  if (NS == 3) fills();
  auto pre2 = [&]() {
    int w_ = wave, mg_ = mg, ng_ = ng, tp_ = tp;
    SPIN(w_); SPIN(mg_); SPIN(ng_); SPIN(tp_);
    g2.pre(w_, mg_, ng_); g2.pub(tp_, parity, j, rb);
    SB();
  };
  auto warm = [&](int cnt, int gap) {
    u4 w; w.x = aA0[0] * 0x9E3779B1u; w.y = w.x * 0x85EBCA77u; w.z = w.y ^ (w.x >> 7); w.w = w.z * 0xC2B2AE35u;
    w &= 0xbfffbfffu;
    f4 d; u32 c = cnt;
    asm volatile("s_nop 1\n1:\n\tv_mfma_f32_16x16x32_bf16 %0, %2, %3, 0\n\t.if %4\n\ts_nop %4\n\t.endif\n\tv_mfma_f32_16x16x32_bf16 %0, %3, %2, 0\n\t.if %4\n\ts_nop %4\n\t.endif\n\t"
                 "s_sub_u32 %1, %1, 1\n\ts_cbranch_scc0 1b\n\ts_nop 7" : "=&v"(d), "+s"(c) : "v"(w), "v"(mS ^ w.wzyx), "i"(gap) : "scc");
    SB();
  };
  if (NS == 4 && WARM) warm(WARM - 1, WGAP);
  if (NS == 3 && WARM3) warm(WARM3 - 1, WGAP);
  STAMP(1);
  PST(4);
  if (PBRK && NS == 4) NEW_PAGE();
  lds_bar();
  PST(5);
  STAMP(2);
  CSTAMP(0);
  ldf(0, 0, 0);
#pragma unroll
  for (int s = 0; s < 9; ++s) {
    SB();
    if (s == 8 && NS == 3 && C3E) { PRIO(2, 3); SB(); }
    ldf(1, s, 1);
    mma(0, 0, 3);
    if (s == 8 && NS == 3) {
#pragma unroll
      for (int i = 0; i < 9 - S8D; ++i) {
        __builtin_amdgcn_sched_group_barrier(0x008, 1, 0);
        if (i < S8D) __builtin_amdgcn_sched_group_barrier(0x100, 2, 0); else __builtin_amdgcn_sched_group_barrier(0x100, 1, 0);
      }
    } else pattern();
    SB();
    if (s == 0 && !INIT1) { at.template init<NS>(lane, n, tau, edge, tp, parity, j); SB(); }
    if (s == 8) break;
    if (s == 0 && NS == 3 && WARM2) {
      f4 d; u32 c = WARM2 - 1;
      asm volatile("1:\n\tv_mfma_f32_16x16x32_bf16 %0, %2, %3, 0\n\tv_mfma_f32_16x16x32_bf16 %0, %4, %5, 0\n\ts_sub_u32 %1, %1, 1\n\ts_cbranch_scc0 1b\n\ts_nop 7"
                   : "=&v"(d), "+s"(c) : "v"(A[0][0]), "v"(B[0][0]), "v"(A[0][1]), "v"(B[0][1]) : "scc");
      SB();
    }
    if (s >= 1) KST(2 * s - 1);
    if (s >= 6) EST(2 * s - 11);
    if (s < 3) PST(6 + 2 * s);
    if (NS == 3 && s < 8 && ((PTAB >> (8 * (s & 7))) & 255))
      asm volatile(".rept %0 / 4\n\ts_nop 15\n\t.endr\n\t.if %0 %% 4\n\ts_nop (%0 %% 4) * 4 - 1\n\t.endif" :: "i"((int)((PTAB >> (8 * (s & 7))) & 255)));
    bar();
    if (PBRK && NS == 3 && s == 7) NEW_PAGE();
    if (s < 3) PST(7 + 2 * s);
    KST(2 * s);
    if (s >= 5) EST(2 * s - 10);
    SB();
    ldf(0, s + 1, 0);
    mma(1, 0, 3);
    pattern();
    SB();
  }
  if (NS == 4 || !C3E) PRIO(2, 3);
  KST(15);
  EST(5);
  if (PBRK && NS == 4 && CSD) { pre2(); NEW_PAGE(); fills(); if (CSD > 2) __builtin_amdgcn_s_sleep(CSD - 2); }
  if (NS == 4)
    asm volatile("s_mov_b32 m0, %8\n\ts_mov_b32 exec_lo, 0x10001\n\ts_mov_b32 exec_hi, 0x10001\n\t"
                 "ds_write_addtid_b32 %0\n\tds_write_addtid_b32 %1 offset:4\n\tds_write_addtid_b32 %2 offset:8\n\tds_write_addtid_b32 %3 offset:12\n\t"
                 "ds_write_addtid_b32 %4 offset:16\n\tds_write_addtid_b32 %5 offset:20\n\tds_write_addtid_b32 %6 offset:24\n\tds_write_addtid_b32 %7 offset:28\n\t"
                 "s_mov_b64 exec, -1"
                 :: "v"(acc[0][0][0]), "v"(acc[0][0][1]), "v"(acc[0][0][2]), "v"(acc[0][0][3]), "v"(acc[0][1][0]), "v"(acc[0][1][1]), "v"(acc[0][1][2]), "v"(acc[0][1][3]),
                    "s"(csm0) : "memory");
  SB();
  if (PBRK && NS == 4 && !CSD) NEW_PAGE();
  const bf8 ones = __builtin_bit_cast(bf8, on);
  SB();
  __builtin_amdgcn_s_waitcnt(0xc07f);
  EST(7);
  bar();
  EST(6);
  FST(0);
  SB();
  f2 stt[NS];
  f4 cs[2];
  auto ldqk = [&](int buf, int kk) {
    B[buf][0] = ld_tr8(aB[0] + kk * 4096, 512);
    A[buf][0] = ld_b128((kk ? aA1[0] : aA0[0]) + s0 * 2048);
    B[buf][1] = ld_tr8(aB[0] + kk * 4096 + 8192, 512);
#pragma unroll
    for (int a = 1; a < NS; ++a) A[buf][a] = ld_b128((kk ? aA1[0] : aA0[0]) + (s0 + a) * 2048);
    FENCE();
  };
  auto mmcs = [&](int buf) { if (NS == 3) { cs[1] = mfma(B[buf][1], ones, cs[1]); cs[0] = mfma(B[buf][0], ones, cs[0]); } };
  if (NS == 3) { cs[1] = ld_f4(aCS + 16); cs[0] = ld_f4(aCS); FENCE(); }
  ldqk(0, 0);
  ldqk(2, 1);
#pragma unroll
  for (int a = 0; a < NS; ++a) { stt[a].x = ld_f(aST + a * 64); stt[a].y = ld_f(aST + a * 64 + 512); FENCE(); }
  B[0][2] = ld_tr8(aB[0] + 16384, 512); B[2][2] = ld_tr8(aB[0] + 4096 + 16384, 512);
  mma(1, 0, 3);
#pragma unroll
  for (int i = 0; i < 3 * NS; ++i) {
    __builtin_amdgcn_sched_group_barrier(0x008, 1, 0);
    __builtin_amdgcn_sched_group_barrier(0x100, NS == 4 ? RPM4 : RPM, 0);
  }
  SB();
  mmcs(1);
  if (NS == 3) { PIN(cs[0]); PIN(cs[1]); }
  SB();
  FST(1);
  mma(0, 0, 2);
  mmcs(0);
  if (NS == 3) { PIN(cs[0]); PIN(cs[1]); }
  SB();
  auto step = [&](int l) { asm volatile("ds_write_b32 %0, %1" :: "v"(aF), "v"(lv[l - 1]) : "memory"); };
  u32 lvl;
  auto reached = [&](u32 v) { return __builtin_expect((u32)__builtin_amdgcn_readfirstlane(lvl) >= v, 1); };
  mmcs(2);
  if (NS == 3) { PIN(cs[0]); PIN(cs[1]); }
  SB();
  acc[0][1] = mfma(B[2][1], A[2][0], acc[0][1]); SB();
  acc[0][0] = mfma(B[2][0], A[2][0], acc[0][0]); SB();
#pragma unroll
  for (int a = 1; a < NS; ++a) { acc[a][1] = mfma(B[2][1], A[2][a], acc[a][1]); SB(); }
#pragma unroll
  for (int i = 0; i < 5; ++i) {
    if (i + 1 < NS) acc[i + 1][0] = mfma(B[2][0], A[2][i + 1], acc[i + 1][0]);
    if (NS == 4) {
      if (i >= 3) acc[0][2] = mfma(A[2 * (i - 3)][0], B[2 * (i - 3)][2], acc[0][2]);
      SB();
      if (i < 4) {
#pragma unroll
        for (int r = 2 * (i & 1); r < 2 * (i & 1) + 2; ++r) { float v = dppf<0x150>(acc[0][1 - (i >> 1)][r]); PIN(v); cs[1 - (i >> 1)][r] = v; }
      }
    }
    SB();
  }
  PIN(cs[0]); PIN(cs[1]);
#pragma unroll
  for (int a = 0; a < NS; ++a) PIN(stt[a]);
#pragma unroll
  for (int a = 0; a < NS; ++a) { PIN(acc[a][0]); PIN(acc[a][1]); PIN(acc[a][2]); }
  SB();
  CSTAMP(1);
  STAMP(3);
  FST(2);
  auto bc4 = [](float v) { return f4{v, v, v, v}; };
  auto pk4 = [](f4 v) { u2 r; r.x = pk2(v[0], v[1]); r.y = pk2(v[2], v[3]); return r; };
  u2 q[NS], kLo[NS], kHi[NS + 1];
  auto lnq = [&]() {
#pragma unroll
    for (int a = NS - 1; a >= 0; --a) {
      if (QSC) {
#pragma unroll
        for (int r = 0; r < 4; ++r) { float t = fmaf(stt[a].x, cs[0][r], acc[a][0][r]); PIN(t); acc[a][0][r] = t; }
      } else acc[a][0] = __builtin_elementwise_fma(bc4(stt[a].x), cs[0], acc[a][0]);
    }
    SB();
  };
  {
    u2 k[NS];
    if (KSC) {
#pragma unroll
      for (int a = 0; a < NS; ++a)
#pragma unroll
        for (int r = 0; r < 4; ++r) { float t = fmaf(stt[a].x, cs[1][r], acc[a][1][r]); PIN(t); acc[a][1][r] = t; }
      SB();
#pragma unroll
      for (int a = 0; a < NS; ++a)
#pragma unroll
        for (int r = 0; r < 4; ++r) { float t = acc[a][1][r] * stt[a].y; PIN(t); acc[a][1][r] = t; }
      SB();
    } else {
#pragma unroll
      for (int a = 0; a < NS; ++a) { acc[a][1] = __builtin_elementwise_fma(bc4(stt[a].x), cs[1], acc[a][1]); PIN(acc[a][1]); }
      SB();
#pragma unroll
      for (int a = 0; a < NS; ++a) { acc[a][1] *= stt[a].y; PIN(acc[a][1]); }
      SB();
    }
#pragma unroll
    for (int a = NS - 1; a >= 0; --a) { k[a] = pk4(acc[a][1]); PIN(k[a]); }
    SB();
#pragma unroll
    for (int i = 0; i < NS; ++i) {
      const int a = NS - 1 - i;
      asm volatile("v_and_b32 %0, %4, %7\n\tv_and_b32 %1, %5, %7\n\t"
                   "v_and_b32_dpp %0, %4, %6 row_ror:8 row_mask:0xf bank_mask:0x3\n\tv_and_b32_dpp %1, %5, %6 row_ror:8 row_mask:0xf bank_mask:0x3\n\t"
                   "v_and_b32 %2, %4, %6\n\tv_and_b32 %3, %5, %6\n\t"
                   "v_and_b32_dpp %2, %4, %7 row_ror:8 row_mask:0xf bank_mask:0xc\n\tv_and_b32_dpp %3, %5, %7 row_ror:8 row_mask:0xf bank_mask:0xc"
                   : "=&v"(kHi[a + 1].x), "=&v"(kHi[a + 1].y), "=&v"(kLo[a].x), "=&v"(kLo[a].y) : "v"(k[a].x), "v"(k[a].y), "v"(mOwn), "v"(mRot));
      if (NS == 4 && i == 0) { st_u2(L_HK + aH, kHi[NS]); st_f(aCV, acc[0][2][0]); step(1); }
    }
  }
  SB();
  lnq();
  if (NS == 3) { FENCE(); lvl = lds_peek(aL); FENCE(); kHi[0] = ld_u2(L_HK + aH); FENCE(); }
  SB();
  FST(3);
  const f4 z = {0.f, 0.f, 0.f, 0.f};
  f4 t[NS];
  {
    constexpr int L = NS - 1;
    f4 d0[NS], d1[NS];
    u4 qq[NS];
    auto cq_ = [&](int a) { if (a >= 0) q[a] = pk4(acc[a][0]); SB(); };
    auto lo_ = [&](int a) { qq[a].x = q[a].x & mLo; qq[a].y = q[a].y & mLo; SB(); };
    auto hi_ = [&](int a) { qq[a].z = q[a].x & nLo; qq[a].w = q[a].y & nLo; SB(); };
    auto d0_ = [&](int a) { d0[a] = mfma16(kLo[a], q[a], (NS == 4 && a == 0) ? c0 : z); SB(); };
    auto sel = [&](int a, int r0, int r1) { for (int r = r0; r < r1; ++r) t[a][r] = fsel(mS[r], d0[a][r], d1[a][r]); SB(); };
    auto pkt = [&](int a) { tv[a].x = pk2(t[a][0], t[a][1]); tv[a].z = pk2(t[a][2], t[a][3]); SB(); };
    cq_(L); cq_(L - 1);
    d0_(L); lo_(L); hi_(L);
    d0_(L - 1); lo_(L - 1); cq_(L - 2);
    d1[L] = mfma(mk8(kHi[L], kHi[L + 1]), __builtin_bit_cast(bf8, qq[L]), z); SB(); hi_(L - 1); cq_(L - 3);
    if (NS == 3) { fill(10, true); SB(); }
    d1[L - 1] = mfma16(kHi[L - 1], qq[L - 1].xy, z); SB(); lo_(L - 2); hi_(L - 2);
    d1[L - 1] = mfma16(kHi[L], qq[L - 1].zw, d1[L - 1]); SB();
    if (NS == 4) { fill(11, true); SB(); }
    else {
      fill(9, true); fill(8, true);
      PIN(d0[L]); PIN(d0[L - 1]); PIN(d1[L]); PIN(d1[L - 1]); PIN(qq[0]); PIN(q[0]);
      if (!reached(1)) { lds_wait_ge<0>(Y_F + n * 4, 1); kHi[0] = ld_u2(L_HK + aH); }
      SB();
    }
    d0_(L - 2); sel(L, 0, 3);
    d1[L - 2] = mfma(mk8(kHi[L - 2], kHi[L - 1]), __builtin_bit_cast(bf8, qq[L - 2]), z); SB(); sel(L, 3, 4); pkt(L);
    if (NS == 4) {
      d0_(0); fill(10, true); fill(9, true); SB();
      d1[0] = mfma16(kHi[1], q[0], z);
      SB(); fill(8, true); SB();
    }
#pragma unroll
    for (int a = L - 1; a >= 0; --a) { sel(a, 0, 4); pkt(a); }
  }
#pragma unroll
  for (int a = 0; a < NS; ++a) { PIN(t[a]); PIN(tv[a]); }
  FST(4);
  if (isedge()) {
#pragma unroll
    for (int a = NS - 1; a >= 0; --a) raw_store(tv[a], sws, xg + a * 512, 0, 16);
  }
  SB();
  f4 vm[NS], vr[NS];
  float csv, cq[NS];
  u32 aHV;
  auto vmm = [&](int i) { const int a = NS - 1 - i % 3, buf = 2 * (i / 3); acc[a][2] = mfma(A[buf][a], B[buf][2], acc[a][2]); SB(); };
#pragma unroll
  for (int a = 0; a < NS; ++a) {
    asm volatile("ds_write2st64_b32 %0, %1, %2 offset0:%3 offset1:%4" :: "v"(aXw), "v"(tv[a].x), "v"(tv[a].z), "i"(2 * a), "i"(2 * a + 1) : "memory");
    SB();
    if (a == 0) vmm(0);
    if (a == 2) vmm(1);
  }
  step(NS == 4 ? 2 : 1);
  STAMP(10);
  FST(5);
  if (NS == 3) PRIO(Q2I, Q2O); else PRIO(2, PR2);
  SB();
  vmm(2);
  csv = ld_f(aCr); vm[NS - 1] = ld_f4(aMR + (NS - 1) * 64); SB();
  vmm(3);
  vm[NS - 2] = ld_f4(aMR + (NS - 2) * 64); vm[NS - 3] = ld_f4(aMR + (NS - 3) * 64); SB();
  vmm(4);
  if (NS == 4) vm[0] = ld_f4(aMR);
  vr[NS - 1] = ld_f4(aMR + 512 + (NS - 1) * 64); SB();
  vmm(5);
#pragma unroll
  for (int a = NS - 2; a >= 0; --a) vr[a] = ld_f4(aMR + 512 + a * 64);
  SB();
  aHV = L_HV + aH;
  PIN(aHV);
  u4 p0, vp;
  p0.x = p0.y = 0; PIN(p0);
  if (NS == 3) { vp.x = vp.y = 0; PIN(vp); }
  if (NS == 4) { fill(4, true); fill(5, true); }
  fill(6, true); fill(7, true);
#pragma unroll
  for (int a = 0; a < NS; ++a) { cq[a] = stt[a].y * (0.22360679774997896f * 1.4426950408889634f); PIN(cq[a]); }
  SB();
  FST(6);
  u4 e[NS];
#pragma unroll
  for (int a = 0; a < NS; ++a) asm volatile("" : "=v"(e[a]));
  auto look = [&]() {
#pragma unroll
    for (int a = 0; a < NS; ++a) e[a] = raw_load(sws, xr + a * 512, 0, 16);
  };
  u2 vLo[NS];
#pragma unroll
  for (int a = NS - 1; a >= 0; --a) acc[a][2] = __builtin_elementwise_fma(vm[a], bc4(csv), acc[a][2]);
  SB();
#pragma unroll
  for (int a = NS - 1; a >= 0; --a) acc[a][2] *= vr[a];
  SB();
#pragma unroll
  for (int a = NS - 1; a >= 0; --a) { vLo[a] = pk4(acc[a][2]); PIN(vLo[a]); }
  SB();
  if (LKP == 0 || (LKP == 2 && NS == 3)) { if (isedge()) look(); }
  SB();
  constexpr u32 XT = NS == 4 ? 2 : 1;
  u2 y[NS];
  u32 cx;
  auto ldy = [&]() {
    FENCE(); cx = lds_peek(aFn); FENCE();
#pragma unroll
    for (int a = 0; a < NS; ++a) { y[a].x = ld_u(aXr + a * 512); y[a].y = ld_u(aXr + a * 512 + 256); }
    FENCE();
  };
  ldy();
  SB();
  u4 vv[NS];
  {
    u32 w[NS][4];
#pragma unroll
    for (int a = NS - 1; a >= 0; --a) { w[a][2] = vLo[a].x & nV; w[a][3] = vLo[a].y & nV; w[a][0] = vLo[a].x & mV; w[a][1] = vLo[a].y & mV; SB(); }
#pragma unroll
    for (int a = NS - 1; a >= 0; --a) {
      asm volatile("v_permlane32_swap_b32 %0, %2\n\tv_permlane32_swap_b32 %1, %3" : "+v"(w[a][0]), "+v"(w[a][1]), "+v"(w[a][2]), "+v"(w[a][3]));
      vv[a] = u4{w[a][0], w[a][1], w[a][2], w[a][3]};
      if (NS == 4 && a == NS - 2) { st_u2(aHV, vv[NS - 1].zw); step(3); }
      SB();
    }
#pragma unroll
    for (int a = 0; a < NS; ++a) PIN(vv[a]);
  }
  if (NS == 3) { FENCE(); lvl = lds_peek(aL); FENCE(); vp.zw = ld_u2(aHV); FENCE(); }
  SB();
  if (LKP == 1 || (LKP == 2 && NS == 4)) { if (isedge()) look(); }
  SB();
  FST(7);
  if (__any(cx < XT)) {
#pragma nounroll
    for (u32 spin = 0; spin < (1u << 20); ++spin) {
      FENCE();
      if (__all(lds_peek(aFn) >= XT)) break;
    }
    ldy();
  }
  auto addp = [](f4& t, u32 a, u32 b, u32 lo, u32 hi) {
    t[0] = __builtin_amdgcn_fdot2_f32_bf16(__builtin_bit_cast(bf2, a), __builtin_bit_cast(bf2, lo), t[0], false);
    t[1] = __builtin_amdgcn_fdot2_f32_bf16(__builtin_bit_cast(bf2, a), __builtin_bit_cast(bf2, hi), t[1], false);
    t[2] = __builtin_amdgcn_fdot2_f32_bf16(__builtin_bit_cast(bf2, b), __builtin_bit_cast(bf2, lo), t[2], false);
    t[3] = __builtin_amdgcn_fdot2_f32_bf16(__builtin_bit_cast(bf2, b), __builtin_bit_cast(bf2, hi), t[3], false);
  };
#pragma unroll
  for (int a = 0; a < NS; ++a) { addp(t[a], y[a].x, y[a].y, uLo, uHi); PIN(t[a]); }
  if (NS == 3) {
    if (!reached(3)) { lds_wait_ge<0>(Y_F + n * 4, 3); vp.zw = ld_u2(aHV); }
    PIN(vp);
  }
  if (NS == 3) { if (Q3I != Q2I || Q3O != Q2O) PRIO(Q3I, Q3O); } else if (PR3 != PR2) PRIO(2, PR3);
  STAMP(8);
  FST(8);
  if (isedge()) {
    auto bad = [&]() {
      u32 b = __builtin_amdgcn_bitop3_b32(e[0].y, e[0].w, etag, 0x7e);
#pragma unroll
      for (int a = 1; a < NS; ++a) b |= __builtin_amdgcn_bitop3_b32(e[a].y, e[a].w, etag, 0x7e);
      PIN(b);
      return __any(b != 0u);
    };
    if (bad()) {
#pragma nounroll
      for (u32 spin = 0; spin < SPIN_CAP; ++spin) {
        FENCE();
        look();
        if (!bad()) break;
      }
    }
#pragma unroll
    for (int a = 0; a < NS; ++a) { addp(t[a], e[a].x, e[a].z, uLo, uHi); PIN(t[a]); }
  }
  STAMP(6);
  FST(9);
  SB();
  float mx[4];
#pragma unroll
  for (int a = 0; a < NS; ++a) mx[a] = vmax(vmax(t[a][0], t[a][1]), t[a][2]);
  SB();
#pragma unroll
  for (int a = 0; a < NS; ++a) mx[a] = vmax(mx[a], t[a][3]);
  if (NS == 3) mx[3] = mx[2];
  SB();
  {
    u32 c[2];
#pragma unroll
    for (int h = 0; h < 2; ++h) {
      auto w = __builtin_amdgcn_permlane16_swap(__float_as_uint(mx[2 * h]), __float_as_uint(mx[2 * h + 1]), false, false);
      mx[2 * h] = __uint_as_float(w[0]); mx[2 * h + 1] = __uint_as_float(w[1]);
    }
    SB();
#pragma unroll
    for (int h = 0; h < 2; ++h) c[h] = __float_as_uint(vmax(mx[2 * h], mx[2 * h + 1]));
    SB();
#pragma unroll
    for (int h = 0; h < 2; ++h) {
      if (NS == 3 && h == 1) { mx[2] = __uint_as_float(c[1]); continue; }
      auto w = __builtin_amdgcn_permlane16_swap(c[h], c[h], false, false);
      mx[2 * h] = __uint_as_float(w[0]); mx[2 * h + 1] = __uint_as_float(w[1]);
    }
    SB();
  }
#pragma unroll
  for (int a = 0; a < NS; ++a) { mx[a] *= -cq[a]; SB(); }
#pragma unroll
  for (int a = NS - 1; a >= 0; --a) { t[a] = __builtin_elementwise_fma(t[a], bc4(cq[a]), bc4(mx[a])); SB(); }
  f4 o[NS], sm[NS];
  u2 pp[NS], ob[4];
  bf8 p1[NS];
  auto ex = [&](int a, int r) { if (a >= 0) { t[a][r] = __builtin_amdgcn_exp2f(t[a][r]); t[a][r + 1] = __builtin_amdgcn_exp2f(t[a][r + 1]); } SB(); };
  auto rc = [&](int a) { asm volatile("v_rcp_f32 %0, %1" : "=v"(mx[a]) : "v"(sm[a][0]), "v"(sm[a])); };
  ex(NS - 1, 0); ex(NS - 1, 2); ex(NS - 2, 0);
  FST(10);
#pragma unroll
  for (int a = NS - 1; a >= 0; --a) {
    const bool prev = NS == 3 || a > 0;
    pp[a] = pk4(t[a]); SB();
    p1[a] = mk8(and2(pp[a], m0), and2(pp[a], mp)); PIN(p1[a]); SB();
    sm[a] = mfma16(hs, pp[a], z); SB();
    if (prev) { p0.z = pp[a].x & mm.x; p0.w = pp[a].y & mm.y; PIN(p0); }
    if (a == 0) rc(NS - 1);
    SB();
    o[a] = mfma(__builtin_bit_cast(bf8, vv[a]), p1[a], z); SB();
    ex(a - 1, 2);
    if (a == 0) { rc(NS - 2); SB(); }
    if (prev) { o[a] = mfma(__builtin_bit_cast(bf8, a ? vv[a - 1] : vp), __builtin_bit_cast(bf8, p0), o[a]); SB(); }
    ex(a - 2, 0);
  }
  auto put = [&](int h) {
    auto wx = __builtin_amdgcn_permlane32_swap(ob[2 * h].x, ob[2 * h + 1].x, false, false);
    auto wy = __builtin_amdgcn_permlane32_swap(ob[2 * h].y, ob[2 * h + 1].y, false, false);
    u4 v; v.x = wx[0]; v.y = wy[0]; v.z = wx[1]; v.w = wy[1];
    raw_store(v, sws, so[h], 0, 16);
    SB();
  };
  if (NS == 4) { rc(1); SB(); }
#pragma unroll
  for (int a = NS - 1; a > 0; --a) { o[a] *= mx[a]; SB(); }
  PIN(sm[0]);
  mx[0] = __builtin_amdgcn_rcpf(sm[0][0]); SB();
#pragma unroll
  for (int a = NS - 1; a > 1; --a) { ob[a] = pk4(o[a]); SB(); }
  if (NS == 3) asm volatile("" : "=v"(ob[3]));
  o[0] *= mx[0]; SB();
  ob[1] = pk4(o[1]); SB();
  ob[0] = pk4(o[0]); SB();
  put(1); put(0);
  CSTAMP(2);
  STAMP(4);
  FST(11);
  __builtin_amdgcn_s_setprio(0);
  SB();
  if (PBR2) NEW_PAGE();
  SB();
  if (NS == 3) pre2();
}

constexpr u32 A_D = 0, A_G = L_WO, A_U = L_WO + 40960;
constexpr u32 A_XN = 40960;
constexpr u32 A_RED = A_XN + 2560;
constexpr u32 A_H = A_RED + 2048;
constexpr u32 A_TOP = A_H + 1024;

DEV void aux_loader(Params& p, int lane, int lw, int a7, int f, bool ffn, int mg, int ng, int koff, Epi& ei STM_ARG) {
  const u32 ep = epoch_get(p);
  const u4 srdo = srd(p.wo), srdg = srd(p.wg), srdu = srd(p.wu), srdd = srd(p.wd);
  const int rr = lane >> 2;
  u32 vg = (lw * 16 + rr) * (F_ * 2) + f * 64 + (((lane & 3) ^ (((rr >> 3) & 1) << 1)) << 4);
  u32 vd = (f * 32 + lw * 8 + (lane >> 3)) * 1280 + (lane & 7) * 16;
  u32 vt = min((f * 4 + lw) * 30 + min(lane, 29), 6399) * 128;
  u32 ho = (lane | lw) ? 0x80000000u : WS_FLAG + (200 + f) * 256 + 128;
  PIN(vg); PIN(vd); PIN(vt); PIN(ho);
  ei.init(lane, lw, mg, ng);
  WoDma wo; wo.init(lane, lw, ng, 0, 104 + lw);
  u32 tag = epoch_tag(ep);
  PIN(tag);
  if (FFN_DELAY) __builtin_amdgcn_s_sleep(FFN_DELAY);
  STAMP(9);
  if (ffn) {
    const u4 s0 = (f & 1) ? srdu : srdg, s1 = (f & 1) ? srdg : srdu;
    const u32 a0 = ((f & 1) ? A_U : A_G) + lw * 1024, a1 = ((f & 1) ? A_G : A_U) + lw * 1024;
    dma_run<10, 4096, 64 * F_ * 2, 0, TH0>(a0, vg, s0, 0);
    asm volatile("s_mov_b32 m0, %0\n\ts_nop 0\n\tbuffer_load_dword %1, %2, 0 offen lds" :: "s"(L_DUMP + lw * 256), "v"(vt), "s"(srdo));
    STAMP(10);
    dma_run<10, 4096, 64 * F_ * 2, 0, TH1>(a1, vg, s1, 0);
    if (!DLATE) dma_run<10, 4096, 128>(A_D + lw * 1024, vd, srdd, 0);
    STAMP(1);
  } else {
    wo.issue<0, 27, TH0>(srdo, lw);
  }
  wait_bar<DLATE ? 0 : 10>();
  if (DLATE && ffn) dma_run<10, 4096, 128>(A_D + lw * 1024, vd, srdd, 0);
  asm volatile("buffer_store_dword %0, %1, %2, 0 offen sc1" :: "v"(tag), "v"(ho), "s"(srd(p.ws)));
  if (BOXW == 2) asm volatile("buffer_atomic_add %0, %1, %2, 0 offen" :: "v"(0u), "v"((lane | (lw & 2)) ? 0x80000000u : WS_BOX + (mg * 8 + ng) * 256 + lw * 64), "s"(srd(p.ws)));
  bar();
  STAMP(2);
  ei.issue(srd(p.x), srd(p.ws), WS_ST2);
  wo.issue<0, WOC, AW0, 1>(srdo, lw, f);
  asm volatile("s_cmp_gt_u32 %0, 53\n\ts_cbranch_scc1 1f\n\ts_waitcnt vmcnt(%1)\n\ts_barrier\n\ts_branch 2f\n1:\n\ts_waitcnt vmcnt(0)\n2:" :: "s"(f), "i"(WOC + 5) : "scc", "memory");
  STAMP(3);
  ei.wait<WOC>();
  ei.check(p.ws.p, tag);
  ei.tag = tag;
  SB();
#pragma unroll
  for (int i = 0; i < YG; ++i) ei.gate(i);
  SB();
  asm volatile("buffer_load_dwordx4 %0, off, %1, 0 offset:%2 sc1" : "=v"(ei.fin) : "s"(srd(p.ws)), "i"(WS_FIN));
  static_assert((26 - WOC) % 3 == 0, "");
  const u32 skip = lw == 3 ? 99 : f;
  wo.issue<WOC, 27, AWT, 1, 3>(srdo, lw, skip);
  asm volatile("s_cmp_gt_u32 %1, 53\n\ts_cbranch_scc0 1f\n\ts_waitcnt vmcnt(1)\n1:\n\ts_waitcnt vmcnt(%2)" : "+v"(ei.fin) : "s"(skip), "i"((26 - WOC) * 4 / 3 + 1) : "scc");
  STAMP(4);
}

DEV void aux_compute(const Params& p, rsrc_t rws, u32& tag, int lane, int wave, int f, bool ffn, int xcd, int a7, RowIn& rin STM_ARG) {
  const int task = a7 + 7 * wave;
  if (task < 32) rin.stats(p, rws, tag, lane, xcd * 256 + task * 8, A_TOP + wave * 16 STM);
  else tag = epoch_tag(epoch_get(p));
  PST(4);
  const int g = lane >> 4, i16 = lane & 15;
  const int q = i16 >> 2, pp = lane & 3;
  const int t = wave & 1, kq = wave >> 1;
  u32 ab = (kq * 160 + 8 * g + q) * 64 + (((2 * t + (pp >> 1)) ^ ((g & 1) << 1)) << 4) + (pp & 1) * 8;
  u32 xb = A_XN + (i16 & 1) * 1280 + (kq * 160 + 8 * g) * 2;
  u32 ar = i16 < 2 ? A_RED + (wave * 16 + g * 2 + i16) * 16 : L_NONE + lane * 16;
  u32 ad[5], ah[5];
#pragma unroll
  for (int i = 0; i < 5; ++i) {
    const int nt = wave * 5 + i;
    ad[i] = A_D + (8 * g + q) * 128 + pp * 8 + (nt >> 2) * 4096 + (nt & 3) * 32;
    ah[i] = (i16 & 7) == i ? A_H + wave * 128 + (i16 >> 3) * 64 + g * 16 : L_NONE + lane * 16;
    PIN(ad[i]); PIN(ah[i]);
  }
  u32 as = A_RED + (lane >> 5) * 256 + (lane & 31) * 4, aw = A_H + wave * 128 + ((lane >> 2) & 1) * 64 + (4 * (lane >> 3) + (lane & 3)) * 2;
  u32 yo = (i16 & 7) < 5 ? WS_YP + ((wave * 5 + (i16 & 7)) * 56 + f) * 128 + (i16 >> 3) * 64 + g * 16 : 0x80000000u;
  PIN(ab); PIN(xb); PIN(ar); PIN(as); PIN(aw); PIN(yo);
  STAMP(1);
  u32 so = 0x80000000u, xo = 0x80000000u;
  if (wave >= 5) {
    if (wave == 5) st_u(L_SYNC + lane * 4, 0);
    const u32 nt = (a7 < 4 && wave == 5) ? 5 : 4, a = A_TOP + min((u32)lane & 7, nt - 1) * 16;
    u4 k;
#pragma nounroll
    for (u32 spin = 0; spin < (1u << 20); ++spin) {
      FENCE();
      k = __builtin_bit_cast(u4, ld_f4(a));
      if (__all(k.y == tag && k.w == tag)) break;
      __builtin_amdgcn_s_sleep(1);
    }
    if (wave == 5) {
      if ((lane & 7) >= nt) k.x = k.z = 0;
      u32 k1 = k.x, k2 = k.z;
      top2_merge(k1, k2, dppu<0xB1>(k1), dppu<0xB1>(k2));
      top2_merge(k1, k2, dppu<0x4E>(k1), dppu<0x4E>(k2));
      top2_merge(k1, k2, dppu<0x141>(k1), dppu<0x141>(k2));
      k.x = k1; k.z = k2;
      bst128_sc1(rws, lane < 8 ? WS_KEY + f * 128 + lane * 16 : 0x80000000u, k);
      u32 d = __builtin_amdgcn_raw_buffer_load_b32(rws, f >= 40 ? 0x80000000u : lane < 54 ? WS_YP + (f * 56 + lane) * 128 : WS_GR + f * 128, 0, 16);
      PIN(d);
    } else {
      __builtin_amdgcn_s_sleep(KEY_DELAY);
#pragma nounroll
      for (u32 spin = 0;; ++spin) {
        FENCE();
        k = bld128_sc1(rws, WS_KEY + min(lane, 55) * 128);
        if (__all(k.y == tag && k.w == tag) || spin > SPIN_CAP) break;
        __builtin_amdgcn_s_sleep(8);
      }
      u32 k1 = lane < 56 ? k.x : 0, k2 = lane < 56 ? k.z : 0;
      top2_wave(k1, k2);
      k.x = k1; k.z = k2;
      if (f == 55) bst128_sc1(rws, (lane | t * 8) < 8 ? WS_FIN + lane * 16 : 0x80000000u, k);
      const int tk = key_tok(__builtin_amdgcn_readfirstlane(t ? k2 : k1));
      xo = tk * 1280; so = WS_ST2 + ((tk & 1) * 1024 + (tk >> 1)) * 16;
    }
  }
  const rsrc_t rx = __builtin_amdgcn_make_buffer_rsrc(p.x.p, 0, S_ * D_ * 2, 0x00020000);
  const u4 xa = __builtin_amdgcn_raw_buffer_load_b128(rx, xo + lane * 16, 0, 0);
  const u32 xc = __builtin_amdgcn_raw_buffer_load_b32(rx, xo + 1024 + lane * 4, 0, 0);
  u4 sv = bld128_sc1(rws, so);
  FENCE();
  STAMP(2);
  NEW_PAGE();
  lds_bar();
  STAMP(3);
  bf8 Wg[5], Wu[5];
#pragma unroll
  for (int ks = 0; ks < 5; ++ks) { Wg[ks] = ld_tr8(A_G + ab + ks * 2048, 256); Wu[ks] = ld_tr8(A_U + ab + ks * 2048, 256); }
#pragma unroll
  for (int ks = 0; ks < 5; ++ks) { PIN(Wg[ks]); PIN(Wu[ks]); }
  bar();
  if (__builtin_expect(wave >= 6, 1)) {
    if (__builtin_expect(__any(__builtin_amdgcn_bitop3_b32(sv.y, sv.w, tag, 0x7e) != 0u), 0)) {
#pragma nounroll
      for (u32 spin = 0; !__all(sv.y == tag && sv.w == tag) && spin < SPIN_CAP; ++spin) {
        FENCE();
        __builtin_amdgcn_s_sleep(1);
        sv = bld128_sc1(rws, so);
      }
    }
    const float m = __uint_as_float(sv.x), r_ = __uint_as_float(sv.z);
    u4 o; o.x = norm2(xa.x, m, r_); o.y = norm2(xa.y, m, r_); o.z = norm2(xa.z, m, r_); o.w = norm2(xa.w, m, r_);
    st_u4(A_XN + t * 1280 + lane * 16, o); st_u(A_XN + t * 1280 + 1024 + lane * 4, norm2(xc, m, r_));
    lds_add(Y_CB, 1);
  }
  if (__builtin_expect(!ffn, 0)) {
    lds_add(Y_PUB, 1);
    return;
  }
  lds_wait_ge<1>(Y_CB, 2);
  STAMP(8);
  {
    bf8 xb8[5];
#pragma unroll
    for (int ks = 0; ks < 5; ++ks) xb8[ks] = ld_b128(xb + ks * 64);
    SB();
    f4 ag = {0.f, 0.f, 0.f, 0.f}, au = {0.f, 0.f, 0.f, 0.f};
#pragma unroll
    for (int ks = 0; ks < 5; ++ks) { ag = mfma(Wg[ks], xb8[ks], ag); au = mfma(Wu[ks], xb8[ks], au); }
    SB();
    st_f4(ar, ag); st_f4(ar + 128, au);
  }
  STAMP(5);
  lds_bar();
  STAMP(6);
  {
    float a[4], b[4];
#pragma unroll
    for (int k = 0; k < 4; ++k) a[k] = ld_f(as + k * 512);
#pragma unroll
    for (int k = 0; k < 4; ++k) b[k] = ld_f(as + k * 512 + 128);
    bf8 Wd[5], hb[5];
#pragma unroll
    for (int i = 0; i < 5; ++i) Wd[i] = ld_tr8(ad[i], 512);
    SB();
    const float hh = rbf(silu_f(rbf((a[0] + a[1]) + (a[2] + a[3])))) * rbf((b[0] + b[1]) + (b[2] + b[3]));
    *(LAS u16*)(uintptr_t)aw = (u16)(pk2(hh, hh) & 0xffffu);
    FENCE();
#pragma unroll
    for (int i = 0; i < 5; ++i) hb[i] = ld_b128(ah[i]);
    SB();
    f4 y = {0.f, 0.f, 0.f, 0.f};
#pragma unroll
    for (int i = 0; i < 5; ++i) y = mfma(Wd[i], hb[i], y);
    u4 v; v.x = pk2(y[0], y[1]); v.z = pk2(y[2], y[3]); v.y = v.w = tag;
    bst128_sc1(rws, yo, v);
  }
  STAMP(4);
  lds_add(Y_PUB, 1);
}

template <bool MAIN, int WE>
DEV void gemm2(const Params& p, u32 tag, int wave, G2& c STM_ARG) {
  const int m = wave & 3, kh = wave >> 2;
  c.template init<MAIN>(tag);
  const u32 b0 = c.b0, oo = c.oo, fo = c.fo, gb = c.gb, pa = c.pa;
  u32 pm = c.pm;
  u64 pm64 = pm;
  SPIN(pm64);
  u4 of[10];
#define LG "s_nop " STR(LGAP) "\n\t"
#define LOADS "buffer_load_dwordx4 v[124:127], %[oo], %[srd], 0 offen offset:0 sc1\n\t" LG \
              "buffer_load_dwordx4 v[128:131], %[oo], %[srd], 0 offen offset:1024 sc1\n\t" LG \
              "buffer_load_dwordx4 v[132:135], %[oo], %[srd], 0 offen offset:2048 sc1\n\t" LG \
              "buffer_load_dwordx4 v[136:139], %[oo], %[srd], 0 offen offset:3072 sc1\n\t" LG \
              "buffer_load_dwordx4 v[140:143], %[oo], %[srd], %[s4] offen offset:0 sc1\n\t" LG \
              "buffer_load_dwordx4 v[144:147], %[oo], %[srd], %[s4] offen offset:1024 sc1\n\t" LG \
              "buffer_load_dwordx4 v[148:151], %[oo], %[srd], %[s4] offen offset:2048 sc1\n\t" LG \
              "buffer_load_dwordx4 v[152:155], %[oo], %[srd], %[s4] offen offset:3072 sc1\n\t" LG \
              "buffer_load_dwordx4 v[156:159], %[oo], %[srd], %[s8] offen offset:0 sc1\n\t" LG \
              "buffer_load_dwordx4 v[160:163], %[oo], %[srd], %[s8] offen offset:1024 sc1\n\t" LG \
              ""
#define OFS(C) C "{v[124:127]}"(of[0]), C "{v[128:131]}"(of[1]), C "{v[132:135]}"(of[2]), C "{v[136:139]}"(of[3]), C "{v[140:143]}"(of[4]), C "{v[144:147]}"(of[5]), C "{v[148:151]}"(of[6]), C "{v[152:155]}"(of[7]), C "{v[156:159]}"(of[8]), C "{v[160:163]}"(of[9])
#define POLL(R) "v_mov_b32 " R ", 0\n\tbuffer_atomic_add " R ", %[fo], %[srd], 0 offen sc0\n\t"
#define CHK(R) "s_waitcnt vmcnt(2)\n\tv_cmp_ne_u32 vcc, %[tag], " R "\n\ts_cbranch_vccz 2f\n\t" POLL(R)
#define LSPIN(L, O, V) L ":\n\tds_read_b32 %[t], %[pa] offset:" O "\n\ts_sub_u32 %[n], %[n], 1\n\ts_cselect_b32 %[l], -1, 0\n\ts_waitcnt lgkmcnt(0)\n\t" \
                 "v_readfirstlane_b32 %[r], %[t]\n\ts_or_b32 %[r], %[r], %[l]\n\ts_and_b32 %[l], %[r], " V "\n\ts_cbranch_scc0 " L "b\n\t"
  static_assert(Y_WO == Y_PUB + 4, "");
  u32 n = SPIN_CAP << 5, got, t, l, sg = c.sg;
  asm volatile("" : OFS("="));
  bf8 W[WPRE][5];
  auto ldw = [&](int ks) {
#pragma unroll
    for (int i = 0; i < 5; ++i) W[ks][i] = ld_tr8(b0 + ks * 5120 + i * 32, 8 * 160);
  };
  bool early = false;
  if (MAIN) {
    if (WE) {
      early = __builtin_amdgcn_readfirstlane(lds_peek(pa + 4)) & 4;
#pragma unroll
      for (int ks = 0; ks < WE; ++ks)
#pragma unroll
        for (int i = 0; i < 5; ++i) asm volatile("" : "=v"(W[ks][i]));
      if (__builtin_expect(early, 1)) {
#pragma unroll
        for (int ks = 0; ks < WE; ++ks) ldw(ks);
      }
    }
    SB();
    __builtin_amdgcn_s_setprio(3);
    FST(13);
    if (FSTAMPS + 0 == 1) { wait_vm<0>(); FST(14); }
    asm volatile("s_mov_b64 exec, 1\n\ts_waitcnt vmcnt(0)\n\tds_add_rtn_u32 %0, %1, %2\n\ts_mov_b32 exec_lo, %6\n\ts_waitcnt lgkmcnt(0)\n\t"
                 "v_readfirstlane_b32 vcc_lo, %0\n\ts_cmp_lg_u32 vcc_lo, 7\n\ts_cbranch_scc1 1f\n\t"
                 "buffer_atomic_swap %3, %4, %5, 0 offen\n1:\n\ts_mov_b64 exec, -1"
                 : "=&v"(t) : "v"(pa), "v"(c.one), "v"(c.tv), "v"(c.po), "s"(srd(p.ws)), "s"(c.me) : "scc", "vcc", "memory");
    __builtin_amdgcn_s_setprio(0);
    STAMP(5);
    FST(12);
  }
  GST(14);
  if (!__builtin_expect(early, 1)) {
    lds_wait_bit<1>(Y_WO, 4);
    if (MAIN) {
#pragma unroll
      for (int ks = 0; ks < WE; ++ks) ldw(ks);
    }
  }
  GST(15);
#define POLL1(R) asm volatile("s_cmp_eq_u32 %[pm], 0\n\ts_cbranch_scc1 1f\n\ts_mov_b64 exec, %[pm64]\n\t" POLL(R) "s_mov_b64 exec, -1\n1:" \
               : "+{v[124:127]}"(of[0]) : [fo] "v"(fo), [srd] "s"(srd(p.ws)), [pm] "s"(pm), [pm64] "s"(pm64) : "scc", "memory")
#pragma unroll
  for (int ks = MAIN ? WE : 0; ks <= WPRE; ++ks) {
    SB();
    if (ks == (MAIN ? PM0 : PK0)) POLL1("v124");
    if (ks == (MAIN ? PM1 : PK1)) POLL1("v125");
    if (ks == (MAIN ? PM2 : PK2)) POLL1("v126");
    SB();
    if (ks < WPRE) ldw(ks);
  }
  SB();
  __builtin_amdgcn_s_setprio(IPRIO);
  asm volatile("s_cmp_eq_u32 %[pm], 0\n\ts_cbranch_scc1 5f\n\t"
               "s_mov_b64 exec, %[pm64]\n" LSPIN("3", "0", "8") "s_movk_i32 %[l], 0x4000\n"
               "1:\n\t" CHK("v124") CHK("v125") CHK("v126") "s_sub_u32 %[l], %[l], 1\n\ts_cbranch_scc0 1b\n"
               "2:\n\ts_mov_b64 exec, 1\n\tds_or_b32 %[pa], %[gb] offset:4\n\ts_mov_b64 exec, -1\n\t.if " STR(PDL) "\n\ts_sleep " STR(PDL) "\n\t.endif\n\ts_branch 4f\n"
               LSPIN("5", "4", "%[sg]") "4:\n\t" LOADS "s_min_u32 %[n], %[n], 0x100000\n" LSPIN("7", "4", "32")
               : OFS("+"), [n] "+s"(n), [l] "=&s"(l), [r] "=&s"(got), [t] "=&v"(t)
               : [fo] "v"(fo), [srd] "s"(srd(p.ws)), [tag] "s"(tag), [gb] "v"(gb), [pm] "s"(pm), [pm64] "s"(pm64),
                 [pa] "v"(pa), [oo] "v"(oo), [sg] "s"(sg), [s4] "s"(4096), [s8] "s"(8192) : "scc", "vcc", "memory");
  GST(0);
  STAMP(7);
  __builtin_amdgcn_s_setprio(MPRIO);
  u32 sfa = Y_S + m * 4, sfv = kh + 1;
  SPIN(sfa); SPIN(sfv);
  asm volatile("s_mov_b32 m0, %0" :: "s"(wave * ST_W));
  auto landed = [&](int ks) {
    if (ks == 0) asm volatile("s_waitcnt vmcnt(9)" : "+{v[124:127]}"(of[0]));
    if (ks == 1) asm volatile("s_waitcnt vmcnt(8)" : "+{v[128:131]}"(of[1]));
    if (ks == 2) asm volatile("s_waitcnt vmcnt(7)" : "+{v[132:135]}"(of[2]));
    if (ks == 3) asm volatile("s_waitcnt vmcnt(6)" : "+{v[136:139]}"(of[3]));
    if (ks == 4) asm volatile("s_waitcnt vmcnt(5)" : "+{v[140:143]}"(of[4]));
    if (ks == 5) asm volatile("s_waitcnt vmcnt(4)" : "+{v[144:147]}"(of[5]));
    if (ks == 6) asm volatile("s_waitcnt vmcnt(3)" : "+{v[148:151]}"(of[6]));
    if (ks == 7) asm volatile("s_waitcnt vmcnt(2)" : "+{v[152:155]}"(of[7]));
    if (ks == 8) asm volatile("s_waitcnt vmcnt(1)" : "+{v[156:159]}"(of[8]));
    if (ks == 9) asm volatile("s_waitcnt vmcnt(0)" : "+{v[160:163]}"(of[9]));
  };
  SB();
  u32 fa = sfa, fv = sfv;
  GST(1);
  f4 acc[5];
#pragma unroll
  for (int n = 0; n < 5; ++n) acc[n] = f4{0.f, 0.f, 0.f, 0.f};
  constexpr int NR = (10 - WPRE) * 10;
  u2 Wh[NR];
  auto kn = [](int i, int& ks, int& n) { if (i < 40) { ks = i / 5; n = i % 5; } else { ks = 8 + (i & 1); n = (i - 40) >> 1; } };
  auto rdh = [&](int i) { int ks, n; kn(WPRE * 5 + (i >> 1), ks, n); if (i >= 0 && i < NR) Wh[i] = ld_tr(b0 + ks * 5120 + n * 32 + (i & 1) * 1280); };
  auto put = [&](int j) { asm volatile("ds_write_addtid_b32 %0 offset:%1" :: "v"(acc[j >> 2][j & 3]), "i"((j >> 2) * ST_N + (j & 3) * 256) : "memory"); };
#pragma unroll
  for (int i = 0; i < 50; ++i) {
    int ks, n; kn(i, ks, n);
    if (n == 0) { landed(ks); SB(); }
    acc[n] = mfma(__builtin_bit_cast(bf8, of[ks]), i < WPRE * 5 ? W[ks][n] : mk8(Wh[2 * (i - WPRE * 5)], Wh[2 * (i - WPRE * 5) + 1]), acc[n]);
    const int j = i - RD0;
    rdh(j + j / RDX); if (j >= 0 && j % RDX == RDX - 1) rdh(j + j / RDX + 1);
    if (i == 41) PIN(fa);
    if (i == 42) { PIN(fv); PIN(Wh[NR - 1]); }
    SB();
    if (i >= 43) { put(2 * (i - 43)); put(2 * (i - 43) + 1); SB(); }
    if (!ARR && i % 5 == 4) GST(2 + i / 5);
  }
  put(14); put(15);
  asm volatile("s_nop 3");
#pragma unroll
  for (int j = 16; j < 20; ++j) put(j);
  lds_set(fa, fv);
  GST(12);
  lds_bar();
  GST(13);
}

DEV void reduce_ffn(const Params& p, rsrc_t rws, u32 tag, int lane, int f, u32 key STM_ARG) {
  const u32 tok = key_tok(key), so = WS_ST2 + ((tok & 1) * 1024 + (tok >> 1)) * 16;
  const u32 ya = WS_YP + f * (56 * 128) + lane * 16, yl = lane < 48 ? ya + 6144 : 0x80000000u, el = lane < 48 ? tag : 0u;
  const u2 xr = __builtin_amdgcn_raw_buffer_load_b64(__builtin_amdgcn_make_buffer_rsrc(p.x.p, 0, S_ * D_ * 2, 0x00020000), tok * 1280 + f * 32 + (lane & 3) * 8, 0, 0);
  const float sg = __builtin_amdgcn_rcpf(1.f + __expf(-key_r(key)));
  __builtin_amdgcn_s_setprio(3);
  if (RDLY) __builtin_amdgcn_s_sleep(RDLY);
  u4 w[8];
#pragma nounroll
  for (u32 spin = 0; spin < SPIN_CAP; ++spin) {
    FENCE();
    u32 b = 0;
#pragma unroll
    for (int j = 0; j < 8; ++j) w[j] = bld128_sc1(rws, j < 6 ? ya + j * 1024 : j == 6 ? yl : so);
#pragma unroll
    for (int j = 0; j < 8; ++j) b |= __builtin_amdgcn_bitop3_b32(w[j].y, w[j].w, j != 6 ? tag : el, 0x7e);
    if (!__any(b != 0u)) break;
    __builtin_amdgcn_s_sleep(2);
  }
  STAMP(6);
#define EACH(V, X) _Pragma("unroll") for (int e = 0; e < 4; ++e) { V[e] = X; PIN(V[e]); }
  auto up = [](u32 a, u32 b, int e) { const u32 v = e & 2 ? b : a; return e & 1 ? bfhi(v) : bflo(v); };
  float y[4], z[4];
  EACH(y, up(w[0].x, w[0].z, e))
#pragma unroll
  for (int j = 1; j < 7; ++j) { EACH(y, y[e] + up(w[j].x, w[j].z, e)) }
  EACH(y, y[e] + dppf<0x128>(y[e]))
  auto s16 = [](float v) { auto r = __builtin_amdgcn_permlane16_swap(__float_as_uint(v), __float_as_uint(v), false, false); return __uint_as_float(r[0]) + __uint_as_float(r[1]); };
  auto s32 = [](float v) { auto r = __builtin_amdgcn_permlane32_swap(__float_as_uint(v), __float_as_uint(v), false, false); return __uint_as_float(r[0]) + __uint_as_float(r[1]); };
  EACH(y, s16(y[e])) EACH(y, s32(y[e]))
  EACH(z, up(xr.x, xr.y, e) - __uint_as_float(w[7].x)) EACH(z, z[e] * __uint_as_float(w[7].z)) EACH(y, fmaf(y[e], sg, z[e]))
  EACH(z, y[e] * -1.4426950408889634f) EACH(z, __builtin_amdgcn_exp2f(z[e])) EACH(z, 1.f + z[e]) EACH(z, __builtin_amdgcn_rcpf(z[e])) EACH(y, y[e] * z[e])
  u4 v; v.x = pk2(y[0], y[1]); v.z = pk2(y[2], y[3]); v.y = v.w = tag;
  bst128_sc1(rws, lane < 8 ? WS_GR + f * 128 + lane * 16 : 0x80000000u, v);
  STAMP(2);
}

DEV void epilogue(const Params& p, int lane, int m, int mg, int ng, bool is_main, int f, Epi& ei STM_ARG) {
  const rsrc_t rws = __builtin_amdgcn_make_buffer_rsrc(p.ws.p, 0, WS_END, 0x00020000);
  float (&gf)[20] = ei.gf;
  f2 xf[10], g2[10];
#pragma unroll
  for (int i = is_main ? 0 : YG; i < (is_main ? 0 : YG2); ++i) ei.gate(i);
  SB();
  ei.wait<0>();
  lds_add(Y_WO, is_main ? 8 : 9);
  STAMP(5);
  const u32 tag = __builtin_amdgcn_readfirstlane(ei.tag);
  if (is_main && GW) lds_wait_ge<4>(Y_PUB, GW);
  if (is_main) ei.check(p.ws.p, tag);
  u4 fin = ei.fin;
  if (__builtin_expect(!(fin.y == tag && fin.w == tag), 0)) {
#pragma nounroll
    for (u32 spin = 0; !(fin.y == tag && fin.w == tag) && spin < SPIN_CAP; ++spin) {
      FENCE();
      __builtin_amdgcn_s_sleep(2);
      fin = bld128_sc1(rws, WS_FIN);
    }
  }
#pragma unroll
  for (int i = is_main ? 0 : YG2; i < 20; ++i) ei.gate(i);
  STAMP(10);
  if (!is_main && __builtin_expect(f < 40 && m == 3, 1)) reduce_ffn(p, rws, tag, lane, f, (lane & 4) ? fin.z : fin.x STM);
    if (EPM != 2 && !is_main && __builtin_expect(f == 55 && m == 3, 0)) {
      if (EPM == 3) lds_wait_ge<4>(Y_WO, 768);
#pragma nounroll
      for (u32 spin = 0;; ++spin) {
        FENCE();
        u32 bad = 0;
#pragma unroll
        for (int i = 0; i < 4; ++i) bad |= __builtin_amdgcn_raw_buffer_load_b32(rws, WS_FLAG + 128 + (i * 64 + lane) * 256, 0, 16) ^ tag;
        if (__all(bad == 0u) || spin > SPIN_CAP) break;
        __builtin_amdgcn_s_sleep(16);
      }
      if (EPM != 1 && lane == 0) gst32(p.ws.p + WS_EPOCH, tag);
    }
  const u32 row = Epi::row(lane), odd = ng & 1;
  u32 sadr[2];
  sadr[1] = m * ST_W + (row & 3) * 256 + (row >> 2) * 64;
  sadr[0] = sadr[1] + (odd + ((lane >> 1) & 1)) * ST_N + (lane & 1) * 32;
  sadr[1] += (odd ? 0 : 4 * ST_N) + (lane & 3) * 16;
  PIN(sadr[0]); PIN(sadr[1]);
  SB();
  {
    const u32 tok = ((ei.soff >> 3) & 2046) | ((ei.soff >> 14) & 1), c = key_tok(fin.z) == tok;
    static_assert(WS_ST2 % 32768 == 0, "");
    if (c || key_tok(fin.x) == tok) {
      const u32 a = WS_GR + ng * 640 + c * 64, a0 = a + (odd + ((lane >> 1) & 1)) * 128 + (lane & 1) * 32, a1 = a + (odd ? 0 : 512) + (lane & 3) * 16;
      u4 w[5];
      lds_wait_ge<2>(Y_WO, 256);
#pragma nounroll
      for (u32 spin = 0; spin < SPIN_CAP; ++spin) {
        FENCE();
        u32 b = 0;
#pragma unroll
        for (int j = 0; j < 5; ++j) w[j] = bld128_sc1(rws, j < 4 ? a0 + (j >> 1) * 256 + (j & 1) * 16 : a1);
#pragma unroll
        for (int j = 0; j < 5; ++j) b |= __builtin_amdgcn_bitop3_b32(w[j].y, w[j].w, tag, 0x7e);
        if (!__any(b != 0u)) break;
        __builtin_amdgcn_s_sleep(2);
      }
#pragma unroll
      for (int j = 0; j < 5; ++j) { gf[4 * j] = bflo(w[j].x); gf[4 * j + 1] = bfhi(w[j].x); gf[4 * j + 2] = bflo(w[j].z); gf[4 * j + 3] = bfhi(w[j].z); }
    }
  }
#pragma unroll
  for (int e = 0; e < 10; ++e) { xf[e] = f2{bflo(ei.xw(e)), bfhi(ei.xw(e))}; g2[e] = f2{gf[2 * e], gf[2 * e + 1]}; PIN(xf[e]); PIN(g2[e]); }
  STAMP(7);
  __builtin_amdgcn_s_setprio(LPRIO);
  u4 so = srd(p.out);
  u32 fa = Y_S + m * 4, first;
  SPIN(so); PIN(fa);
  GST(1);
  {
    u32 n = 1u << 20, t, l;
    asm volatile("1:\n\ts_sleep 2\n\tds_read_b32 %1, %4\n\ts_sub_u32 %0, %0, 1\n\ts_cselect_b32 %2, 0, 1\n\ts_waitcnt lgkmcnt(0)\n\t"
                 "v_readfirstlane_b32 %3, %1\n\ts_cmp_lt_u32 %3, %2\n\ts_cbranch_scc1 1b"
                 : "+s"(n), "=&v"(t), "=&s"(l), "=&s"(first) : "v"(fa) : "scc", "memory");
  }
  GST(2);
  f4 pa[5], pb[5];
  auto rd = [&](f4 (&d)[5], u32 a0, u32 a1) {
#pragma unroll
    for (int j = 0; j < 5; ++j) d[j] = ld_f4(j < 4 ? a0 + (j >> 1) * 2 * ST_N + (j & 1) * 16 : a1);
  };
  u32 o0 = (first - 1) * (4 * ST_W), o1 = 4 * ST_W - o0;
  SPIN(o0); SPIN(o1);
  rd(pa, sadr[0] + o0, sadr[1] + o0);
  SB();
  sadr[0] += o1; sadr[1] += o1;
  PIN(sadr[0]); PIN(sadr[1]);
#pragma unroll
  for (int j = 0; j < 5; ++j) PIN(pa[j]);
  GST(3);
  __builtin_amdgcn_s_setprio(3);
  bar();
  GST(4);
  rd(pb, sadr[0], sadr[1]);
  SB();
  auto fin2 = [&](int e) {
    const f4 s4 = pa[e >> 1] + pb[e >> 1];
    const f2 r = __builtin_elementwise_fma(f2{s4[2 * (e & 1)], s4[2 * (e & 1) + 1]}, g2[e], xf[e]);
    return pk2(r.x, r.y);
  };
  u4 o[2]; u2 oh;
#pragma unroll
  for (int ps = 0; ps < 2; ++ps) {
#pragma unroll
    for (int e = 0; e < 4; ++e) o[ps][e] = fin2(4 * ps + e);
    asm volatile("buffer_store_dwordx4 %0, %1, %2, 0 offen offset:%3 " OUTPOL :: "v"(o[ps]), "v"(ei.goff[0]), "s"(so), "i"(ps * 64) : "memory");
    SB();
  }
  oh.x = fin2(8); oh.y = fin2(9);
  asm volatile("buffer_store_dwordx2 %0, %1, %2, 0 offen " OUTPOL :: "v"(oh), "v"(ei.goff[1]), "s"(so), "v"(o[0]), "v"(o[1]) : "memory");
  if (EPM == 1 && !is_main && f == 55 && m == 3 && lane == 0) gst32(p.ws.p + WS_EPOCH, tag);
  GST(5);
  STAMP(8);
}

#define NTW 2
#define NTX 2
#define PUT_STAMPS(m) do {} while (0)
#define ROLE extern "C" __global__ __launch_bounds__(768, 3) void
#define PAGE __attribute__((aligned(4096)))
#define STAMP0 ""
#define STAMP_INIT() do {} while (0)

#define ROWS3(W, X0, X1) \
    "buffer_load_dwordx4 " W ", v3, s[8:11], s15 offen\n\tbuffer_load_dwordx4 " X0 ", v4, s[4:7], s15 offen\n\t" \
    "buffer_load_dwordx4 " X1 ", v5, s[4:7], s15 offen\n\ts_addk_i32 s15, 256\n\ts_cmpk_eq_u32 s15, 1280\n\ts_cselect_b32 s15, 0, s15\n\t"
ROLE PAGE fused_kernel(Params p) {
  asm volatile(
    STAMP0
    "s_load_dword s94, s[0:1], 0\n\t"
    "s_load_dword s95, s[0:1], 64\n\t"
    "v_readfirstlane_b32 s3, v0\n\t"
    "s_cmpk_ge_u32 s2, 200\n\t"
    "s_cbranch_scc1 3f\n\t"
    ".if " STR(EW) "\n\t"
    "s_lshr_b32 s4, s3, 6\n\t"
    "s_bitcmp1_b32 " STR(EWM) ", s4\n\t"
    "s_cbranch_scc0 8f\n\t"
    "v_mul_u32_u24 v4, 0x1e3779, v0\n\tv_mul_u32_u24 v5, 0x2b5a3, v4\n\tv_xor_b32 v6, v4, v5\n\tv_and_b32 v7, 0x3fff3fff, v5\n\t"
    "s_movk_i32 s4, " STR(EW) "\n"
    "6:\n\tv_mfma_f32_16x16x32_bf16 v[8:11], v[4:7], v[4:7], 0\n\ts_nop " STR(EWG) "\n\ts_sub_u32 s4, s4, 1\n\ts_cbranch_scc0 6b\n\ts_nop 7\n"
    "8:\n\t"
    ".endif\n\t"
    "s_cmpk_lt_u32 s3, 256\n\t"
    "s_cbranch_scc1 role_c4\n\t"
    "s_cmpk_lt_u32 s3, 512\n\t"
    "s_cbranch_scc1 role_c3\n\t"
    "s_setprio 3\n\t"
    "s_load_dwordx8 s[4:11], s[0:1], 0\n\t"
    "s_bfe_u32 s12, s3, 0x20006\n\t"
    "s_lshr_b32 s13, s2, 3\n\t"
    "s_mul_i32 s14, s13, 13\n\t"
    "s_lshr_b32 s14, s14, 6\n\t"
    "s_mul_i32 s15, s14, 5\n\t"
    "s_sub_u32 s13, s13, s15\n\t"
    "s_add_u32 s15, s13, s14\n\t"
    "s_sub_u32 s16, s15, 5\n\t"
    "s_min_u32 s15, s15, s16\n\t"
    "s_bitcmp1_b32 s15, 1\n\t"
    "s_cselect_b64 vcc, -1, 0\n\t"
    "s_and_b32 s16, s15, 1\n\t"
    "s_lshl_b32 s17, s2, 1\n\t"
    "s_and_b32 s17, s17, 12\n\t"
    "s_lshr_b32 s17, " STR(KOFF) ", s17\n\t"
    "s_and_b32 s17, s17, 6\n\t"
    "s_add_u32 s16, s16, s17\n\t"
    "s_bitcmp1_b32 s2, 1\n\t"
    "s_cselect_b32 s17, 5, 0\n\t"
    "s_add_u32 s14, s14, s17\n\t"
    "s_bitcmp1_b32 s2, 0\n\t"
    "s_cselect_b32 s17, 5, 0\n\t"
    "s_add_u32 s13, s13, s17\n\t"
    "s_mul_i32 s18, s14, 13\n\t"
    "s_add_u32 s18, s18, s12\n\t"
    "s_sub_u32 s18, s18, 1\n\t"
    "s_mulk_i32 s18, 20480\n\t"
    "s_add_u32 s19, s18, 81920\n\t"
    "s_add_u32 s20, s18, 163840\n\t"
    "s_add_u32 s21, s18, 245760\n\t"
    "s_min_u32 s21, s21, 2600960\n\t"
    "s_max_i32 s18, s18, 0\n\t"
    "s_cmp_lt_u32 s12, 2\n\t"
    "s_cselect_b64 s[22:23], -1, 0\n\t"
    "s_mov_b64 s[24:25], -1\n\t"
    "s_cmp_eq_u32 s12, 0\n\t"
    "s_cmov_b32 s24, 0xffffff00\n\t"
    "s_lshl_b32 s26, s12, 10\n\t"
    "s_add_u32 s26, s26, 116736\n\t"
    "s_mov_b32 s27, 122880\n\t"
    "s_bfe_u32 s17, s2, 0x10002\n\t"
    "s_mul_i32 s17, s17, 10\n\t"
    "s_add_u32 s17, s17, s16\n\t"
    "s_lshl3_add_u32 s28, s16, s12\n\t"
    "s_mulk_i32 s28, 240\n\t"
    "s_add_u32 s28, s28, s13\n\t"
    "s_lshl_b32 s29, s12, 2\n\t"
    "v_bfe_u32 v3, v0, 3, 3\n\t"
    "v_xor_b32 v4, s29, v0\n\t"
    "v_lshrrev_b32 v5, 1, v3\n\t"
    "v_bitop3_b32 v5, v4, v5, 7 bitop3:0x28\n\t"
    "v_and_b32 v6, 2, v3\n\t"
    "v_bitop3_b32 v6, v4, v6, 7 bitop3:0x28\n\t"
    "v_mad_u32_u24 v1, v3, 20, s17\n\t"
    "v_mad_u32_u24 v2, v3, 30, s28\n\t"
    "v_lshl_or_b32 v1, v1, 3, v5\n\t"
    "v_lshl_or_b32 v2, v2, 3, v6\n\t"
    "v_lshlrev_b32 v1, 4, v1\n\t"
    "v_lshlrev_b32 v2, 4, v2\n\t"
    "s_waitcnt lgkmcnt(0)\n\t"
    "s_cbranch_vccnz 5f\n"
    "4:\n\t"
    HALF_A("s26", "v1", "v2", "s[4:7]", "s[8:11]", "s18", "s21", "s[24:25]", "s[22:23]")
    "s_cbranch_vccnz role_ml\n"
    "5:\n\t"
    HALF_B("s26", "v1", "v2", "s[4:7]", "s[8:11]", "s19", "s20", "s27")
    "s_cbranch_vccnz 4b\n\t"
    "s_branch role_ml\n"
    "3:\n\t"
    "s_cmpk_ge_u32 s3, 512\n\t"
    "s_cbranch_scc1 role_al\n\t"
    "s_load_dwordx4 s[4:7], s[0:1], 0\n\t"
    "s_load_dwordx4 s[8:11], s[0:1], 64\n\t"
    "s_load_dwordx2 s[16:17], s[0:1], 32\n\t"
    "s_getpc_b64 s[12:13]\n"
    "2:\n\t"
    "s_lshr_b32 s3, s3, 6\n\t"
    "s_and_b32 s14, s2, -8\n\t"
    "s_add_u32 s14, s14, s3\n\t"
    "s_addk_i32 s14, -200\n\t"
    "s_mulk_i32 s14, 1280\n\t"
    "v_and_b32 v1, 63, v0\n\t"
    "v_min_u32 v2, 9, v1\n\t"
    "v_lshl_add_u32 v2, v2, 7, s14\n\t"
    "v_min_u32 v2, code_end - 2b, v2\n\t"
    "global_load_dword v167, v2, s[12:13]\n\t"
    "s_mul_i32 s3, s3, 7\n\t"
    "s_lshr_b32 s14, s2, 3\n\t"
    "s_add_u32 s3, s3, s14\n\t"
    "s_sub_u32 s3, s3, 25\n\t"
    "s_and_b32 s14, s2, 3\n\t"
    "s_lshl_b32 s14, s14, 5\n\t"
    "s_add_u32 s14, s14, s3\n\t"
    "s_bfe_u32 s15, s2, 0x10002\n\t"
    "s_lshl4_add_u32 s14, s14, s15\n\t"
    "s_mulk_i32 s14, 1280\n\t"
    "s_lshl_b32 s15, s2, 1\n\t"
    "s_and_b32 s15, s15, 12\n\t"
    "s_lshr_b32 s15, " STR(KOFF) ", s15\n\t"
    "s_and_b32 s15, s15, 6\n\t"
    "s_lshl_b32 s15, s15, 7\n\t"
    "s_addk_i32 s15, 256\n\t"
    "v_and_b32 v2, 3, v0\n\t"
    "v_and_b32 v3, 48, v0\n\t"
    "v_lshlrev_b32 v3, 2, v3\n\t"
    "v_lshl_or_b32 v3, v2, 4, v3\n\t"
    "v_bfe_u32 v2, v0, 2, 2\n\t"
    "v_mul_u32_u24 v2, 2560, v2\n\t"
    "v_add3_u32 v4, v2, v3, s14\n\t"
    "v_add_u32 v5, 10240, v4\n\t"
    "s_cmp_ge_u32 s3, 32\n\t"
    "s_waitcnt lgkmcnt(0)\n\t"
    "s_load_dword s94, s[16:17], 0\n\t"
    "s_cbranch_scc1 role_ac\n\t"
    ROWS3("v[100:103]", "v[104:107]", "v[108:111]") ROWS3("v[112:115]", "v[116:119]", "v[120:123]") ROWS3("v[124:127]", "v[128:131]", "v[132:135]")
    ROWS3("v[136:139]", "v[140:143]", "v[144:147]") ROWS3("v[148:151]", "v[152:155]", "v[156:159]")
    "s_branch role_ac"
    :: "s"(__builtin_amdgcn_kernarg_segment_ptr()), "s"(__builtin_amdgcn_workgroup_id_x()), "v"(__builtin_amdgcn_workitem_id_x())
    : "v167", "s99", "vcc", "scc", "memory");
}

struct Ids {
  int lane, wave, xcd, r, mg, ng, ra, rb, rt, j, a7, f, koff;
  DEV Ids() {
    lane = threadIdx.x & 63; wave = __builtin_amdgcn_readfirstlane(threadIdx.x >> 6);
    const int b = blockIdx.x;
    xcd = b & 7; r = b >> 3;
    mg = xcd * 4 + (r >> 3); ng = r & 7;
    ra = (r * 13) >> 6; rb = r - 5 * ra;
    rt = (xcd >> 1) * 5 + ra; j = (xcd & 1) * 5 + rb;
    a7 = r - 25;
    f = a7 < 6 ? 2 * (xcd + 8 * (a7 >> 1)) + (a7 & 1) : 48 + ((FPERM >> (4 * xcd)) & 7);
    koff = (KOFF >> ((xcd >> 1) * 4)) & 6;
  }
};

template <int NS>
DEV void main_wave(Params& p) {
  STAMP_INIT();
  const Ids id;
  const int lane = id.lane, wave = id.wave;
  const rsrc_t rws = __builtin_amdgcn_make_buffer_rsrc(p.ws.p, 0, WS_END, 0x00020000);
  u32 tag;
  G2 g2;
  main_compute<NS>(p, rws, tag, lane, wave, ((id.xcd >> 1) & 1) * 5 + id.ra, id.xcd >> 2, id.rb, id.j, id.mg, id.ng, g2 STM);
  gemm2<true, NS == 4 ? WE4 : WE3>(p, tag, wave, g2 STM);
  PUT_STAMPS(true);
}
ROLE role_ml(Params p) {
  LdIn in; in.take(); SB();
  STAMP_INIT();
  const Ids id;
  const int lane = id.lane, wave = id.wave;
  Epi ei;
  main_loader(p, in, lane, id.xcd >> 2, id.mg, id.ng, id.koff, ei STM);
  epilogue(p, lane, in.lw, id.mg, id.ng, true, 0, ei STM);
  PUT_STAMPS(true);
}
ROLE PAGE role_c3(Params p) { main_wave<3>(p); }
ROLE role_c4(Params p) { main_wave<4>(p); }
ROLE PAGE role_al(Params p) {
  STAMP_INIT();
  const Ids id;
  const int lane = id.lane, wave = id.wave, lw = wave - 8;
  Epi ei;
  aux_loader(p, lane, lw, id.a7, id.f, id.f < 54, id.mg, id.ng, id.koff, ei STM);
  epilogue(p, lane, lw, id.mg, id.ng, false, id.f, ei STM);
  PUT_STAMPS(false);
}
ROLE PAGE __attribute__((amdgpu_num_vgpr(160))) role_ac(Params p) {
  RowIn rin; rin.take(); SB();
  STAMP_INIT();
  const Ids id;
  const int lane = id.lane, wave = id.wave;
  const rsrc_t rws = __builtin_amdgcn_make_buffer_rsrc(p.ws.p, 0, WS_END, 0x00020000);
  u32 tag;
  aux_compute(p, rws, tag, lane, wave, id.f, id.f < 54, id.xcd, id.a7, rin STM);
  NEW_PAGE();
  G2 g2;
  g2.pre(wave, id.mg, id.ng);
  tag = __builtin_amdgcn_readfirstlane(tag);
  SPIN(tag);
  __builtin_amdgcn_s_waitcnt(0x0f70);
  gemm2<false, 0>(p, tag, wave, g2 STM);
  PUT_STAMPS(false);
}
ROLE code_end(Params p) {}

extern "C" void kernel_launch(void* const* d_in, const int* in_sizes, int n_in,
                              void* d_out, int out_size, void* d_ws, size_t ws_size,
                              hipStream_t stream) {
  auto buf = [](void* q, u32 bytes) { Buf r; r.p = (char*)q; r.bytes = bytes; r.flags = 0x00020000u; return r; };
  Params p;
  p.x = buf(d_in[0], S_ * D_ * 2); p.wqkv = buf(d_in[1], D_ * 3 * D_ * 2); p.wo = buf(d_in[2], D_ * D_ * 2); p.wr = buf(d_in[4], D_ * 2);
  p.wg = buf(d_in[5], D_ * F_ * 2); p.wu = buf(d_in[6], D_ * F_ * 2); p.wd = buf(d_in[7], F_ * D_ * 2);
  p.out = buf(d_out, S_ * D_ * 2); p.ws = buf(d_ws, WS_END);
  (void)hipFuncSetAttribute((const void*)fused_kernel, hipFuncAttributeMaxDynamicSharedMemorySize, LDS_BYTES);
  fused_kernel<<<256, 768, LDS_BYTES, stream>>>(p);
}
```
